```python
import torch

def _softmax(x, axis=-1):
    return torch.softmax(x, dim=axis)

def _gelu(x):
    return torch.nn.functional.gelu(x, approximate='tanh')

def _silu(x):
    return torch.nn.functional.silu(x)

def _sigmoid(x):
    return torch.sigmoid(x)

def _layernorm(x, axis=-1, eps=1e-05):
    m = x.mean(dim=axis, keepdim=True)
    v = x.var(dim=axis, keepdim=True, unbiased=False)
    return (x - m) / torch.sqrt(v + eps)

def _scatter_add(tgt, idx, src, axis=0):
    return tgt.clone().index_add_(axis, idx, src)

def _segment_sum(src, seg, n):
    out = torch.zeros((n,) + src.shape[1:], dtype=src.dtype)
    return out.index_add_(0, seg, src)

def _pad(x, pw):
    flat = tuple((v for pair in reversed(pw) for v in pair))
    return torch.nn.functional.pad(x, flat)
kernel_name = 'adaLN_cond__memory_aug_recurrent_mem_tr__dense_activation_silu__prompt'

def setup_inputs():

    _w = lambda a: (lambda t: t.to(torch.bfloat16) if t.is_floating_point() else t)(torch.as_tensor(a))

    def _A_setup():
        B, S, Dm = (1, 2048, 640)
        H, Dh = (32, 128)
        M, seg = (8, 32)
        seg = min(seg, S)
        S = S // seg * seg
        return {'x': _w((lambda *s: torch.randn(*s, dtype=torch.bfloat16) * 0.05)(B, S, Dm)), 'mem0': _w((lambda *s: torch.randn(*s, dtype=torch.bfloat16) * 0.8)(B, M, Dm)), 'Wq': _w((lambda *s: torch.randn(*s, dtype=torch.bfloat16) * 1.6)(Dm, H, Dh) * Dm ** (-0.5)), 'Wk': _w((lambda *s: torch.randn(*s, dtype=torch.bfloat16) * 1.6)(Dm, H, Dh) * Dm ** (-0.5)), 'Wv': _w((lambda *s: torch.randn(*s, dtype=torch.bfloat16) * 0.8)(Dm, H, Dh) * Dm ** (-0.5)), 'Wo': _w((lambda *s: torch.randn(*s, dtype=torch.bfloat16) * 0.8)(H, Dh, Dm) * (H * Dh) ** (-0.5)), 'seg_len': seg}
    _A_in = _A_setup()

    def _B_setup():
        B, S, D, F = (1, 2048, 640, 1728)
        return {'x': _w((lambda *s: torch.randn(*s, dtype=torch.bfloat16) * 0.05)(B, S, D)), 'w1': _w((lambda *s: torch.randn(*s, dtype=torch.bfloat16) * 3.2)(D, F) * D ** (-0.5)), 'w2': _w((lambda *s: torch.randn(*s, dtype=torch.bfloat16) * 0.2)(F, D) * F ** (-0.5))}
    _B_in = _B_setup()
    return {'A_x': _A_in['x'], 'A_mem0': _A_in['mem0'], 'A_Wq': _A_in['Wq'], 'A_Wk': _A_in['Wk'], 'A_Wv': _A_in['Wv'], 'A_Wo': _A_in['Wo'], 'A_seg_len': _A_in['seg_len'], 'B_x': _B_in['x'], 'B_w1': _B_in['w1'], 'B_w2': _B_in['w2'], 'c': _w((lambda *s: torch.randn(*s, dtype=torch.bfloat16) * 0.1)(1, 640) * 8.0), 'w_mod': _w((lambda *s: torch.randn(*s, dtype=torch.bfloat16) * 0.1)(640, 6 * 640) * 640 ** (-0.5) * 8.0)}

def reference(A_x, A_mem0, A_Wq, A_Wk, A_Wv, A_Wo, A_seg_len, B_x, B_w1, B_w2, c, w_mod):

    def _A_ref(x, mem0, Wq, Wk, Wv, Wo, seg_len):
        B, S, Dm = x.shape
        H, Dh = (Wq.shape[1], Wq.shape[2])
        M = mem0.shape[1]
        scale = Dh ** (-0.5)
        n_seg = S // seg_len
        L = M + seg_len
        idx = torch.arange(L)
        mask = (idx[:, None] < M) | (idx[:, None] >= idx[None, :])
        mem = mem0
        outs = []
        for s in range(n_seg):
            xs = x[:, s * seg_len:(s + 1) * seg_len, :].clone()
            h = torch.concatenate([mem, xs], axis=1)
            q = torch.einsum('btd,dhe->bhte', h, Wq)
            k = torch.einsum('btd,dhe->bhte', h, Wk)
            v = torch.einsum('btd,dhe->bhte', h, Wv)
            scores = torch.einsum('bhqe,bhke->bhqk', q, k) * scale
            scores = torch.where(mask[None, None], scores, -1000000000.0)
            p = _softmax(scores, axis=-1)
            o = torch.einsum('bhqk,bhke->bhqe', p, v)
            o = torch.einsum('bhte,hed->btd', o, Wo)
            mem = o[:, :M, :].clone()
            outs.append(o[:, M:, :])
        return torch.concatenate(outs, axis=1)

    def _B_ref(x, w1, w2):
        h = torch.einsum('bsd,df->bsf', x, w1)
        h = _silu(h)
        out = torch.einsum('bsf,fd->bsd', h, w2)
        return out
    x = A_x
    mod = (_silu(c) @ w_mod).reshape(c.shape[0], 6, 1, -1)
    sa, ca, ga, sb, cb, gb = (mod[:, 0].clone(), mod[:, 1].clone(), mod[:, 2].clone(), mod[:, 3].clone(), mod[:, 4].clone(), mod[:, 5].clone())
    h = _layernorm(x, axis=-1) * (1 + ca) + sa
    x = x + ga * _A_ref(h, A_mem0, A_Wq, A_Wk, A_Wv, A_Wo, A_seg_len)
    h = _layernorm(x, axis=-1) * (1 + cb) + sb
    x = x + gb * _B_ref(h, B_w1, B_w2)
    return x
```

```cpp
#include <hip/hip_runtime.h>
#include <hip/hip_bf16.h>
#include <stdint.h>
typedef unsigned short u16;
__device__ __forceinline__ float b2f(u16 v){ return __uint_as_float(((unsigned)v)<<16); }
__device__ __forceinline__ u16 f2b(float f){ __hip_bfloat16 h = __float2bfloat16(f); return *(u16*)&h; }
__device__ __forceinline__ float rb(float f){ return b2f(f2b(f)); }
constexpr int S=2048, D=640, H=32, DH=128, HD=4096, M=8, SEG=32, NSEG=64, F=1728;

__global__ void k_mod(const u16* c, const u16* wmod, float* mod){
  int j = blockIdx.x*blockDim.x+threadIdx.x; if (j>=6*D) return;
  float acc=0; for(int k=0;k<D;k++){ float cv=b2f(c[k]); float s=rb(cv/(1.f+__expf(-cv))); acc+=s*b2f(wmod[k*6*D+j]); }
  mod[j]=rb(acc);
}
__global__ void k_ln(const u16* x, const float* shift, const float* scale, u16* h){
  int r=blockIdx.x, t=threadIdx.x; float v[10]; float s=0;
  for(int i=0;i<10;i++){ v[i]=b2f(x[r*D+t+64*i]); s+=v[i]; }
  for(int o=32;o;o>>=1) s+=__shfl_xor(s,o);
  float mean=s/D; float q=0; for(int i=0;i<10;i++){ float d=v[i]-mean; q+=d*d; }
  for(int o=32;o;o>>=1) q+=__shfl_xor(q,o);
  float m=rb(mean), var=rb(q/D); float sd=rb(sqrtf(rb(var+1e-5f)));
  for(int i=0;i<10;i++){ int d=t+64*i; float y=rb(rb(v[i]-m)/sd); y=rb(y*rb(1.f+scale[d])); y=rb(y+shift[d]); h[r*D+d]=f2b(y); }
}
__global__ void k_gemm(const u16* A, int lda, const u16* B, int ldb, u16* C, int ldc, int Mr, int N, int K, int mode, const u16* res, const float* g){
  __shared__ float As[16][17], Bs[16][17];
  int tx=threadIdx.x, ty=threadIdx.y; int row=blockIdx.y*16+ty, col=blockIdx.x*16+tx; float acc=0;
  for(int k0=0;k0<K;k0+=16){
    As[ty][tx] = row<Mr ? b2f(A[(size_t)row*lda+k0+tx]) : 0.f;
    Bs[ty][tx] = b2f(B[(size_t)(k0+ty)*ldb+col]);
    __syncthreads();
    for(int k=0;k<16;k++) acc+=As[ty][k]*Bs[k][tx];
    __syncthreads();
  }
  if(row<Mr){ float y=rb(acc);
    if(mode==1) y=rb(y/(1.f+__expf(-y)));
    if(mode==2) y=rb(b2f(res[(size_t)row*ldc+col])+rb(g[col]*y));
    C[(size_t)row*ldc+col]=f2b(y);}
}
__global__ void k_attn(const u16* qm,const u16* km,const u16* vm,const u16* Q,const u16* Kx,const u16* V,u16* om,u16* O,int seg0,int r0,int r1,int memstride){
  int h=blockIdx.x, seg=seg0+blockIdx.y; int t=threadIdx.x;
  qm+= (size_t)blockIdx.y*memstride; km+=(size_t)blockIdx.y*memstride; vm+=(size_t)blockIdx.y*memstride;
  __shared__ float p[40];
  for(int r=r0;r<r1;r++){
    const u16* q = r<M ? qm+r*HD+h*DH : Q+(size_t)(seg*SEG+r-M)*HD+h*DH;
    if(t<40){ const u16* k = t<M ? km+t*HD+h*DH : Kx+(size_t)(seg*SEG+t-M)*HD+h*DH; float a=0; for(int e=0;e<DH;e++) a+=b2f(q[e])*b2f(k[e]);
      a=rb(rb(a)*0.08838834764831845f); bool ok = (r<M)||(r>=t); p[t]= ok? a : rb(-1e9f);}
    __syncthreads();
    float mx=-INFINITY; for(int j=0;j<40;j++) mx=fmaxf(mx,p[j]); float sum=0; for(int j=0;j<40;j++) sum+=__expf(p[j]-mx);
    float o=0; for(int j=0;j<40;j++){ float pj=rb(__expf(p[j]-mx)/sum); const u16* v = j<M ? vm+j*HD+h*DH : V+(size_t)(seg*SEG+j-M)*HD+h*DH; o+=pj*b2f(v[t]); }
    if(r<M) om[r*HD+h*DH+t]=f2b(o); else O[(size_t)(seg*SEG+r-M)*HD+h*DH+t]=f2b(o);
    __syncthreads();
  }
}
extern "C" void kernel_launch(void* const* d_in, const int* in_sizes, int n_in, void* d_out, int out_size, void* d_ws, size_t ws_size, hipStream_t stream){
  const u16* x=(const u16*)d_in[0]; const u16* mem0=(const u16*)d_in[1]; const u16* Wq=(const u16*)d_in[2]; const u16* Wk=(const u16*)d_in[3]; const u16* Wv=(const u16*)d_in[4];
  const u16* Wo=(const u16*)d_in[5]; const u16* w1=(const u16*)d_in[8]; const u16* w2=(const u16*)d_in[9]; const u16* c=(const u16*)d_in[10]; const u16* wmod=(const u16*)d_in[11];
  u16* out=(u16*)d_out; char* ws=(char*)d_ws; size_t off=0;
  auto alloc=[&](size_t b){ char* p=ws+off; off+=(b+255)/256*256; return p; };
  float* mod=(float*)alloc(6*D*4); u16* h=(u16*)alloc(S*D*2); u16* Q=(u16*)alloc((size_t)S*HD*2); u16* K=(u16*)alloc((size_t)S*HD*2); u16* V=(u16*)alloc((size_t)S*HD*2);
  u16* O=(u16*)alloc((size_t)S*HD*2); u16* qm=(u16*)alloc((size_t)NSEG*M*HD*2); u16* km=(u16*)alloc((size_t)NSEG*M*HD*2); u16* vm=(u16*)alloc((size_t)NSEG*M*HD*2);
  u16* om=(u16*)alloc(M*HD*2); u16* mem=(u16*)alloc(M*D*2); u16* x1=(u16*)alloc(S*D*2); u16* h2=(u16*)alloc(S*D*2); u16* ff=(u16*)alloc((size_t)S*F*2);
  k_mod<<<15,256,0,stream>>>(c,wmod,mod);
  k_ln<<<S,64,0,stream>>>(x,mod+0*D,mod+1*D,h);
  dim3 b(16,16);
  k_gemm<<<dim3(HD/16,S/16),b,0,stream>>>(h,D,Wq,HD,Q,HD,S,HD,D,0,nullptr,nullptr);
  k_gemm<<<dim3(HD/16,S/16),b,0,stream>>>(h,D,Wk,HD,K,HD,S,HD,D,0,nullptr,nullptr);
  k_gemm<<<dim3(HD/16,S/16),b,0,stream>>>(h,D,Wv,HD,V,HD,S,HD,D,0,nullptr,nullptr);
  for(int s=0;s<NSEG;s++){
    const u16* m = s==0? mem0 : mem; size_t o=(size_t)s*M*HD;
    k_gemm<<<dim3(HD/16,1),b,0,stream>>>(m,D,Wq,HD,qm+o,HD,M,HD,D,0,nullptr,nullptr);
    k_gemm<<<dim3(HD/16,1),b,0,stream>>>(m,D,Wk,HD,km+o,HD,M,HD,D,0,nullptr,nullptr);
    k_gemm<<<dim3(HD/16,1),b,0,stream>>>(m,D,Wv,HD,vm+o,HD,M,HD,D,0,nullptr,nullptr);
    k_attn<<<dim3(H,1),128,0,stream>>>(qm+o,km+o,vm+o,Q,K,V,om,O,s,0,M,0);
    k_gemm<<<dim3(D/16,1),b,0,stream>>>(om,HD,Wo,D,mem,D,M,D,HD,0,nullptr,nullptr);
  }
  k_attn<<<dim3(H,NSEG),128,0,stream>>>(qm,km,vm,Q,K,V,om,O,0,M,40,M*HD);
  k_gemm<<<dim3(D/16,S/16),b,0,stream>>>(O,HD,Wo,D,x1,D,S,D,HD,2,x,mod+2*D);
  k_ln<<<S,64,0,stream>>>(x1,mod+3*D,mod+4*D,h2);
  k_gemm<<<dim3(F/16,S/16),b,0,stream>>>(h2,D,w1,F,ff,F,S,F,D,1,nullptr,nullptr);
  k_gemm<<<dim3(D/16,S/16),b,0,stream>>>(ff,F,w2,D,out,D,S,D,F,2,x1,mod+5*D);
}
```

```cpp
#include <hip/hip_runtime.h>
#include <hip/hip_bf16.h>
#include <stdint.h>
#define REC_QK_ASM \
  "s_lshl_b32 s86, s64, 12\n" \
  "s_add_u32 s86, s86, 32768\n" \
  "s_add_u32 m0, s86, 0\n" \
  "s_mov_b32 s85, 2621440\n" \
  "buffer_load_dwordx4 v16, s[56:59], s85 offen nt lds\n" \
  "s_add_u32 m0, s86, 1024\n" \
  "s_mov_b32 s85, 2654208\n" \
  "buffer_load_dwordx4 v16, s[56:59], s85 offen nt lds\n" \
  "s_add_u32 m0, s86, 2048\n" \
  "s_mov_b32 s85, 2686976\n" \
  "buffer_load_dwordx4 v17, s[56:59], s85 offen nt lds\n" \
  "s_add_u32 m0, s86, 3072\n" \
  "s_mov_b32 s85, 2719744\n" \
  "buffer_load_dwordx4 v17, s[56:59], s85 offen nt lds\n" \
  "s_add_u32 m0, s86, 16384\n" \
  "s_mov_b32 s85, 2621440\n" \
  "buffer_load_dwordx4 v16, s[60:63], s85 offen nt lds\n" \
  "s_add_u32 m0, s86, 17408\n" \
  "s_mov_b32 s85, 2654208\n" \
  "buffer_load_dwordx4 v16, s[60:63], s85 offen nt lds\n" \
  "s_add_u32 m0, s86, 18432\n" \
  "s_mov_b32 s85, 2686976\n" \
  "buffer_load_dwordx4 v17, s[60:63], s85 offen nt lds\n" \
  "s_add_u32 m0, s86, 19456\n" \
  "s_mov_b32 s85, 2719744\n" \
  "buffer_load_dwordx4 v17, s[60:63], s85 offen nt lds\n" \
  "s_add_u32 m0, s86, 32768\n" \
  "s_mov_b32 s85, 3145728\n" \
  "buffer_load_dwordx4 v16, s[56:59], s85 offen nt lds\n" \
  "s_add_u32 m0, s86, 33792\n" \
  "s_mov_b32 s85, 3178496\n" \
  "buffer_load_dwordx4 v16, s[56:59], s85 offen nt lds\n" \
  "s_add_u32 m0, s86, 34816\n" \
  "s_mov_b32 s85, 3211264\n" \
  "buffer_load_dwordx4 v17, s[56:59], s85 offen nt lds\n" \
  "s_add_u32 m0, s86, 35840\n" \
  "s_mov_b32 s85, 3244032\n" \
  "buffer_load_dwordx4 v17, s[56:59], s85 offen nt lds\n" \
  "s_add_u32 m0, s86, 49152\n" \
  "s_mov_b32 s85, 3145728\n" \
  "buffer_load_dwordx4 v16, s[60:63], s85 offen nt lds\n" \
  "s_add_u32 m0, s86, 50176\n" \
  "s_mov_b32 s85, 3178496\n" \
  "buffer_load_dwordx4 v16, s[60:63], s85 offen nt lds\n" \
  "s_add_u32 m0, s86, 51200\n" \
  "s_mov_b32 s85, 3211264\n" \
  "buffer_load_dwordx4 v17, s[60:63], s85 offen nt lds\n" \
  "s_add_u32 m0, s86, 52224\n" \
  "s_mov_b32 s85, 3244032\n" \
  "buffer_load_dwordx4 v17, s[60:63], s85 offen nt lds\n" \
  "s_add_u32 m0, s86, 65536\n" \
  "s_mov_b32 s85, 3670016\n" \
  "buffer_load_dwordx4 v16, s[56:59], s85 offen nt lds\n" \
  "s_add_u32 m0, s86, 66560\n" \
  "s_mov_b32 s85, 3702784\n" \
  "buffer_load_dwordx4 v16, s[56:59], s85 offen nt lds\n" \
  "s_add_u32 m0, s86, 67584\n" \
  "s_mov_b32 s85, 3735552\n" \
  "buffer_load_dwordx4 v17, s[56:59], s85 offen nt lds\n" \
  "s_add_u32 m0, s86, 68608\n" \
  "s_mov_b32 s85, 3768320\n" \
  "buffer_load_dwordx4 v17, s[56:59], s85 offen nt lds\n" \
  "s_add_u32 m0, s86, 81920\n" \
  "s_mov_b32 s85, 3670016\n" \
  "buffer_load_dwordx4 v16, s[60:63], s85 offen nt lds\n" \
  "s_add_u32 m0, s86, 82944\n" \
  "s_mov_b32 s85, 3702784\n" \
  "buffer_load_dwordx4 v16, s[60:63], s85 offen nt lds\n" \
  "s_add_u32 m0, s86, 83968\n" \
  "s_mov_b32 s85, 3735552\n" \
  "buffer_load_dwordx4 v17, s[60:63], s85 offen nt lds\n" \
  "s_add_u32 m0, s86, 84992\n" \
  "s_mov_b32 s85, 3768320\n" \
  "buffer_load_dwordx4 v17, s[60:63], s85 offen nt lds\n" \
  "s_waitcnt vmcnt(16) lgkmcnt(0)\n" \
  "s_barrier\n" \
  "s_add_u32 m0, s86, 98304\n" \
  "s_mov_b32 s85, 4194304\n" \
  "buffer_load_dwordx4 v16, s[56:59], s85 offen nt lds\n" \
  "s_add_u32 m0, s86, 99328\n" \
  "s_mov_b32 s85, 4227072\n" \
  "buffer_load_dwordx4 v16, s[56:59], s85 offen nt lds\n" \
  "s_add_u32 m0, s86, 100352\n" \
  "s_mov_b32 s85, 4259840\n" \
  "buffer_load_dwordx4 v17, s[56:59], s85 offen nt lds\n" \
  "s_add_u32 m0, s86, 101376\n" \
  "s_mov_b32 s85, 4292608\n" \
  "buffer_load_dwordx4 v17, s[56:59], s85 offen nt lds\n" \
  "s_add_u32 m0, s86, 114688\n" \
  "s_mov_b32 s85, 4194304\n" \
  "buffer_load_dwordx4 v16, s[60:63], s85 offen nt lds\n" \
  "s_add_u32 m0, s86, 115712\n" \
  "s_mov_b32 s85, 4227072\n" \
  "buffer_load_dwordx4 v16, s[60:63], s85 offen nt lds\n" \
  "s_add_u32 m0, s86, 116736\n" \
  "s_mov_b32 s85, 4259840\n" \
  "buffer_load_dwordx4 v17, s[60:63], s85 offen nt lds\n" \
  "s_add_u32 m0, s86, 117760\n" \
  "s_mov_b32 s85, 4292608\n" \
  "buffer_load_dwordx4 v17, s[60:63], s85 offen nt lds\n" \
  "ds_read_b64_tr_b16 a[40:41], v18 offset:0\n" \
  "ds_read_b64_tr_b16 a[42:43], v18 offset:1024\n" \
  "ds_read_b64_tr_b16 a[44:45], v18 offset:8192\n" \
  "ds_read_b64_tr_b16 a[46:47], v18 offset:9216\n" \
  "ds_read_b64_tr_b16 a[120:121], v19 offset:0\n" \
  "ds_read_b64_tr_b16 a[122:123], v19 offset:1024\n" \
  "ds_read_b64_tr_b16 a[124:125], v19 offset:8192\n" \
  "ds_read_b64_tr_b16 a[126:127], v19 offset:9216\n" \
  "ds_read_b64_tr_b16 a[200:201], v18 offset:16384\n" \
  "ds_read_b64_tr_b16 a[202:203], v18 offset:17408\n" \
  "ds_read_b64_tr_b16 a[204:205], v18 offset:24576\n" \
  "ds_read_b64_tr_b16 a[206:207], v18 offset:25600\n" \
  "ds_read_b64_tr_b16 v[216:217], v19 offset:16384\n" \
  "ds_read_b64_tr_b16 v[218:219], v19 offset:17408\n" \
  "ds_read_b64_tr_b16 v[220:221], v19 offset:24576\n" \
  "ds_read_b64_tr_b16 v[222:223], v19 offset:25600\n" \
  "s_waitcnt vmcnt(16) lgkmcnt(0)\n" \
  "s_barrier\n" \
  "s_add_u32 m0, s86, 0\n" \
  "s_mov_b32 s85, 4718592\n" \
  "buffer_load_dwordx4 v16, s[56:59], s85 offen nt lds\n" \
  "s_add_u32 m0, s86, 1024\n" \
  "s_mov_b32 s85, 4751360\n" \
  "buffer_load_dwordx4 v16, s[56:59], s85 offen nt lds\n" \
  "s_add_u32 m0, s86, 2048\n" \
  "s_mov_b32 s85, 4784128\n" \
  "buffer_load_dwordx4 v17, s[56:59], s85 offen nt lds\n" \
  "s_add_u32 m0, s86, 3072\n" \
  "s_mov_b32 s85, 4816896\n" \
  "buffer_load_dwordx4 v17, s[56:59], s85 offen nt lds\n" \
  "s_add_u32 m0, s86, 16384\n" \
  "s_mov_b32 s85, 4718592\n" \
  "buffer_load_dwordx4 v16, s[60:63], s85 offen nt lds\n" \
  "s_add_u32 m0, s86, 17408\n" \
  "s_mov_b32 s85, 4751360\n" \
  "buffer_load_dwordx4 v16, s[60:63], s85 offen nt lds\n" \
  "s_add_u32 m0, s86, 18432\n" \
  "s_mov_b32 s85, 4784128\n" \
  "buffer_load_dwordx4 v17, s[60:63], s85 offen nt lds\n" \
  "s_add_u32 m0, s86, 19456\n" \
  "s_mov_b32 s85, 4816896\n" \
  "buffer_load_dwordx4 v17, s[60:63], s85 offen nt lds\n" \
  "ds_read_b64_tr_b16 a[48:49], v18 offset:32768\n" \
  "ds_read_b64_tr_b16 a[50:51], v18 offset:33792\n" \
  "ds_read_b64_tr_b16 a[52:53], v18 offset:40960\n" \
  "ds_read_b64_tr_b16 a[54:55], v18 offset:41984\n" \
  "ds_read_b64_tr_b16 a[128:129], v19 offset:32768\n" \
  "ds_read_b64_tr_b16 a[130:131], v19 offset:33792\n" \
  "ds_read_b64_tr_b16 a[132:133], v19 offset:40960\n" \
  "ds_read_b64_tr_b16 a[134:135], v19 offset:41984\n" \
  "ds_read_b64_tr_b16 a[208:209], v18 offset:49152\n" \
  "ds_read_b64_tr_b16 a[210:211], v18 offset:50176\n" \
  "ds_read_b64_tr_b16 a[212:213], v18 offset:57344\n" \
  "ds_read_b64_tr_b16 a[214:215], v18 offset:58368\n" \
  "ds_read_b64_tr_b16 v[224:225], v19 offset:49152\n" \
  "ds_read_b64_tr_b16 v[226:227], v19 offset:50176\n" \
  "ds_read_b64_tr_b16 v[228:229], v19 offset:57344\n" \
  "ds_read_b64_tr_b16 v[230:231], v19 offset:58368\n" \
  "s_waitcnt vmcnt(16) lgkmcnt(0)\n" \
  "s_barrier\n" \
  "s_add_u32 m0, s86, 32768\n" \
  "s_mov_b32 s85, 0\n" \
  "buffer_load_dwordx4 v16, s[56:59], s85 offen nt lds\n" \
  "s_add_u32 m0, s86, 33792\n" \
  "s_mov_b32 s85, 32768\n" \
  "buffer_load_dwordx4 v16, s[56:59], s85 offen nt lds\n" \
  "s_add_u32 m0, s86, 34816\n" \
  "s_mov_b32 s85, 65536\n" \
  "buffer_load_dwordx4 v17, s[56:59], s85 offen nt lds\n" \
  "s_add_u32 m0, s86, 35840\n" \
  "s_mov_b32 s85, 98304\n" \
  "buffer_load_dwordx4 v17, s[56:59], s85 offen nt lds\n" \
  "s_add_u32 m0, s86, 49152\n" \
  "s_mov_b32 s85, 0\n" \
  "buffer_load_dwordx4 v16, s[60:63], s85 offen nt lds\n" \
  "s_add_u32 m0, s86, 50176\n" \
  "s_mov_b32 s85, 32768\n" \
  "buffer_load_dwordx4 v16, s[60:63], s85 offen nt lds\n" \
  "s_add_u32 m0, s86, 51200\n" \
  "s_mov_b32 s85, 65536\n" \
  "buffer_load_dwordx4 v17, s[60:63], s85 offen nt lds\n" \
  "s_add_u32 m0, s86, 52224\n" \
  "s_mov_b32 s85, 98304\n" \
  "buffer_load_dwordx4 v17, s[60:63], s85 offen nt lds\n" \
  "v_xor_b32 v18, 0x10000, v18\n" \
  "v_xor_b32 v19, 0x10000, v19\n" \
  "ds_read_b64_tr_b16 a[56:57], v18 offset:0\n" \
  "ds_read_b64_tr_b16 a[58:59], v18 offset:1024\n" \
  "ds_read_b64_tr_b16 a[60:61], v18 offset:8192\n" \
  "ds_read_b64_tr_b16 a[62:63], v18 offset:9216\n" \
  "ds_read_b64_tr_b16 a[136:137], v19 offset:0\n" \
  "ds_read_b64_tr_b16 a[138:139], v19 offset:1024\n" \
  "ds_read_b64_tr_b16 a[140:141], v19 offset:8192\n" \
  "ds_read_b64_tr_b16 a[142:143], v19 offset:9216\n" \
  "ds_read_b64_tr_b16 a[216:217], v18 offset:16384\n" \
  "ds_read_b64_tr_b16 a[218:219], v18 offset:17408\n" \
  "ds_read_b64_tr_b16 a[220:221], v18 offset:24576\n" \
  "ds_read_b64_tr_b16 a[222:223], v18 offset:25600\n" \
  "ds_read_b64_tr_b16 v[232:233], v19 offset:16384\n" \
  "ds_read_b64_tr_b16 v[234:235], v19 offset:17408\n" \
  "ds_read_b64_tr_b16 v[236:237], v19 offset:24576\n" \
  "ds_read_b64_tr_b16 v[238:239], v19 offset:25600\n" \
  "s_waitcnt vmcnt(16) lgkmcnt(0)\n" \
  "s_barrier\n" \
  "s_add_u32 m0, s86, 65536\n" \
  "s_mov_b32 s85, 524288\n" \
  "buffer_load_dwordx4 v16, s[56:59], s85 offen nt lds\n" \
  "s_add_u32 m0, s86, 66560\n" \
  "s_mov_b32 s85, 557056\n" \
  "buffer_load_dwordx4 v16, s[56:59], s85 offen nt lds\n" \
  "s_add_u32 m0, s86, 67584\n" \
  "s_mov_b32 s85, 589824\n" \
  "buffer_load_dwordx4 v17, s[56:59], s85 offen nt lds\n" \
  "s_add_u32 m0, s86, 68608\n" \
  "s_mov_b32 s85, 622592\n" \
  "buffer_load_dwordx4 v17, s[56:59], s85 offen nt lds\n" \
  "s_add_u32 m0, s86, 81920\n" \
  "s_mov_b32 s85, 524288\n" \
  "buffer_load_dwordx4 v16, s[60:63], s85 offen nt lds\n" \
  "s_add_u32 m0, s86, 82944\n" \
  "s_mov_b32 s85, 557056\n" \
  "buffer_load_dwordx4 v16, s[60:63], s85 offen nt lds\n" \
  "s_add_u32 m0, s86, 83968\n" \
  "s_mov_b32 s85, 589824\n" \
  "buffer_load_dwordx4 v17, s[60:63], s85 offen nt lds\n" \
  "s_add_u32 m0, s86, 84992\n" \
  "s_mov_b32 s85, 622592\n" \
  "buffer_load_dwordx4 v17, s[60:63], s85 offen nt lds\n" \
  "ds_read_b64_tr_b16 a[64:65], v18 offset:32768\n" \
  "ds_read_b64_tr_b16 a[66:67], v18 offset:33792\n" \
  "ds_read_b64_tr_b16 a[68:69], v18 offset:40960\n" \
  "ds_read_b64_tr_b16 a[70:71], v18 offset:41984\n" \
  "ds_read_b64_tr_b16 a[144:145], v19 offset:32768\n" \
  "ds_read_b64_tr_b16 a[146:147], v19 offset:33792\n" \
  "ds_read_b64_tr_b16 a[148:149], v19 offset:40960\n" \
  "ds_read_b64_tr_b16 a[150:151], v19 offset:41984\n" \
  "ds_read_b64_tr_b16 a[224:225], v18 offset:49152\n" \
  "ds_read_b64_tr_b16 a[226:227], v18 offset:50176\n" \
  "ds_read_b64_tr_b16 a[228:229], v18 offset:57344\n" \
  "ds_read_b64_tr_b16 a[230:231], v18 offset:58368\n" \
  "ds_read_b64_tr_b16 v[240:241], v19 offset:49152\n" \
  "ds_read_b64_tr_b16 v[242:243], v19 offset:50176\n" \
  "ds_read_b64_tr_b16 v[244:245], v19 offset:57344\n" \
  "ds_read_b64_tr_b16 v[246:247], v19 offset:58368\n" \
  "s_waitcnt vmcnt(16) lgkmcnt(0)\n" \
  "s_barrier\n" \
  "s_add_u32 m0, s86, 98304\n" \
  "s_mov_b32 s85, 1048576\n" \
  "buffer_load_dwordx4 v16, s[56:59], s85 offen nt lds\n" \
  "s_add_u32 m0, s86, 99328\n" \
  "s_mov_b32 s85, 1081344\n" \
  "buffer_load_dwordx4 v16, s[56:59], s85 offen nt lds\n" \
  "s_add_u32 m0, s86, 100352\n" \
  "s_mov_b32 s85, 1114112\n" \
  "buffer_load_dwordx4 v17, s[56:59], s85 offen nt lds\n" \
  "s_add_u32 m0, s86, 101376\n" \
  "s_mov_b32 s85, 1146880\n" \
  "buffer_load_dwordx4 v17, s[56:59], s85 offen nt lds\n" \
  "s_add_u32 m0, s86, 114688\n" \
  "s_mov_b32 s85, 1048576\n" \
  "buffer_load_dwordx4 v16, s[60:63], s85 offen nt lds\n" \
  "s_add_u32 m0, s86, 115712\n" \
  "s_mov_b32 s85, 1081344\n" \
  "buffer_load_dwordx4 v16, s[60:63], s85 offen nt lds\n" \
  "s_add_u32 m0, s86, 116736\n" \
  "s_mov_b32 s85, 1114112\n" \
  "buffer_load_dwordx4 v17, s[60:63], s85 offen nt lds\n" \
  "s_add_u32 m0, s86, 117760\n" \
  "s_mov_b32 s85, 1146880\n" \
  "buffer_load_dwordx4 v17, s[60:63], s85 offen nt lds\n" \
  "v_xor_b32 v18, 0x10000, v18\n" \
  "v_xor_b32 v19, 0x10000, v19\n" \
  "ds_read_b64_tr_b16 a[72:73], v18 offset:0\n" \
  "ds_read_b64_tr_b16 a[74:75], v18 offset:1024\n" \
  "ds_read_b64_tr_b16 a[76:77], v18 offset:8192\n" \
  "ds_read_b64_tr_b16 a[78:79], v18 offset:9216\n" \
  "ds_read_b64_tr_b16 a[152:153], v19 offset:0\n" \
  "ds_read_b64_tr_b16 a[154:155], v19 offset:1024\n" \
  "ds_read_b64_tr_b16 a[156:157], v19 offset:8192\n" \
  "ds_read_b64_tr_b16 a[158:159], v19 offset:9216\n" \
  "ds_read_b64_tr_b16 a[232:233], v18 offset:16384\n" \
  "ds_read_b64_tr_b16 a[234:235], v18 offset:17408\n" \
  "ds_read_b64_tr_b16 a[236:237], v18 offset:24576\n" \
  "ds_read_b64_tr_b16 a[238:239], v18 offset:25600\n" \
  "ds_read_b64_tr_b16 v[248:249], v19 offset:16384\n" \
  "ds_read_b64_tr_b16 v[250:251], v19 offset:17408\n" \
  "ds_read_b64_tr_b16 v[252:253], v19 offset:24576\n" \
  "ds_read_b64_tr_b16 v[254:255], v19 offset:25600\n" \
  "s_waitcnt vmcnt(16) lgkmcnt(0)\n" \
  "s_barrier\n" \
  "s_add_u32 m0, s86, 0\n" \
  "s_mov_b32 s85, 1572864\n" \
  "buffer_load_dwordx4 v16, s[56:59], s85 offen nt lds\n" \
  "s_add_u32 m0, s86, 1024\n" \
  "s_mov_b32 s85, 1605632\n" \
  "buffer_load_dwordx4 v16, s[56:59], s85 offen nt lds\n" \
  "s_add_u32 m0, s86, 2048\n" \
  "s_mov_b32 s85, 1638400\n" \
  "buffer_load_dwordx4 v17, s[56:59], s85 offen nt lds\n" \
  "s_add_u32 m0, s86, 3072\n" \
  "s_mov_b32 s85, 1671168\n" \
  "buffer_load_dwordx4 v17, s[56:59], s85 offen nt lds\n" \
  "s_add_u32 m0, s86, 16384\n" \
  "s_mov_b32 s85, 1572864\n" \
  "buffer_load_dwordx4 v16, s[60:63], s85 offen nt lds\n" \
  "s_add_u32 m0, s86, 17408\n" \
  "s_mov_b32 s85, 1605632\n" \
  "buffer_load_dwordx4 v16, s[60:63], s85 offen nt lds\n" \
  "s_add_u32 m0, s86, 18432\n" \
  "s_mov_b32 s85, 1638400\n" \
  "buffer_load_dwordx4 v17, s[60:63], s85 offen nt lds\n" \
  "s_add_u32 m0, s86, 19456\n" \
  "s_mov_b32 s85, 1671168\n" \
  "buffer_load_dwordx4 v17, s[60:63], s85 offen nt lds\n" \
  "ds_read_b64_tr_b16 a[0:1], v18 offset:32768\n" \
  "ds_read_b64_tr_b16 a[2:3], v18 offset:33792\n" \
  "ds_read_b64_tr_b16 a[4:5], v18 offset:40960\n" \
  "ds_read_b64_tr_b16 a[6:7], v18 offset:41984\n" \
  "ds_read_b64_tr_b16 a[80:81], v19 offset:32768\n" \
  "ds_read_b64_tr_b16 a[82:83], v19 offset:33792\n" \
  "ds_read_b64_tr_b16 a[84:85], v19 offset:40960\n" \
  "ds_read_b64_tr_b16 a[86:87], v19 offset:41984\n" \
  "ds_read_b64_tr_b16 a[160:161], v18 offset:49152\n" \
  "ds_read_b64_tr_b16 a[162:163], v18 offset:50176\n" \
  "ds_read_b64_tr_b16 a[164:165], v18 offset:57344\n" \
  "ds_read_b64_tr_b16 a[166:167], v18 offset:58368\n" \
  "ds_read_b64_tr_b16 a[240:241], v19 offset:49152\n" \
  "ds_read_b64_tr_b16 a[242:243], v19 offset:50176\n" \
  "ds_read_b64_tr_b16 a[244:245], v19 offset:57344\n" \
  "ds_read_b64_tr_b16 a[246:247], v19 offset:58368\n" \
  "s_waitcnt vmcnt(16) lgkmcnt(0)\n" \
  "s_barrier\n" \
  "s_add_u32 m0, s86, 32768\n" \
  "s_mov_b32 s85, 2097152\n" \
  "buffer_load_dwordx4 v16, s[56:59], s85 offen nt lds\n" \
  "s_add_u32 m0, s86, 33792\n" \
  "s_mov_b32 s85, 2129920\n" \
  "buffer_load_dwordx4 v16, s[56:59], s85 offen nt lds\n" \
  "s_add_u32 m0, s86, 34816\n" \
  "s_mov_b32 s85, 2162688\n" \
  "buffer_load_dwordx4 v17, s[56:59], s85 offen nt lds\n" \
  "s_add_u32 m0, s86, 35840\n" \
  "s_mov_b32 s85, 2195456\n" \
  "buffer_load_dwordx4 v17, s[56:59], s85 offen nt lds\n" \
  "s_add_u32 m0, s86, 49152\n" \
  "s_mov_b32 s85, 2097152\n" \
  "buffer_load_dwordx4 v16, s[60:63], s85 offen nt lds\n" \
  "s_add_u32 m0, s86, 50176\n" \
  "s_mov_b32 s85, 2129920\n" \
  "buffer_load_dwordx4 v16, s[60:63], s85 offen nt lds\n" \
  "s_add_u32 m0, s86, 51200\n" \
  "s_mov_b32 s85, 2162688\n" \
  "buffer_load_dwordx4 v17, s[60:63], s85 offen nt lds\n" \
  "s_add_u32 m0, s86, 52224\n" \
  "s_mov_b32 s85, 2195456\n" \
  "buffer_load_dwordx4 v17, s[60:63], s85 offen nt lds\n" \
  "v_xor_b32 v18, 0x10000, v18\n" \
  "v_xor_b32 v19, 0x10000, v19\n" \
  "ds_read_b64_tr_b16 a[8:9], v18 offset:0\n" \
  "ds_read_b64_tr_b16 a[10:11], v18 offset:1024\n" \
  "ds_read_b64_tr_b16 a[12:13], v18 offset:8192\n" \
  "ds_read_b64_tr_b16 a[14:15], v18 offset:9216\n" \
  "ds_read_b64_tr_b16 a[88:89], v19 offset:0\n" \
  "ds_read_b64_tr_b16 a[90:91], v19 offset:1024\n" \
  "ds_read_b64_tr_b16 a[92:93], v19 offset:8192\n" \
  "ds_read_b64_tr_b16 a[94:95], v19 offset:9216\n" \
  "ds_read_b64_tr_b16 a[168:169], v18 offset:16384\n" \
  "ds_read_b64_tr_b16 a[170:171], v18 offset:17408\n" \
  "ds_read_b64_tr_b16 a[172:173], v18 offset:24576\n" \
  "ds_read_b64_tr_b16 a[174:175], v18 offset:25600\n" \
  "ds_read_b64_tr_b16 a[248:249], v19 offset:16384\n" \
  "ds_read_b64_tr_b16 a[250:251], v19 offset:17408\n" \
  "ds_read_b64_tr_b16 a[252:253], v19 offset:24576\n" \
  "ds_read_b64_tr_b16 a[254:255], v19 offset:25600\n" \
  "s_waitcnt vmcnt(16) lgkmcnt(0)\n" \
  "s_barrier\n" \
  "ds_read_b64_tr_b16 a[16:17], v18 offset:32768\n" \
  "ds_read_b64_tr_b16 a[18:19], v18 offset:33792\n" \
  "ds_read_b64_tr_b16 a[20:21], v18 offset:40960\n" \
  "ds_read_b64_tr_b16 a[22:23], v18 offset:41984\n" \
  "ds_read_b64_tr_b16 a[96:97], v19 offset:32768\n" \
  "ds_read_b64_tr_b16 a[98:99], v19 offset:33792\n" \
  "ds_read_b64_tr_b16 a[100:101], v19 offset:40960\n" \
  "ds_read_b64_tr_b16 a[102:103], v19 offset:41984\n" \
  "ds_read_b64_tr_b16 a[176:177], v18 offset:49152\n" \
  "ds_read_b64_tr_b16 a[178:179], v18 offset:50176\n" \
  "ds_read_b64_tr_b16 a[180:181], v18 offset:57344\n" \
  "ds_read_b64_tr_b16 a[182:183], v18 offset:58368\n" \
  "ds_read_b64_tr_b16 v[192:193], v19 offset:49152\n" \
  "ds_read_b64_tr_b16 v[194:195], v19 offset:50176\n" \
  "ds_read_b64_tr_b16 v[196:197], v19 offset:57344\n" \
  "ds_read_b64_tr_b16 v[198:199], v19 offset:58368\n" \
  "s_waitcnt vmcnt(8) lgkmcnt(0)\n" \
  "s_barrier\n" \
  "v_xor_b32 v18, 0x10000, v18\n" \
  "v_xor_b32 v19, 0x10000, v19\n" \
  "ds_read_b64_tr_b16 a[24:25], v18 offset:0\n" \
  "ds_read_b64_tr_b16 a[26:27], v18 offset:1024\n" \
  "ds_read_b64_tr_b16 a[28:29], v18 offset:8192\n" \
  "ds_read_b64_tr_b16 a[30:31], v18 offset:9216\n" \
  "ds_read_b64_tr_b16 a[104:105], v19 offset:0\n" \
  "ds_read_b64_tr_b16 a[106:107], v19 offset:1024\n" \
  "ds_read_b64_tr_b16 a[108:109], v19 offset:8192\n" \
  "ds_read_b64_tr_b16 a[110:111], v19 offset:9216\n" \
  "ds_read_b64_tr_b16 a[184:185], v18 offset:16384\n" \
  "ds_read_b64_tr_b16 a[186:187], v18 offset:17408\n" \
  "ds_read_b64_tr_b16 a[188:189], v18 offset:24576\n" \
  "ds_read_b64_tr_b16 a[190:191], v18 offset:25600\n" \
  "ds_read_b64_tr_b16 v[200:201], v19 offset:16384\n" \
  "ds_read_b64_tr_b16 v[202:203], v19 offset:17408\n" \
  "ds_read_b64_tr_b16 v[204:205], v19 offset:24576\n" \
  "ds_read_b64_tr_b16 v[206:207], v19 offset:25600\n" \
  "s_waitcnt vmcnt(0) lgkmcnt(0)\n" \
  "s_barrier\n" \
  "ds_read_b64_tr_b16 a[32:33], v18 offset:32768\n" \
  "ds_read_b64_tr_b16 a[34:35], v18 offset:33792\n" \
  "ds_read_b64_tr_b16 a[36:37], v18 offset:40960\n" \
  "ds_read_b64_tr_b16 a[38:39], v18 offset:41984\n" \
  "ds_read_b64_tr_b16 a[112:113], v19 offset:32768\n" \
  "ds_read_b64_tr_b16 a[114:115], v19 offset:33792\n" \
  "ds_read_b64_tr_b16 a[116:117], v19 offset:40960\n" \
  "ds_read_b64_tr_b16 a[118:119], v19 offset:41984\n" \
  "ds_read_b64_tr_b16 a[192:193], v18 offset:49152\n" \
  "ds_read_b64_tr_b16 a[194:195], v18 offset:50176\n" \
  "ds_read_b64_tr_b16 a[196:197], v18 offset:57344\n" \
  "ds_read_b64_tr_b16 a[198:199], v18 offset:58368\n" \
  "ds_read_b64_tr_b16 v[208:209], v19 offset:49152\n" \
  "ds_read_b64_tr_b16 v[210:211], v19 offset:50176\n" \
  "ds_read_b64_tr_b16 v[212:213], v19 offset:57344\n" \
  "ds_read_b64_tr_b16 v[214:215], v19 offset:58368\n" \
  "s_waitcnt lgkmcnt(0)\n" \
  "s_barrier\n" \
  "s_mov_b32 s80, 0\n" \
  "s_cmp_lg_u32 s64, 0\n" \
  "s_cbranch_scc1 LY%=\n" \
  "s_mov_b32 s55, 0x40000\n" \
  "s_mov_b32 s88, 119808\n" \
  "s_mov_b64 exec, 1\n" \
  "buffer_load_dwordx2 v[188:189], off, s[36:39], s88 sc1\n" \
  "IW%=:\n" \
  "buffer_load_dwordx2 v[190:191], off, s[36:39], s65 offset:8 sc1\n" \
  "s_sub_u32 s55, s55, 1\n" \
  "s_cbranch_scc1 IX%=\n" \
  "s_waitcnt vmcnt(0)\n" \
  "v_readfirstlane_b32 s85, v188\n" \
  "v_readfirstlane_b32 s86, v189\n" \
  "v_readfirstlane_b32 s10, v190\n" \
  "v_readfirstlane_b32 s11, v191\n" \
  "s_mul_i32 s12, s85, 0x9e3779b1\n" \
  "s_add_u32 s12, s12, 0x7f4a7c15\n" \
  "s_mul_i32 s13, s86, 0x85ebca6b\n" \
  "s_add_u32 s13, s13, 0xc2b2ae35\n" \
  "s_xor_b64 s[10:11], s[10:11], s[12:13]\n" \
  "s_lshr_b32 s12, s11, 1\n" \
  "s_or_b32 s12, s12, s10\n" \
  "s_cbranch_scc0 IX%=\n" \
  "s_sleep 3\n" \
  "s_branch IW%=\n" \
  "IX%=:\n" \
  "s_waitcnt vmcnt(0)\n" \
  "s_mov_b64 exec, -1\n" \
  "s_mov_b32 s55, 0x10000\n" \
  "LY%=:\n" \
  "s_barrier\n" \
  "s_mul_i32 s78, s64, 10240\n" \
  "s_add_u32 s78, s78, 32768\n" \
  "s_mov_b32 s79, 0\n" \
  "s_add_u32 m0, s78, 0\n" \
  "s_add_u32 s87, s48, 0\n" \
  "buffer_load_dwordx4 v0, s[44:47], s87 offen sc1 lds\n" \
  "buffer_load_dwordx4 v0, s[44:47], s87 offen offset:1024 sc1 lds\n" \
  "buffer_load_dwordx4 v0, s[44:47], s87 offen offset:2048 sc1 lds\n" \
  "buffer_load_dwordx4 v0, s[44:47], s87 offen offset:3072 sc1 lds\n" \
  "s_add_u32 m0, s78, 4096\n" \
  "s_add_u32 s87, s48, 4096\n" \
  "buffer_load_dwordx4 v0, s[44:47], s87 offen sc1 lds\n" \
  "buffer_load_dwordx4 v0, s[44:47], s87 offen offset:1024 sc1 lds\n" \
  "buffer_load_dwordx4 v0, s[44:47], s87 offen offset:2048 sc1 lds\n" \
  "buffer_load_dwordx4 v0, s[44:47], s87 offen offset:3072 sc1 lds\n" \
  "s_add_u32 m0, s78, 8192\n" \
  "s_add_u32 s87, s48, 8192\n" \
  "buffer_load_dwordx4 v0, s[44:47], s87 offen sc1 lds\n" \
  "buffer_load_dwordx4 v0, s[44:47], s87 offen offset:1024 sc1 lds\n" \
  "s_add_u32 s48, s48, 40960\n" \
  "s_add_u32 s78, s78, 40960\n" \
  "s_add_u32 m0, s78, 0\n" \
  "s_add_u32 s87, s48, 0\n" \
  "buffer_load_dwordx4 v0, s[44:47], s87 offen sc1 lds\n" \
  "buffer_load_dwordx4 v0, s[44:47], s87 offen offset:1024 sc1 lds\n" \
  "buffer_load_dwordx4 v0, s[44:47], s87 offen offset:2048 sc1 lds\n" \
  "buffer_load_dwordx4 v0, s[44:47], s87 offen offset:3072 sc1 lds\n" \
  "s_add_u32 m0, s78, 4096\n" \
  "s_add_u32 s87, s48, 4096\n" \
  "buffer_load_dwordx4 v0, s[44:47], s87 offen sc1 lds\n" \
  "buffer_load_dwordx4 v0, s[44:47], s87 offen offset:1024 sc1 lds\n" \
  "buffer_load_dwordx4 v0, s[44:47], s87 offen offset:2048 sc1 lds\n" \
  "buffer_load_dwordx4 v0, s[44:47], s87 offen offset:3072 sc1 lds\n" \
  "s_add_u32 m0, s78, 8192\n" \
  "s_add_u32 s87, s48, 8192\n" \
  "buffer_load_dwordx4 v0, s[44:47], s87 offen sc1 lds\n" \
  "buffer_load_dwordx4 v0, s[44:47], s87 offen offset:1024 sc1 lds\n" \
  "s_add_u32 s48, s48, 40960\n" \
  "s_add_u32 s78, s78, 40960\n" \
  "s_mov_b32 s30, 0x80000000\n" \
  "s_mov_b32 s31, 0x80000000\n" \
  "s_waitcnt vmcnt(10)\n" \
  "s_barrier\n" \
  "ds_read_b128 v[32:35], v1 offset:0\n" \
  "ds_read_b128 v[36:39], v1 offset:1024\n" \
  "ds_read_b128 v[40:43], v1 offset:2048\n" \
  "s_waitcnt lgkmcnt(0)\n" \
  "s_memtime s[20:21]\n" \
  "s_memrealtime s[22:23]\n" \
  "s_mov_b32 s24, 0\n" \
  "s_waitcnt lgkmcnt(0)\n" \
  "LOOP%=:\n" \
  "v_mfma_f32_16x16x32_bf16 v[44:47], a[160:163], v[32:35], 0\n" \
  "ds_read_b128 v[48:51], v1 offset:3072\n" \
  "v_mfma_f32_16x16x32_bf16 v[52:55], a[240:243], v[32:35], 0\n" \
  "v_mfma_f32_16x16x32_bf16 v[56:59], a[0:3], v[32:35], 0\n" \
  "buffer_load_dword v12, v9, s[36:39], 0 offen sc1\n" \
  "v_mfma_f32_16x16x32_bf16 v[60:63], a[80:83], v[32:35], 0\n" \
  "v_mfma_f32_16x16x32_bf16 v[44:47], a[164:167], v[36:39], v[44:47]\n" \
  "ds_read_b128 v[32:35], v1 offset:4096\n" \
  "v_mfma_f32_16x16x32_bf16 v[52:55], a[244:247], v[36:39], v[52:55]\n" \
  "v_cvt_pk_bf16_f32 v64, v16, v17\n" \
  "v_cvt_pk_bf16_f32 v65, v18, v19\n" \
  "v_mfma_f32_16x16x32_bf16 v[56:59], a[4:7], v[36:39], v[56:59]\n" \
  "v_cvt_pk_bf16_f32 v66, v20, v21\n" \
  "v_cvt_pk_bf16_f32 v67, v22, v23\n" \
  "v_mfma_f32_16x16x32_bf16 v[60:63], a[84:87], v[36:39], v[60:63]\n" \
  "buffer_store_dwordx4 v[64:67], v0, s[44:47], s30 offen sc1\n" \
  "v_mfma_f32_16x16x32_bf16 v[44:47], a[168:171], v[40:43], v[44:47]\n" \
  "ds_read_b128 v[36:39], v1 offset:5120\n" \
  "v_mfma_f32_16x16x32_bf16 v[52:55], a[248:251], v[40:43], v[52:55]\n" \
  "v_cvt_pk_bf16_f32 v64, v24, v25\n" \
  "v_cvt_pk_bf16_f32 v65, v26, v27\n" \
  "v_mfma_f32_16x16x32_bf16 v[56:59], a[8:11], v[40:43], v[56:59]\n" \
  "v_cvt_pk_bf16_f32 v66, v28, v29\n" \
  "v_cvt_pk_bf16_f32 v67, v30, v31\n" \
  "v_mfma_f32_16x16x32_bf16 v[60:63], a[88:91], v[40:43], v[60:63]\n" \
  "buffer_store_dwordx4 v[64:67], v0, s[44:47], s31 offen sc1\n" \
  "s_waitcnt lgkmcnt(2)\n" \
  "v_mfma_f32_16x16x32_bf16 v[44:47], a[172:175], v[48:51], v[44:47]\n" \
  "ds_read_b128 v[40:43], v1 offset:6144\n" \
  "v_mfma_f32_16x16x32_bf16 v[52:55], a[252:255], v[48:51], v[52:55]\n" \
  "s_cmp_lg_u32 s80, 12\n" \
  "s_cbranch_scc1 MY%=\n" \
  "s_mov_b32 s55, 0x10000\n" \
  "s_mov_b64 exec, 1\n" \
  "MW%=:\n" \
  "buffer_load_dword v11, off, s[36:39], s65 offset:2048 sc1\n" \
  "s_sub_u32 s55, s55, 1\n" \
  "s_cbranch_scc1 MX%=\n" \
  "s_waitcnt vmcnt(0)\n" \
  "v_readfirstlane_b32 s29, v11\n" \
  "s_cmp_lg_u32 s29, -1\n" \
  "s_cbranch_scc1 MX%=\n" \
  "s_sleep 3\n" \
  "s_branch MW%=\n" \
  "MX%=:\n" \
  "s_waitcnt vmcnt(0)\n" \
  "s_mov_b64 exec, -1\n" \
  "s_mov_b32 s55, 0x10000\n" \
  "MY%=:\n" \
  "v_mfma_f32_16x16x32_bf16 v[56:59], a[12:15], v[48:51], v[56:59]\n" \
  "s_add_u32 m0, s78, 0\n" \
  "s_add_u32 s87, s48, 0\n" \
  "buffer_load_dwordx4 v0, s[44:47], s87 offen sc1 lds\n" \
  "v_mfma_f32_16x16x32_bf16 v[60:63], a[92:95], v[48:51], v[60:63]\n" \
  "s_waitcnt lgkmcnt(2)\n" \
  "v_mfma_f32_16x16x32_bf16 v[44:47], a[176:179], v[32:35], v[44:47]\n" \
  "ds_read_b128 v[16:19], v1 offset:7168\n" \
  "v_mfma_f32_16x16x32_bf16 v[52:55], v[192:195], v[32:35], v[52:55]\n" \
  "v_mfma_f32_16x16x32_bf16 v[56:59], a[16:19], v[32:35], v[56:59]\n" \
  "v_mfma_f32_16x16x32_bf16 v[60:63], a[96:99], v[32:35], v[60:63]\n" \
  "s_waitcnt lgkmcnt(2)\n" \
  "v_mfma_f32_16x16x32_bf16 v[44:47], a[180:183], v[36:39], v[44:47]\n" \
  "ds_read_b128 v[20:23], v1 offset:8192\n" \
  "buffer_load_dwordx4 v0, s[44:47], s87 offen offset:1024 sc1 lds\n" \
  "v_mfma_f32_16x16x32_bf16 v[52:55], v[196:199], v[36:39], v[52:55]\n" \
  "v_mfma_f32_16x16x32_bf16 v[56:59], a[20:23], v[36:39], v[56:59]\n" \
  "v_mfma_f32_16x16x32_bf16 v[60:63], a[100:103], v[36:39], v[60:63]\n" \
  "s_waitcnt lgkmcnt(2)\n" \
  "v_mfma_f32_16x16x32_bf16 v[44:47], a[184:187], v[40:43], v[44:47]\n" \
  "ds_read_b128 v[24:27], v1 offset:9216\n" \
  "v_mfma_f32_16x16x32_bf16 v[52:55], v[200:203], v[40:43], v[52:55]\n" \
  "v_mfma_f32_16x16x32_bf16 v[56:59], a[24:27], v[40:43], v[56:59]\n" \
  "buffer_load_dwordx4 v0, s[44:47], s87 offen offset:2048 sc1 lds\n" \
  "v_mfma_f32_16x16x32_bf16 v[60:63], a[104:107], v[40:43], v[60:63]\n" \
  "s_waitcnt lgkmcnt(2)\n" \
  "v_mfma_f32_16x16x32_bf16 v[44:47], a[188:191], v[16:19], v[44:47]\n" \
  "ds_read_b128 v[28:31], v1 offset:10240\n" \
  "v_mfma_f32_16x16x32_bf16 v[52:55], v[204:207], v[16:19], v[52:55]\n" \
  "v_mfma_f32_16x16x32_bf16 v[56:59], a[28:31], v[16:19], v[56:59]\n" \
  "v_mfma_f32_16x16x32_bf16 v[60:63], a[108:111], v[16:19], v[60:63]\n" \
  "s_waitcnt lgkmcnt(2)\n" \
  "v_mfma_f32_16x16x32_bf16 v[44:47], a[192:195], v[20:23], v[44:47]\n" \
  "ds_read_b128 v[16:19], v1 offset:11264\n" \
  "buffer_load_dwordx4 v0, s[44:47], s87 offen offset:3072 sc1 lds\n" \
  "v_mfma_f32_16x16x32_bf16 v[52:55], v[208:211], v[20:23], v[52:55]\n" \
  "v_mfma_f32_16x16x32_bf16 v[56:59], a[32:35], v[20:23], v[56:59]\n" \
  "v_mfma_f32_16x16x32_bf16 v[60:63], a[112:115], v[20:23], v[60:63]\n" \
  "s_waitcnt lgkmcnt(2)\n" \
  "v_mfma_f32_16x16x32_bf16 v[44:47], a[196:199], v[24:27], v[44:47]\n" \
  "ds_read_b128 v[20:23], v1 offset:12288\n" \
  "v_mfma_f32_16x16x32_bf16 v[52:55], v[212:215], v[24:27], v[52:55]\n" \
  "v_mfma_f32_16x16x32_bf16 v[56:59], a[36:39], v[24:27], v[56:59]\n" \
  "s_add_u32 m0, s78, 4096\n" \
  "s_add_u32 s87, s48, 4096\n" \
  "buffer_load_dwordx4 v0, s[44:47], s87 offen sc1 lds\n" \
  "v_mfma_f32_16x16x32_bf16 v[60:63], a[116:119], v[24:27], v[60:63]\n" \
  "s_waitcnt lgkmcnt(2)\n" \
  "v_mfma_f32_16x16x32_bf16 v[44:47], a[200:203], v[28:31], v[44:47]\n" \
  "ds_read_b128 v[24:27], v1 offset:13312\n" \
  "v_mfma_f32_16x16x32_bf16 v[52:55], v[216:219], v[28:31], v[52:55]\n" \
  "v_mfma_f32_16x16x32_bf16 v[56:59], a[40:43], v[28:31], v[56:59]\n" \
  "v_mfma_f32_16x16x32_bf16 v[60:63], a[120:123], v[28:31], v[60:63]\n" \
  "s_waitcnt lgkmcnt(2)\n" \
  "v_mfma_f32_16x16x32_bf16 v[44:47], a[204:207], v[16:19], v[44:47]\n" \
  "ds_read_b128 v[28:31], v1 offset:14336\n" \
  "buffer_load_dwordx4 v0, s[44:47], s87 offen offset:1024 sc1 lds\n" \
  "v_mfma_f32_16x16x32_bf16 v[52:55], v[220:223], v[16:19], v[52:55]\n" \
  "v_mfma_f32_16x16x32_bf16 v[56:59], a[44:47], v[16:19], v[56:59]\n" \
  "v_mfma_f32_16x16x32_bf16 v[60:63], a[124:127], v[16:19], v[60:63]\n" \
  "s_waitcnt lgkmcnt(2)\n" \
  "v_mfma_f32_16x16x32_bf16 v[44:47], a[208:211], v[20:23], v[44:47]\n" \
  "ds_read_b128 v[16:19], v1 offset:15360\n" \
  "v_mfma_f32_16x16x32_bf16 v[52:55], v[224:227], v[20:23], v[52:55]\n" \
  "v_mfma_f32_16x16x32_bf16 v[56:59], a[48:51], v[20:23], v[56:59]\n" \
  "buffer_load_dwordx4 v0, s[44:47], s87 offen offset:2048 sc1 lds\n" \
  "v_mfma_f32_16x16x32_bf16 v[60:63], a[128:131], v[20:23], v[60:63]\n" \
  "s_waitcnt lgkmcnt(2)\n" \
  "v_mfma_f32_16x16x32_bf16 v[44:47], a[212:215], v[24:27], v[44:47]\n" \
  "ds_read_b128 v[20:23], v1 offset:16384\n" \
  "v_mfma_f32_16x16x32_bf16 v[52:55], v[228:231], v[24:27], v[52:55]\n" \
  "v_mfma_f32_16x16x32_bf16 v[56:59], a[52:55], v[24:27], v[56:59]\n" \
  "v_mfma_f32_16x16x32_bf16 v[60:63], a[132:135], v[24:27], v[60:63]\n" \
  "s_waitcnt lgkmcnt(2)\n" \
  "v_mfma_f32_16x16x32_bf16 v[44:47], a[216:219], v[28:31], v[44:47]\n" \
  "ds_read_b128 v[24:27], v1 offset:17408\n" \
  "buffer_load_dwordx4 v0, s[44:47], s87 offen offset:3072 sc1 lds\n" \
  "v_mfma_f32_16x16x32_bf16 v[52:55], v[232:235], v[28:31], v[52:55]\n" \
  "v_mfma_f32_16x16x32_bf16 v[56:59], a[56:59], v[28:31], v[56:59]\n" \
  "v_mfma_f32_16x16x32_bf16 v[60:63], a[136:139], v[28:31], v[60:63]\n" \
  "s_cmp_eq_u32 s80, 0\n" \
  "s_cbranch_scc1 NP%=\n" \
  "s_waitcnt vmcnt(8)\n" \
  "s_mov_b64 exec, 1\n" \
  "buffer_atomic_add v10, off, s[36:39], s52\n" \
  "s_mov_b64 exec, -1\n" \
  "NP%=:\n" \
  "s_waitcnt lgkmcnt(2)\n" \
  "v_mfma_f32_16x16x32_bf16 v[44:47], a[220:223], v[16:19], v[44:47]\n" \
  "ds_read_b128 v[28:31], v1 offset:18432\n" \
  "v_mfma_f32_16x16x32_bf16 v[52:55], v[236:239], v[16:19], v[52:55]\n" \
  "v_mfma_f32_16x16x32_bf16 v[56:59], a[60:63], v[16:19], v[56:59]\n" \
  "s_add_u32 m0, s78, 8192\n" \
  "s_add_u32 s87, s48, 8192\n" \
  "buffer_load_dwordx4 v0, s[44:47], s87 offen sc1 lds\n" \
  "v_mfma_f32_16x16x32_bf16 v[60:63], a[140:143], v[16:19], v[60:63]\n" \
  "s_waitcnt lgkmcnt(2)\n" \
  "v_mfma_f32_16x16x32_bf16 v[44:47], a[224:227], v[20:23], v[44:47]\n" \
  "ds_read_b128 v[16:19], v1 offset:19456\n" \
  "v_mfma_f32_16x16x32_bf16 v[52:55], v[240:243], v[20:23], v[52:55]\n" \
  "v_mfma_f32_16x16x32_bf16 v[56:59], a[64:67], v[20:23], v[56:59]\n" \
  "v_mfma_f32_16x16x32_bf16 v[60:63], a[144:147], v[20:23], v[60:63]\n" \
  "s_waitcnt lgkmcnt(2)\n" \
  "v_mfma_f32_16x16x32_bf16 v[44:47], a[228:231], v[24:27], v[44:47]\n" \
  "ds_read_b128 v[32:35], v1 offset:20480\n" \
  "buffer_load_dwordx4 v0, s[44:47], s87 offen offset:1024 sc1 lds\n" \
  "v_mfma_f32_16x16x32_bf16 v[52:55], v[244:247], v[24:27], v[52:55]\n" \
  "v_mfma_f32_16x16x32_bf16 v[56:59], a[68:71], v[24:27], v[56:59]\n" \
  "v_mfma_f32_16x16x32_bf16 v[60:63], a[148:151], v[24:27], v[60:63]\n" \
  "s_waitcnt lgkmcnt(2)\n" \
  "v_mfma_f32_16x16x32_bf16 v[44:47], a[232:235], v[28:31], v[44:47]\n" \
  "ds_read_b128 v[36:39], v1 offset:21504\n" \
  "v_mfma_f32_16x16x32_bf16 v[52:55], v[248:251], v[28:31], v[52:55]\n" \
  "v_mfma_f32_16x16x32_bf16 v[56:59], a[72:75], v[28:31], v[56:59]\n" \
  "v_mfma_f32_16x16x32_bf16 v[60:63], a[152:155], v[28:31], v[60:63]\n" \
  "s_waitcnt lgkmcnt(2)\n" \
  "v_mfma_f32_16x16x32_bf16 v[44:47], a[236:239], v[16:19], v[44:47]\n" \
  "ds_read_b128 v[40:43], v1 offset:22528\n" \
  "v_mfma_f32_16x16x32_bf16 v[52:55], v[252:255], v[16:19], v[52:55]\n" \
  "v_mfma_f32_16x16x32_bf16 v[56:59], a[76:79], v[16:19], v[56:59]\n" \
  "v_mfma_f32_16x16x32_bf16 v[60:63], a[156:159], v[16:19], v[60:63]\n" \
  "s_waitcnt lgkmcnt(2)\n" \
  "v_mfma_f32_16x16x32_bf16 v[16:19], a[160:163], v[32:35], 0\n" \
  "ds_read_b128 v[48:51], v1 offset:23552\n" \
  "v_mfma_f32_16x16x32_bf16 v[20:23], a[240:243], v[32:35], 0\n" \
  "v_mfma_f32_16x16x32_bf16 v[24:27], a[0:3], v[32:35], 0\n" \
  "v_mfma_f32_16x16x32_bf16 v[28:31], a[80:83], v[32:35], 0\n" \
  "s_waitcnt lgkmcnt(2)\n" \
  "v_mfma_f32_16x16x32_bf16 v[16:19], a[164:167], v[36:39], v[16:19]\n" \
  "ds_read_b128 v[32:35], v1 offset:24576\n" \
  "v_mfma_f32_16x16x32_bf16 v[20:23], a[244:247], v[36:39], v[20:23]\n" \
  "v_mfma_f32_16x16x32_bf16 v[24:27], a[4:7], v[36:39], v[24:27]\n" \
  "v_mfma_f32_16x16x32_bf16 v[28:31], a[84:87], v[36:39], v[28:31]\n" \
  "s_waitcnt lgkmcnt(2)\n" \
  "v_mfma_f32_16x16x32_bf16 v[16:19], a[168:171], v[40:43], v[16:19]\n" \
  "ds_read_b128 v[36:39], v1 offset:25600\n" \
  "v_mfma_f32_16x16x32_bf16 v[20:23], a[248:251], v[40:43], v[20:23]\n" \
  "v_cvt_pk_bf16_f32 v64, v44, v45\n" \
  "v_cvt_pk_bf16_f32 v65, v46, v47\n" \
  "v_mfma_f32_16x16x32_bf16 v[24:27], a[8:11], v[40:43], v[24:27]\n" \
  "v_cvt_pk_bf16_f32 v66, v52, v53\n" \
  "v_cvt_pk_bf16_f32 v67, v54, v55\n" \
  "v_mfma_f32_16x16x32_bf16 v[28:31], a[88:91], v[40:43], v[28:31]\n" \
  "buffer_store_dwordx4 v[64:67], v0, s[44:47], s49 offen sc1\n" \
  "s_waitcnt lgkmcnt(2)\n" \
  "v_mfma_f32_16x16x32_bf16 v[16:19], a[172:175], v[48:51], v[16:19]\n" \
  "ds_read_b128 v[40:43], v1 offset:26624\n" \
  "v_mfma_f32_16x16x32_bf16 v[20:23], a[252:255], v[48:51], v[20:23]\n" \
  "v_cvt_pk_bf16_f32 v44, v56, v57\n" \
  "v_cvt_pk_bf16_f32 v45, v58, v59\n" \
  "v_mfma_f32_16x16x32_bf16 v[24:27], a[12:15], v[48:51], v[24:27]\n" \
  "v_cvt_pk_bf16_f32 v46, v60, v61\n" \
  "v_cvt_pk_bf16_f32 v47, v62, v63\n" \
  "v_mfma_f32_16x16x32_bf16 v[28:31], a[92:95], v[48:51], v[28:31]\n" \
  "buffer_store_dwordx4 v[44:47], v0, s[44:47], s50 offen sc1\n" \
  "s_waitcnt lgkmcnt(2)\n" \
  "v_mfma_f32_16x16x32_bf16 v[16:19], a[176:179], v[32:35], v[16:19]\n" \
  "ds_read_b128 v[44:47], v1 offset:27648\n" \
  "v_mfma_f32_16x16x32_bf16 v[20:23], v[192:195], v[32:35], v[20:23]\n" \
  "v_mfma_f32_16x16x32_bf16 v[24:27], a[16:19], v[32:35], v[24:27]\n" \
  "v_mfma_f32_16x16x32_bf16 v[28:31], a[96:99], v[32:35], v[28:31]\n" \
  "s_waitcnt lgkmcnt(2)\n" \
  "v_mfma_f32_16x16x32_bf16 v[16:19], a[180:183], v[36:39], v[16:19]\n" \
  "ds_read_b128 v[32:35], v1 offset:28672\n" \
  "v_mfma_f32_16x16x32_bf16 v[20:23], v[196:199], v[36:39], v[20:23]\n" \
  "s_add_u32 s30, s49, 4096\n" \
  "s_add_u32 s31, s50, 4096\n" \
  "v_mfma_f32_16x16x32_bf16 v[24:27], a[20:23], v[36:39], v[24:27]\n" \
  "s_add_u32 s49, s49, 8192\n" \
  "s_add_u32 s50, s50, 8192\n" \
  "v_mfma_f32_16x16x32_bf16 v[28:31], a[100:103], v[36:39], v[28:31]\n" \
  "s_bitset0_b32 s49, 17\n" \
  "s_bitset0_b32 s50, 17\n" \
  "s_waitcnt lgkmcnt(2)\n" \
  "v_mfma_f32_16x16x32_bf16 v[16:19], a[184:187], v[40:43], v[16:19]\n" \
  "ds_read_b128 v[36:39], v1 offset:29696\n" \
  "v_mfma_f32_16x16x32_bf16 v[20:23], v[200:203], v[40:43], v[20:23]\n" \
  "s_add_u32 s78, s78, 40960\n" \
  "s_add_u32 s79, s79, 1\n" \
  "v_mfma_f32_16x16x32_bf16 v[24:27], a[24:27], v[40:43], v[24:27]\n" \
  "s_cmp_eq_u32 s79, 1\n" \
  "s_cselect_b32 s85, 122880, 0\n" \
  "v_mfma_f32_16x16x32_bf16 v[28:31], a[104:107], v[40:43], v[28:31]\n" \
  "s_waitcnt lgkmcnt(2)\n" \
  "v_mfma_f32_16x16x32_bf16 v[16:19], a[188:191], v[44:47], v[16:19]\n" \
  "ds_read_b128 v[40:43], v1 offset:30720\n" \
  "v_mfma_f32_16x16x32_bf16 v[20:23], v[204:207], v[44:47], v[20:23]\n" \
  "s_sub_u32 s78, s78, s85\n" \
  "s_cmp_eq_u32 s79, 3\n" \
  "v_mfma_f32_16x16x32_bf16 v[24:27], a[28:31], v[44:47], v[24:27]\n" \
  "s_cselect_b32 s85, 122880, 0\n" \
  "s_cselect_b32 s79, 0, s79\n" \
  "v_mfma_f32_16x16x32_bf16 v[28:31], a[108:111], v[44:47], v[28:31]\n" \
  "s_waitcnt lgkmcnt(2)\n" \
  "v_mfma_f32_16x16x32_bf16 v[16:19], a[192:195], v[32:35], v[16:19]\n" \
  "ds_read_b128 v[44:47], v1 offset:31744\n" \
  "v_mfma_f32_16x16x32_bf16 v[20:23], v[208:211], v[32:35], v[20:23]\n" \
  "s_add_u32 s48, s48, 40960\n" \
  "s_add_u32 s80, s80, 1\n" \
  "v_mfma_f32_16x16x32_bf16 v[24:27], a[32:35], v[32:35], v[24:27]\n" \
  "v_mfma_f32_16x16x32_bf16 v[28:31], a[112:115], v[32:35], v[28:31]\n" \
  "s_waitcnt lgkmcnt(2)\n" \
  "v_mfma_f32_16x16x32_bf16 v[16:19], a[196:199], v[36:39], v[16:19]\n" \
  "ds_read_b128 v[32:35], v1 offset:32768\n" \
  "v_mfma_f32_16x16x32_bf16 v[20:23], v[212:215], v[36:39], v[20:23]\n" \
  "v_mfma_f32_16x16x32_bf16 v[24:27], a[36:39], v[36:39], v[24:27]\n" \
  "v_mfma_f32_16x16x32_bf16 v[28:31], a[116:119], v[36:39], v[28:31]\n" \
  "s_waitcnt lgkmcnt(2)\n" \
  "v_mfma_f32_16x16x32_bf16 v[16:19], a[200:203], v[40:43], v[16:19]\n" \
  "ds_read_b128 v[36:39], v1 offset:33792\n" \
  "v_mfma_f32_16x16x32_bf16 v[20:23], v[216:219], v[40:43], v[20:23]\n" \
  "v_mfma_f32_16x16x32_bf16 v[24:27], a[40:43], v[40:43], v[24:27]\n" \
  "v_mfma_f32_16x16x32_bf16 v[28:31], a[120:123], v[40:43], v[28:31]\n" \
  "s_waitcnt lgkmcnt(2)\n" \
  "v_mfma_f32_16x16x32_bf16 v[16:19], a[204:207], v[44:47], v[16:19]\n" \
  "ds_read_b128 v[40:43], v1 offset:34816\n" \
  "v_mfma_f32_16x16x32_bf16 v[20:23], v[220:223], v[44:47], v[20:23]\n" \
  "v_mfma_f32_16x16x32_bf16 v[24:27], a[44:47], v[44:47], v[24:27]\n" \
  "v_mfma_f32_16x16x32_bf16 v[28:31], a[124:127], v[44:47], v[28:31]\n" \
  "s_waitcnt lgkmcnt(2)\n" \
  "v_mfma_f32_16x16x32_bf16 v[16:19], a[208:211], v[32:35], v[16:19]\n" \
  "ds_read_b128 v[44:47], v1 offset:35840\n" \
  "v_mfma_f32_16x16x32_bf16 v[20:23], v[224:227], v[32:35], v[20:23]\n" \
  "v_mfma_f32_16x16x32_bf16 v[24:27], a[48:51], v[32:35], v[24:27]\n" \
  "v_mfma_f32_16x16x32_bf16 v[28:31], a[128:131], v[32:35], v[28:31]\n" \
  "s_waitcnt lgkmcnt(2)\n" \
  "v_mfma_f32_16x16x32_bf16 v[16:19], a[212:215], v[36:39], v[16:19]\n" \
  "ds_read_b128 v[32:35], v1 offset:36864\n" \
  "v_mfma_f32_16x16x32_bf16 v[20:23], v[228:231], v[36:39], v[20:23]\n" \
  "v_mfma_f32_16x16x32_bf16 v[24:27], a[52:55], v[36:39], v[24:27]\n" \
  "v_mfma_f32_16x16x32_bf16 v[28:31], a[132:135], v[36:39], v[28:31]\n" \
  "s_waitcnt lgkmcnt(2)\n" \
  "v_mfma_f32_16x16x32_bf16 v[16:19], a[216:219], v[40:43], v[16:19]\n" \
  "ds_read_b128 v[48:51], v1 offset:37888\n" \
  "v_mfma_f32_16x16x32_bf16 v[20:23], v[232:235], v[40:43], v[20:23]\n" \
  "v_mfma_f32_16x16x32_bf16 v[24:27], a[56:59], v[40:43], v[24:27]\n" \
  "v_mfma_f32_16x16x32_bf16 v[28:31], a[136:139], v[40:43], v[28:31]\n" \
  "s_waitcnt lgkmcnt(2)\n" \
  "v_mfma_f32_16x16x32_bf16 v[16:19], a[220:223], v[44:47], v[16:19]\n" \
  "ds_read_b128 v[52:55], v1 offset:38912\n" \
  "v_mfma_f32_16x16x32_bf16 v[20:23], v[236:239], v[44:47], v[20:23]\n" \
  "v_mfma_f32_16x16x32_bf16 v[24:27], a[60:63], v[44:47], v[24:27]\n" \
  "v_mfma_f32_16x16x32_bf16 v[28:31], a[140:143], v[44:47], v[28:31]\n" \
  "s_waitcnt lgkmcnt(2)\n" \
  "v_mfma_f32_16x16x32_bf16 v[16:19], a[224:227], v[32:35], v[16:19]\n" \
  "ds_read_b128 v[44:47], v1 offset:39936\n" \
  "v_mfma_f32_16x16x32_bf16 v[20:23], v[240:243], v[32:35], v[20:23]\n" \
  "v_add_u32 v1, 0xa000, v1\n" \
  "v_subrev_u32 v1, s85, v1\n" \
  "v_mfma_f32_16x16x32_bf16 v[24:27], a[64:67], v[32:35], v[24:27]\n" \
  "v_mfma_f32_16x16x32_bf16 v[28:31], a[144:147], v[32:35], v[28:31]\n" \
  "s_waitcnt vmcnt(12) lgkmcnt(0)\n" \
  "s_cmp_lg_u32 s64, 0\n" \
  "s_cbranch_scc1 NCb%=\n" \
  "v_cmp_gt_i32 vcc, s53, v12\n" \
  "s_cbranch_vccz NCb%=\n" \
  "NBb%=:\n" \
  "s_cmp_eq_u32 s55, 0\n" \
  "s_cbranch_scc1 NCb%=\n" \
  "s_sub_u32 s55, s55, 1\n" \
  "s_sleep 8\n" \
  "buffer_load_dword v12, v9, s[36:39], 0 offen sc1\n" \
  "s_waitcnt vmcnt(0)\n" \
  "v_cmp_gt_i32 vcc, s53, v12\n" \
  "s_cbranch_vccnz NBb%=\n" \
  "NCb%=:\n" \
  "s_add_u32 s53, s53, 1\n" \
  "s_barrier\n" \
  "v_mfma_f32_16x16x32_bf16 v[16:19], a[228:231], v[48:51], v[16:19]\n" \
  "ds_read_b128 v[32:35], v1 offset:0\n" \
  "v_mfma_f32_16x16x32_bf16 v[20:23], v[244:247], v[48:51], v[20:23]\n" \
  "v_mfma_f32_16x16x32_bf16 v[24:27], a[68:71], v[48:51], v[24:27]\n" \
  "ds_read_b128 v[36:39], v1 offset:1024\n" \
  "v_mfma_f32_16x16x32_bf16 v[28:31], a[148:151], v[48:51], v[28:31]\n" \
  "v_mfma_f32_16x16x32_bf16 v[16:19], a[232:235], v[52:55], v[16:19]\n" \
  "ds_read_b128 v[40:43], v1 offset:2048\n" \
  "v_mfma_f32_16x16x32_bf16 v[20:23], v[248:251], v[52:55], v[20:23]\n" \
  "v_mfma_f32_16x16x32_bf16 v[24:27], a[72:75], v[52:55], v[24:27]\n" \
  "v_mfma_f32_16x16x32_bf16 v[28:31], a[152:155], v[52:55], v[28:31]\n" \
  "v_mfma_f32_16x16x32_bf16 v[16:19], a[236:239], v[44:47], v[16:19]\n" \
  "v_mfma_f32_16x16x32_bf16 v[20:23], v[252:255], v[44:47], v[20:23]\n" \
  "v_mfma_f32_16x16x32_bf16 v[24:27], a[76:79], v[44:47], v[24:27]\n" \
  "s_cmp_lg_u32 s80, 64\n" \
  "v_mfma_f32_16x16x32_bf16 v[28:31], a[156:159], v[44:47], v[28:31]\n" \
  "s_waitcnt lgkmcnt(0)\n" \
  "s_cbranch_scc1 LOOP%=\n" \
  "s_nop 1\n" \
  "v_cvt_pk_bf16_f32 v44, v16, v17\n" \
  "v_cvt_pk_bf16_f32 v45, v18, v19\n" \
  "v_cvt_pk_bf16_f32 v46, v20, v21\n" \
  "v_cvt_pk_bf16_f32 v47, v22, v23\n" \
  "buffer_store_dwordx4 v[44:47], v0, s[44:47], s30 offen sc1\n" \
  "s_nop 1\n" \
  "v_cvt_pk_bf16_f32 v44, v24, v25\n" \
  "v_cvt_pk_bf16_f32 v45, v26, v27\n" \
  "v_cvt_pk_bf16_f32 v46, v28, v29\n" \
  "v_cvt_pk_bf16_f32 v47, v30, v31\n" \
  "buffer_store_dwordx4 v[44:47], v0, s[44:47], s31 offen sc1\n" \
  "s_waitcnt vmcnt(0)\n" \
  "s_mov_b64 exec, 1\n" \
  "buffer_atomic_add v10, off, s[36:39], s52\n" \
  "s_mov_b64 exec, -1\n" \
  "DONE%=:\n" \
  "s_waitcnt vmcnt(0) lgkmcnt(0)\n" \
  "s_cmp_eq_u32 s73, -1\n" \
  "s_cbranch_scc1 END%=\n" \
  "s_memrealtime vcc\n" \
  "s_waitcnt lgkmcnt(0)\n" \
  "v_mov_b32 v32, vcc_lo\n" \
  "s_sub_u32 s22, vcc_lo, s22\n" \
  "s_memtime vcc\n" \
  "s_waitcnt lgkmcnt(0)\n" \
  "s_sub_u32 s20, vcc_lo, s20\n" \
  "v_mov_b32 v33, s20\n" \
  "v_mov_b32 v34, s22\n" \
  "v_mov_b32 v35, s24\n" \
  "s_mov_b64 exec, 1\n" \
  "buffer_store_dword v32, off, s[44:47], s73\n" \
  "buffer_store_dword v33, off, s[44:47], s73 offset:1024\n" \
  "buffer_store_dword v34, off, s[44:47], s73 offset:2048\n" \
  "buffer_store_dword v35, off, s[44:47], s73 offset:3072\n" \
  "s_waitcnt vmcnt(0)\n" \
  "END%=:\n" \
  "s_endpgm\n"
#define REC_VU_ASM \
  "s_lshl_b32 s86, s64, 12\n" \
  "s_add_u32 s86, s86, 32768\n" \
  "s_add_u32 m0, s86, 0\n" \
  "s_mov_b32 s85, 0\n" \
  "buffer_load_dwordx4 v16, s[56:59], s85 offen nt lds\n" \
  "s_add_u32 m0, s86, 1024\n" \
  "s_mov_b32 s85, 32768\n" \
  "buffer_load_dwordx4 v16, s[56:59], s85 offen nt lds\n" \
  "s_add_u32 m0, s86, 2048\n" \
  "s_mov_b32 s85, 65536\n" \
  "buffer_load_dwordx4 v17, s[56:59], s85 offen nt lds\n" \
  "s_add_u32 m0, s86, 3072\n" \
  "s_mov_b32 s85, 98304\n" \
  "buffer_load_dwordx4 v17, s[56:59], s85 offen nt lds\n" \
  "s_add_u32 m0, s86, 16384\n" \
  "s_mov_b32 s85, 524288\n" \
  "buffer_load_dwordx4 v16, s[56:59], s85 offen nt lds\n" \
  "s_add_u32 m0, s86, 17408\n" \
  "s_mov_b32 s85, 557056\n" \
  "buffer_load_dwordx4 v16, s[56:59], s85 offen nt lds\n" \
  "s_add_u32 m0, s86, 18432\n" \
  "s_mov_b32 s85, 589824\n" \
  "buffer_load_dwordx4 v17, s[56:59], s85 offen nt lds\n" \
  "s_add_u32 m0, s86, 19456\n" \
  "s_mov_b32 s85, 622592\n" \
  "buffer_load_dwordx4 v17, s[56:59], s85 offen nt lds\n" \
  "s_add_u32 m0, s86, 32768\n" \
  "s_mov_b32 s85, 1048576\n" \
  "buffer_load_dwordx4 v16, s[56:59], s85 offen nt lds\n" \
  "s_add_u32 m0, s86, 33792\n" \
  "s_mov_b32 s85, 1081344\n" \
  "buffer_load_dwordx4 v16, s[56:59], s85 offen nt lds\n" \
  "s_add_u32 m0, s86, 34816\n" \
  "s_mov_b32 s85, 1114112\n" \
  "buffer_load_dwordx4 v17, s[56:59], s85 offen nt lds\n" \
  "s_add_u32 m0, s86, 35840\n" \
  "s_mov_b32 s85, 1146880\n" \
  "buffer_load_dwordx4 v17, s[56:59], s85 offen nt lds\n" \
  "s_add_u32 m0, s86, 49152\n" \
  "s_mov_b32 s85, 1572864\n" \
  "buffer_load_dwordx4 v16, s[56:59], s85 offen nt lds\n" \
  "s_add_u32 m0, s86, 50176\n" \
  "s_mov_b32 s85, 1605632\n" \
  "buffer_load_dwordx4 v16, s[56:59], s85 offen nt lds\n" \
  "s_add_u32 m0, s86, 51200\n" \
  "s_mov_b32 s85, 1638400\n" \
  "buffer_load_dwordx4 v17, s[56:59], s85 offen nt lds\n" \
  "s_add_u32 m0, s86, 52224\n" \
  "s_mov_b32 s85, 1671168\n" \
  "buffer_load_dwordx4 v17, s[56:59], s85 offen nt lds\n" \
  "s_add_u32 m0, s86, 65536\n" \
  "s_mov_b32 s85, 2097152\n" \
  "buffer_load_dwordx4 v16, s[56:59], s85 offen nt lds\n" \
  "s_add_u32 m0, s86, 66560\n" \
  "s_mov_b32 s85, 2129920\n" \
  "buffer_load_dwordx4 v16, s[56:59], s85 offen nt lds\n" \
  "s_add_u32 m0, s86, 67584\n" \
  "s_mov_b32 s85, 2162688\n" \
  "buffer_load_dwordx4 v17, s[56:59], s85 offen nt lds\n" \
  "s_add_u32 m0, s86, 68608\n" \
  "s_mov_b32 s85, 2195456\n" \
  "buffer_load_dwordx4 v17, s[56:59], s85 offen nt lds\n" \
  "s_add_u32 m0, s86, 81920\n" \
  "s_mov_b32 s85, 2621440\n" \
  "buffer_load_dwordx4 v16, s[56:59], s85 offen nt lds\n" \
  "s_add_u32 m0, s86, 82944\n" \
  "s_mov_b32 s85, 2654208\n" \
  "buffer_load_dwordx4 v16, s[56:59], s85 offen nt lds\n" \
  "s_add_u32 m0, s86, 83968\n" \
  "s_mov_b32 s85, 2686976\n" \
  "buffer_load_dwordx4 v17, s[56:59], s85 offen nt lds\n" \
  "s_add_u32 m0, s86, 84992\n" \
  "s_mov_b32 s85, 2719744\n" \
  "buffer_load_dwordx4 v17, s[56:59], s85 offen nt lds\n" \
  "s_waitcnt vmcnt(16) lgkmcnt(0)\n" \
  "s_barrier\n" \
  "s_add_u32 m0, s86, 98304\n" \
  "s_mov_b32 s85, 3145728\n" \
  "buffer_load_dwordx4 v16, s[56:59], s85 offen nt lds\n" \
  "s_add_u32 m0, s86, 99328\n" \
  "s_mov_b32 s85, 3178496\n" \
  "buffer_load_dwordx4 v16, s[56:59], s85 offen nt lds\n" \
  "s_add_u32 m0, s86, 100352\n" \
  "s_mov_b32 s85, 3211264\n" \
  "buffer_load_dwordx4 v17, s[56:59], s85 offen nt lds\n" \
  "s_add_u32 m0, s86, 101376\n" \
  "s_mov_b32 s85, 3244032\n" \
  "buffer_load_dwordx4 v17, s[56:59], s85 offen nt lds\n" \
  "s_add_u32 m0, s86, 114688\n" \
  "s_mov_b32 s85, 3670016\n" \
  "buffer_load_dwordx4 v16, s[56:59], s85 offen nt lds\n" \
  "s_add_u32 m0, s86, 115712\n" \
  "s_mov_b32 s85, 3702784\n" \
  "buffer_load_dwordx4 v16, s[56:59], s85 offen nt lds\n" \
  "s_add_u32 m0, s86, 116736\n" \
  "s_mov_b32 s85, 3735552\n" \
  "buffer_load_dwordx4 v17, s[56:59], s85 offen nt lds\n" \
  "s_add_u32 m0, s86, 117760\n" \
  "s_mov_b32 s85, 3768320\n" \
  "buffer_load_dwordx4 v17, s[56:59], s85 offen nt lds\n" \
  "ds_read_b64_tr_b16 a[160:161], v18 offset:0\n" \
  "ds_read_b64_tr_b16 a[162:163], v18 offset:1024\n" \
  "ds_read_b64_tr_b16 a[164:165], v18 offset:8192\n" \
  "ds_read_b64_tr_b16 a[166:167], v18 offset:9216\n" \
  "ds_read_b64_tr_b16 a[240:241], v19 offset:0\n" \
  "ds_read_b64_tr_b16 a[242:243], v19 offset:1024\n" \
  "ds_read_b64_tr_b16 a[244:245], v19 offset:8192\n" \
  "ds_read_b64_tr_b16 a[246:247], v19 offset:9216\n" \
  "ds_read_b64_tr_b16 a[168:169], v18 offset:16384\n" \
  "ds_read_b64_tr_b16 a[170:171], v18 offset:17408\n" \
  "ds_read_b64_tr_b16 a[172:173], v18 offset:24576\n" \
  "ds_read_b64_tr_b16 a[174:175], v18 offset:25600\n" \
  "ds_read_b64_tr_b16 a[248:249], v19 offset:16384\n" \
  "ds_read_b64_tr_b16 a[250:251], v19 offset:17408\n" \
  "ds_read_b64_tr_b16 a[252:253], v19 offset:24576\n" \
  "ds_read_b64_tr_b16 a[254:255], v19 offset:25600\n" \
  "s_waitcnt vmcnt(16) lgkmcnt(0)\n" \
  "s_barrier\n" \
  "s_add_u32 m0, s86, 0\n" \
  "s_mov_b32 s85, 4194304\n" \
  "buffer_load_dwordx4 v16, s[56:59], s85 offen nt lds\n" \
  "s_add_u32 m0, s86, 1024\n" \
  "s_mov_b32 s85, 4227072\n" \
  "buffer_load_dwordx4 v16, s[56:59], s85 offen nt lds\n" \
  "s_add_u32 m0, s86, 2048\n" \
  "s_mov_b32 s85, 4259840\n" \
  "buffer_load_dwordx4 v17, s[56:59], s85 offen nt lds\n" \
  "s_add_u32 m0, s86, 3072\n" \
  "s_mov_b32 s85, 4292608\n" \
  "buffer_load_dwordx4 v17, s[56:59], s85 offen nt lds\n" \
  "s_add_u32 m0, s86, 16384\n" \
  "s_mov_b32 s85, 4718592\n" \
  "buffer_load_dwordx4 v16, s[56:59], s85 offen nt lds\n" \
  "s_add_u32 m0, s86, 17408\n" \
  "s_mov_b32 s85, 4751360\n" \
  "buffer_load_dwordx4 v16, s[56:59], s85 offen nt lds\n" \
  "s_add_u32 m0, s86, 18432\n" \
  "s_mov_b32 s85, 4784128\n" \
  "buffer_load_dwordx4 v17, s[56:59], s85 offen nt lds\n" \
  "s_add_u32 m0, s86, 19456\n" \
  "s_mov_b32 s85, 4816896\n" \
  "buffer_load_dwordx4 v17, s[56:59], s85 offen nt lds\n" \
  "ds_read_b64_tr_b16 a[176:177], v18 offset:32768\n" \
  "ds_read_b64_tr_b16 a[178:179], v18 offset:33792\n" \
  "ds_read_b64_tr_b16 a[180:181], v18 offset:40960\n" \
  "ds_read_b64_tr_b16 a[182:183], v18 offset:41984\n" \
  "ds_read_b64_tr_b16 v[192:193], v19 offset:32768\n" \
  "ds_read_b64_tr_b16 v[194:195], v19 offset:33792\n" \
  "ds_read_b64_tr_b16 v[196:197], v19 offset:40960\n" \
  "ds_read_b64_tr_b16 v[198:199], v19 offset:41984\n" \
  "ds_read_b64_tr_b16 a[184:185], v18 offset:49152\n" \
  "ds_read_b64_tr_b16 a[186:187], v18 offset:50176\n" \
  "ds_read_b64_tr_b16 a[188:189], v18 offset:57344\n" \
  "ds_read_b64_tr_b16 a[190:191], v18 offset:58368\n" \
  "ds_read_b64_tr_b16 v[200:201], v19 offset:49152\n" \
  "ds_read_b64_tr_b16 v[202:203], v19 offset:50176\n" \
  "ds_read_b64_tr_b16 v[204:205], v19 offset:57344\n" \
  "ds_read_b64_tr_b16 v[206:207], v19 offset:58368\n" \
  "s_waitcnt vmcnt(16) lgkmcnt(0)\n" \
  "s_barrier\n" \
  "s_add_u32 m0, s86, 32768\n" \
  "s_mov_b32 s85, 0\n" \
  "buffer_load_dwordx4 v20, s[60:63], s85 offen nt lds\n" \
  "s_add_u32 m0, s86, 33792\n" \
  "s_mov_b32 s85, 5120\n" \
  "buffer_load_dwordx4 v20, s[60:63], s85 offen nt lds\n" \
  "s_add_u32 m0, s86, 34816\n" \
  "s_mov_b32 s85, 10240\n" \
  "buffer_load_dwordx4 v21, s[60:63], s85 offen nt lds\n" \
  "s_add_u32 m0, s86, 35840\n" \
  "s_mov_b32 s85, 15360\n" \
  "buffer_load_dwordx4 v21, s[60:63], s85 offen nt lds\n" \
  "s_add_u32 m0, s86, 49152\n" \
  "s_mov_b32 s85, 81920\n" \
  "buffer_load_dwordx4 v20, s[60:63], s85 offen nt lds\n" \
  "s_add_u32 m0, s86, 50176\n" \
  "s_mov_b32 s85, 87040\n" \
  "buffer_load_dwordx4 v20, s[60:63], s85 offen nt lds\n" \
  "s_add_u32 m0, s86, 51200\n" \
  "s_mov_b32 s85, 92160\n" \
  "buffer_load_dwordx4 v21, s[60:63], s85 offen nt lds\n" \
  "s_add_u32 m0, s86, 52224\n" \
  "s_mov_b32 s85, 97280\n" \
  "buffer_load_dwordx4 v21, s[60:63], s85 offen nt lds\n" \
  "v_xor_b32 v18, 0x10000, v18\n" \
  "v_xor_b32 v19, 0x10000, v19\n" \
  "v_xor_b32 v22, 0x10000, v22\n" \
  "v_xor_b32 v23, 0x10000, v23\n" \
  "ds_read_b64_tr_b16 a[192:193], v18 offset:0\n" \
  "ds_read_b64_tr_b16 a[194:195], v18 offset:1024\n" \
  "ds_read_b64_tr_b16 a[196:197], v18 offset:8192\n" \
  "ds_read_b64_tr_b16 a[198:199], v18 offset:9216\n" \
  "ds_read_b64_tr_b16 v[208:209], v19 offset:0\n" \
  "ds_read_b64_tr_b16 v[210:211], v19 offset:1024\n" \
  "ds_read_b64_tr_b16 v[212:213], v19 offset:8192\n" \
  "ds_read_b64_tr_b16 v[214:215], v19 offset:9216\n" \
  "ds_read_b64_tr_b16 a[200:201], v18 offset:16384\n" \
  "ds_read_b64_tr_b16 a[202:203], v18 offset:17408\n" \
  "ds_read_b64_tr_b16 a[204:205], v18 offset:24576\n" \
  "ds_read_b64_tr_b16 a[206:207], v18 offset:25600\n" \
  "ds_read_b64_tr_b16 v[216:217], v19 offset:16384\n" \
  "ds_read_b64_tr_b16 v[218:219], v19 offset:17408\n" \
  "ds_read_b64_tr_b16 v[220:221], v19 offset:24576\n" \
  "ds_read_b64_tr_b16 v[222:223], v19 offset:25600\n" \
  "s_waitcnt vmcnt(16) lgkmcnt(0)\n" \
  "s_barrier\n" \
  "s_add_u32 m0, s86, 65536\n" \
  "s_mov_b32 s85, 256\n" \
  "buffer_load_dwordx4 v20, s[60:63], s85 offen nt lds\n" \
  "s_add_u32 m0, s86, 66560\n" \
  "s_mov_b32 s85, 5376\n" \
  "buffer_load_dwordx4 v20, s[60:63], s85 offen nt lds\n" \
  "s_add_u32 m0, s86, 67584\n" \
  "s_mov_b32 s85, 10496\n" \
  "buffer_load_dwordx4 v21, s[60:63], s85 offen nt lds\n" \
  "s_add_u32 m0, s86, 68608\n" \
  "s_mov_b32 s85, 15616\n" \
  "buffer_load_dwordx4 v21, s[60:63], s85 offen nt lds\n" \
  "s_add_u32 m0, s86, 81920\n" \
  "s_mov_b32 s85, 82176\n" \
  "buffer_load_dwordx4 v20, s[60:63], s85 offen nt lds\n" \
  "s_add_u32 m0, s86, 82944\n" \
  "s_mov_b32 s85, 87296\n" \
  "buffer_load_dwordx4 v20, s[60:63], s85 offen nt lds\n" \
  "s_add_u32 m0, s86, 83968\n" \
  "s_mov_b32 s85, 92416\n" \
  "buffer_load_dwordx4 v21, s[60:63], s85 offen nt lds\n" \
  "s_add_u32 m0, s86, 84992\n" \
  "s_mov_b32 s85, 97536\n" \
  "buffer_load_dwordx4 v21, s[60:63], s85 offen nt lds\n" \
  "ds_read_b64_tr_b16 a[208:209], v18 offset:32768\n" \
  "ds_read_b64_tr_b16 a[210:211], v18 offset:33792\n" \
  "ds_read_b64_tr_b16 a[212:213], v18 offset:40960\n" \
  "ds_read_b64_tr_b16 a[214:215], v18 offset:41984\n" \
  "ds_read_b64_tr_b16 v[224:225], v19 offset:32768\n" \
  "ds_read_b64_tr_b16 v[226:227], v19 offset:33792\n" \
  "ds_read_b64_tr_b16 v[228:229], v19 offset:40960\n" \
  "ds_read_b64_tr_b16 v[230:231], v19 offset:41984\n" \
  "ds_read_b64_tr_b16 a[216:217], v18 offset:49152\n" \
  "ds_read_b64_tr_b16 a[218:219], v18 offset:50176\n" \
  "ds_read_b64_tr_b16 a[220:221], v18 offset:57344\n" \
  "ds_read_b64_tr_b16 a[222:223], v18 offset:58368\n" \
  "ds_read_b64_tr_b16 v[232:233], v19 offset:49152\n" \
  "ds_read_b64_tr_b16 v[234:235], v19 offset:50176\n" \
  "ds_read_b64_tr_b16 v[236:237], v19 offset:57344\n" \
  "ds_read_b64_tr_b16 v[238:239], v19 offset:58368\n" \
  "s_waitcnt vmcnt(16) lgkmcnt(0)\n" \
  "s_barrier\n" \
  "s_add_u32 m0, s86, 98304\n" \
  "s_mov_b32 s85, 512\n" \
  "buffer_load_dwordx4 v20, s[60:63], s85 offen nt lds\n" \
  "s_add_u32 m0, s86, 99328\n" \
  "s_mov_b32 s85, 5632\n" \
  "buffer_load_dwordx4 v20, s[60:63], s85 offen nt lds\n" \
  "s_add_u32 m0, s86, 100352\n" \
  "s_mov_b32 s85, 10752\n" \
  "buffer_load_dwordx4 v21, s[60:63], s85 offen nt lds\n" \
  "s_add_u32 m0, s86, 101376\n" \
  "s_mov_b32 s85, 15872\n" \
  "buffer_load_dwordx4 v21, s[60:63], s85 offen nt lds\n" \
  "s_add_u32 m0, s86, 114688\n" \
  "s_mov_b32 s85, 82432\n" \
  "buffer_load_dwordx4 v20, s[60:63], s85 offen nt lds\n" \
  "s_add_u32 m0, s86, 115712\n" \
  "s_mov_b32 s85, 87552\n" \
  "buffer_load_dwordx4 v20, s[60:63], s85 offen nt lds\n" \
  "s_add_u32 m0, s86, 116736\n" \
  "s_mov_b32 s85, 92672\n" \
  "buffer_load_dwordx4 v21, s[60:63], s85 offen nt lds\n" \
  "s_add_u32 m0, s86, 117760\n" \
  "s_mov_b32 s85, 97792\n" \
  "buffer_load_dwordx4 v21, s[60:63], s85 offen nt lds\n" \
  "v_xor_b32 v18, 0x10000, v18\n" \
  "v_xor_b32 v19, 0x10000, v19\n" \
  "v_xor_b32 v22, 0x10000, v22\n" \
  "v_xor_b32 v23, 0x10000, v23\n" \
  "ds_read_b64_tr_b16 a[224:225], v18 offset:0\n" \
  "ds_read_b64_tr_b16 a[226:227], v18 offset:1024\n" \
  "ds_read_b64_tr_b16 a[228:229], v18 offset:8192\n" \
  "ds_read_b64_tr_b16 a[230:231], v18 offset:9216\n" \
  "ds_read_b64_tr_b16 v[240:241], v19 offset:0\n" \
  "ds_read_b64_tr_b16 v[242:243], v19 offset:1024\n" \
  "ds_read_b64_tr_b16 v[244:245], v19 offset:8192\n" \
  "ds_read_b64_tr_b16 v[246:247], v19 offset:9216\n" \
  "ds_read_b64_tr_b16 a[232:233], v18 offset:16384\n" \
  "ds_read_b64_tr_b16 a[234:235], v18 offset:17408\n" \
  "ds_read_b64_tr_b16 a[236:237], v18 offset:24576\n" \
  "ds_read_b64_tr_b16 a[238:239], v18 offset:25600\n" \
  "ds_read_b64_tr_b16 v[248:249], v19 offset:16384\n" \
  "ds_read_b64_tr_b16 v[250:251], v19 offset:17408\n" \
  "ds_read_b64_tr_b16 v[252:253], v19 offset:24576\n" \
  "ds_read_b64_tr_b16 v[254:255], v19 offset:25600\n" \
  "s_waitcnt vmcnt(16) lgkmcnt(0)\n" \
  "s_barrier\n" \
  "s_add_u32 m0, s86, 0\n" \
  "s_mov_b32 s85, 768\n" \
  "buffer_load_dwordx4 v20, s[60:63], s85 offen nt lds\n" \
  "s_add_u32 m0, s86, 1024\n" \
  "s_mov_b32 s85, 5888\n" \
  "buffer_load_dwordx4 v20, s[60:63], s85 offen nt lds\n" \
  "s_add_u32 m0, s86, 2048\n" \
  "s_mov_b32 s85, 11008\n" \
  "buffer_load_dwordx4 v21, s[60:63], s85 offen nt lds\n" \
  "s_add_u32 m0, s86, 3072\n" \
  "s_mov_b32 s85, 16128\n" \
  "buffer_load_dwordx4 v21, s[60:63], s85 offen nt lds\n" \
  "s_add_u32 m0, s86, 16384\n" \
  "s_mov_b32 s85, 82688\n" \
  "buffer_load_dwordx4 v20, s[60:63], s85 offen nt lds\n" \
  "s_add_u32 m0, s86, 17408\n" \
  "s_mov_b32 s85, 87808\n" \
  "buffer_load_dwordx4 v20, s[60:63], s85 offen nt lds\n" \
  "s_add_u32 m0, s86, 18432\n" \
  "s_mov_b32 s85, 92928\n" \
  "buffer_load_dwordx4 v21, s[60:63], s85 offen nt lds\n" \
  "s_add_u32 m0, s86, 19456\n" \
  "s_mov_b32 s85, 98048\n" \
  "buffer_load_dwordx4 v21, s[60:63], s85 offen nt lds\n" \
  "ds_read_b64_tr_b16 a[0:1], v22 offset:32768\n" \
  "ds_read_b64_tr_b16 a[2:3], v22 offset:33792\n" \
  "ds_read_b64_tr_b16 a[4:5], v22 offset:40960\n" \
  "ds_read_b64_tr_b16 a[6:7], v22 offset:41984\n" \
  "ds_read_b64_tr_b16 a[16:17], v23 offset:32768\n" \
  "ds_read_b64_tr_b16 a[18:19], v23 offset:33792\n" \
  "ds_read_b64_tr_b16 a[20:21], v23 offset:40960\n" \
  "ds_read_b64_tr_b16 a[22:23], v23 offset:41984\n" \
  "ds_read_b64_tr_b16 a[8:9], v22 offset:49152\n" \
  "ds_read_b64_tr_b16 a[10:11], v22 offset:50176\n" \
  "ds_read_b64_tr_b16 a[12:13], v22 offset:57344\n" \
  "ds_read_b64_tr_b16 a[14:15], v22 offset:58368\n" \
  "ds_read_b64_tr_b16 a[24:25], v23 offset:49152\n" \
  "ds_read_b64_tr_b16 a[26:27], v23 offset:50176\n" \
  "ds_read_b64_tr_b16 a[28:29], v23 offset:57344\n" \
  "ds_read_b64_tr_b16 a[30:31], v23 offset:58368\n" \
  "s_waitcnt vmcnt(16) lgkmcnt(0)\n" \
  "s_barrier\n" \
  "s_add_u32 m0, s86, 32768\n" \
  "s_mov_b32 s85, 1024\n" \
  "buffer_load_dwordx4 v20, s[60:63], s85 offen nt lds\n" \
  "s_add_u32 m0, s86, 33792\n" \
  "s_mov_b32 s85, 6144\n" \
  "buffer_load_dwordx4 v20, s[60:63], s85 offen nt lds\n" \
  "s_add_u32 m0, s86, 34816\n" \
  "s_mov_b32 s85, 11264\n" \
  "buffer_load_dwordx4 v21, s[60:63], s85 offen nt lds\n" \
  "s_add_u32 m0, s86, 35840\n" \
  "s_mov_b32 s85, 16384\n" \
  "buffer_load_dwordx4 v21, s[60:63], s85 offen nt lds\n" \
  "s_add_u32 m0, s86, 49152\n" \
  "s_mov_b32 s85, 82944\n" \
  "buffer_load_dwordx4 v20, s[60:63], s85 offen nt lds\n" \
  "s_add_u32 m0, s86, 50176\n" \
  "s_mov_b32 s85, 88064\n" \
  "buffer_load_dwordx4 v20, s[60:63], s85 offen nt lds\n" \
  "s_add_u32 m0, s86, 51200\n" \
  "s_mov_b32 s85, 93184\n" \
  "buffer_load_dwordx4 v21, s[60:63], s85 offen nt lds\n" \
  "s_add_u32 m0, s86, 52224\n" \
  "s_mov_b32 s85, 98304\n" \
  "buffer_load_dwordx4 v21, s[60:63], s85 offen nt lds\n" \
  "v_xor_b32 v18, 0x10000, v18\n" \
  "v_xor_b32 v19, 0x10000, v19\n" \
  "v_xor_b32 v22, 0x10000, v22\n" \
  "v_xor_b32 v23, 0x10000, v23\n" \
  "ds_read_b64_tr_b16 a[32:33], v22 offset:0\n" \
  "ds_read_b64_tr_b16 a[34:35], v22 offset:1024\n" \
  "ds_read_b64_tr_b16 a[36:37], v22 offset:8192\n" \
  "ds_read_b64_tr_b16 a[38:39], v22 offset:9216\n" \
  "ds_read_b64_tr_b16 a[48:49], v23 offset:0\n" \
  "ds_read_b64_tr_b16 a[50:51], v23 offset:1024\n" \
  "ds_read_b64_tr_b16 a[52:53], v23 offset:8192\n" \
  "ds_read_b64_tr_b16 a[54:55], v23 offset:9216\n" \
  "ds_read_b64_tr_b16 a[40:41], v22 offset:16384\n" \
  "ds_read_b64_tr_b16 a[42:43], v22 offset:17408\n" \
  "ds_read_b64_tr_b16 a[44:45], v22 offset:24576\n" \
  "ds_read_b64_tr_b16 a[46:47], v22 offset:25600\n" \
  "ds_read_b64_tr_b16 a[56:57], v23 offset:16384\n" \
  "ds_read_b64_tr_b16 a[58:59], v23 offset:17408\n" \
  "ds_read_b64_tr_b16 a[60:61], v23 offset:24576\n" \
  "ds_read_b64_tr_b16 a[62:63], v23 offset:25600\n" \
  "s_waitcnt vmcnt(16) lgkmcnt(0)\n" \
  "s_barrier\n" \
  "ds_read_b64_tr_b16 a[64:65], v22 offset:32768\n" \
  "ds_read_b64_tr_b16 a[66:67], v22 offset:33792\n" \
  "ds_read_b64_tr_b16 a[68:69], v22 offset:40960\n" \
  "ds_read_b64_tr_b16 a[70:71], v22 offset:41984\n" \
  "ds_read_b64_tr_b16 a[80:81], v23 offset:32768\n" \
  "ds_read_b64_tr_b16 a[82:83], v23 offset:33792\n" \
  "ds_read_b64_tr_b16 a[84:85], v23 offset:40960\n" \
  "ds_read_b64_tr_b16 a[86:87], v23 offset:41984\n" \
  "ds_read_b64_tr_b16 a[72:73], v22 offset:49152\n" \
  "ds_read_b64_tr_b16 a[74:75], v22 offset:50176\n" \
  "ds_read_b64_tr_b16 a[76:77], v22 offset:57344\n" \
  "ds_read_b64_tr_b16 a[78:79], v22 offset:58368\n" \
  "ds_read_b64_tr_b16 a[88:89], v23 offset:49152\n" \
  "ds_read_b64_tr_b16 a[90:91], v23 offset:50176\n" \
  "ds_read_b64_tr_b16 a[92:93], v23 offset:57344\n" \
  "ds_read_b64_tr_b16 a[94:95], v23 offset:58368\n" \
  "s_waitcnt vmcnt(8) lgkmcnt(0)\n" \
  "s_barrier\n" \
  "v_xor_b32 v18, 0x10000, v18\n" \
  "v_xor_b32 v19, 0x10000, v19\n" \
  "v_xor_b32 v22, 0x10000, v22\n" \
  "v_xor_b32 v23, 0x10000, v23\n" \
  "ds_read_b64_tr_b16 a[96:97], v22 offset:0\n" \
  "ds_read_b64_tr_b16 a[98:99], v22 offset:1024\n" \
  "ds_read_b64_tr_b16 a[100:101], v22 offset:8192\n" \
  "ds_read_b64_tr_b16 a[102:103], v22 offset:9216\n" \
  "ds_read_b64_tr_b16 a[112:113], v23 offset:0\n" \
  "ds_read_b64_tr_b16 a[114:115], v23 offset:1024\n" \
  "ds_read_b64_tr_b16 a[116:117], v23 offset:8192\n" \
  "ds_read_b64_tr_b16 a[118:119], v23 offset:9216\n" \
  "ds_read_b64_tr_b16 a[104:105], v22 offset:16384\n" \
  "ds_read_b64_tr_b16 a[106:107], v22 offset:17408\n" \
  "ds_read_b64_tr_b16 a[108:109], v22 offset:24576\n" \
  "ds_read_b64_tr_b16 a[110:111], v22 offset:25600\n" \
  "ds_read_b64_tr_b16 a[120:121], v23 offset:16384\n" \
  "ds_read_b64_tr_b16 a[122:123], v23 offset:17408\n" \
  "ds_read_b64_tr_b16 a[124:125], v23 offset:24576\n" \
  "ds_read_b64_tr_b16 a[126:127], v23 offset:25600\n" \
  "s_waitcnt vmcnt(0) lgkmcnt(0)\n" \
  "s_barrier\n" \
  "ds_read_b64_tr_b16 a[128:129], v22 offset:32768\n" \
  "ds_read_b64_tr_b16 a[130:131], v22 offset:33792\n" \
  "ds_read_b64_tr_b16 a[132:133], v22 offset:40960\n" \
  "ds_read_b64_tr_b16 a[134:135], v22 offset:41984\n" \
  "ds_read_b64_tr_b16 a[144:145], v23 offset:32768\n" \
  "ds_read_b64_tr_b16 a[146:147], v23 offset:33792\n" \
  "ds_read_b64_tr_b16 a[148:149], v23 offset:40960\n" \
  "ds_read_b64_tr_b16 a[150:151], v23 offset:41984\n" \
  "ds_read_b64_tr_b16 a[136:137], v22 offset:49152\n" \
  "ds_read_b64_tr_b16 a[138:139], v22 offset:50176\n" \
  "ds_read_b64_tr_b16 a[140:141], v22 offset:57344\n" \
  "ds_read_b64_tr_b16 a[142:143], v22 offset:58368\n" \
  "ds_read_b64_tr_b16 a[152:153], v23 offset:49152\n" \
  "ds_read_b64_tr_b16 a[154:155], v23 offset:50176\n" \
  "ds_read_b64_tr_b16 a[156:157], v23 offset:57344\n" \
  "ds_read_b64_tr_b16 a[158:159], v23 offset:58368\n" \
  "s_waitcnt lgkmcnt(0)\n" \
  "s_barrier\n" \
  "s_mov_b32 s80, 0\n" \
  "s_cmp_lg_u32 s64, 0\n" \
  "s_cbranch_scc1 LY%=\n" \
  "s_mov_b32 s55, 0x40000\n" \
  "s_mov_b32 s88, 119808\n" \
  "s_mov_b64 exec, 1\n" \
  "buffer_load_dwordx2 v[188:189], off, s[36:39], s88 sc1\n" \
  "IW%=:\n" \
  "buffer_load_dwordx2 v[190:191], off, s[36:39], s65 offset:8 sc1\n" \
  "s_sub_u32 s55, s55, 1\n" \
  "s_cbranch_scc1 IX%=\n" \
  "s_waitcnt vmcnt(0)\n" \
  "v_readfirstlane_b32 s85, v188\n" \
  "v_readfirstlane_b32 s86, v189\n" \
  "v_readfirstlane_b32 s10, v190\n" \
  "v_readfirstlane_b32 s11, v191\n" \
  "s_mul_i32 s12, s85, 0x9e3779b1\n" \
  "s_add_u32 s12, s12, 0x7f4a7c15\n" \
  "s_mul_i32 s13, s86, 0x85ebca6b\n" \
  "s_add_u32 s13, s13, 0xc2b2ae35\n" \
  "s_xor_b64 s[10:11], s[10:11], s[12:13]\n" \
  "s_lshr_b32 s12, s11, 1\n" \
  "s_or_b32 s12, s12, s10\n" \
  "s_cbranch_scc0 IX%=\n" \
  "s_sleep 3\n" \
  "s_branch IW%=\n" \
  "IX%=:\n" \
  "s_waitcnt vmcnt(0)\n" \
  "s_mov_b64 exec, -1\n" \
  "s_mov_b32 s55, 0x10000\n" \
  "LY%=:\n" \
  "s_barrier\n" \
  "s_mul_i32 s78, s64, 10240\n" \
  "s_add_u32 s78, s78, 32768\n" \
  "s_mov_b32 s79, 0\n" \
  "s_add_u32 m0, s78, 0\n" \
  "s_add_u32 s87, s48, 0\n" \
  "buffer_load_dwordx4 v0, s[44:47], s87 offen sc1 lds\n" \
  "buffer_load_dwordx4 v0, s[44:47], s87 offen offset:1024 sc1 lds\n" \
  "buffer_load_dwordx4 v0, s[44:47], s87 offen offset:2048 sc1 lds\n" \
  "buffer_load_dwordx4 v0, s[44:47], s87 offen offset:3072 sc1 lds\n" \
  "s_add_u32 m0, s78, 4096\n" \
  "s_add_u32 s87, s48, 4096\n" \
  "buffer_load_dwordx4 v0, s[44:47], s87 offen sc1 lds\n" \
  "buffer_load_dwordx4 v0, s[44:47], s87 offen offset:1024 sc1 lds\n" \
  "buffer_load_dwordx4 v0, s[44:47], s87 offen offset:2048 sc1 lds\n" \
  "buffer_load_dwordx4 v0, s[44:47], s87 offen offset:3072 sc1 lds\n" \
  "s_add_u32 m0, s78, 8192\n" \
  "s_add_u32 s87, s48, 8192\n" \
  "buffer_load_dwordx4 v0, s[44:47], s87 offen sc1 lds\n" \
  "buffer_load_dwordx4 v0, s[44:47], s87 offen offset:1024 sc1 lds\n" \
  "s_add_u32 s48, s48, 40960\n" \
  "s_add_u32 s78, s78, 40960\n" \
  "s_add_u32 m0, s78, 0\n" \
  "s_add_u32 s87, s48, 0\n" \
  "buffer_load_dwordx4 v0, s[44:47], s87 offen sc1 lds\n" \
  "buffer_load_dwordx4 v0, s[44:47], s87 offen offset:1024 sc1 lds\n" \
  "buffer_load_dwordx4 v0, s[44:47], s87 offen offset:2048 sc1 lds\n" \
  "buffer_load_dwordx4 v0, s[44:47], s87 offen offset:3072 sc1 lds\n" \
  "s_add_u32 m0, s78, 4096\n" \
  "s_add_u32 s87, s48, 4096\n" \
  "buffer_load_dwordx4 v0, s[44:47], s87 offen sc1 lds\n" \
  "buffer_load_dwordx4 v0, s[44:47], s87 offen offset:1024 sc1 lds\n" \
  "buffer_load_dwordx4 v0, s[44:47], s87 offen offset:2048 sc1 lds\n" \
  "buffer_load_dwordx4 v0, s[44:47], s87 offen offset:3072 sc1 lds\n" \
  "s_add_u32 m0, s78, 8192\n" \
  "s_add_u32 s87, s48, 8192\n" \
  "buffer_load_dwordx4 v0, s[44:47], s87 offen sc1 lds\n" \
  "buffer_load_dwordx4 v0, s[44:47], s87 offen offset:1024 sc1 lds\n" \
  "s_add_u32 s48, s48, 40960\n" \
  "s_add_u32 s78, s78, 40960\n" \
  "s_waitcnt vmcnt(10)\n" \
  "s_barrier\n" \
  "ds_read_b128 v[44:47], v1 offset:0\n" \
  "ds_read_b128 v[48:51], v1 offset:1024\n" \
  "ds_read_b128 v[52:55], v1 offset:2048\n" \
  "ds_read_b128 v[56:59], v1 offset:3072\n" \
  "ds_read_b128 v[60:63], v1 offset:4096\n" \
  "ds_read_b128 v[64:67], v1 offset:5120\n" \
  "s_waitcnt lgkmcnt(5)\n" \
  "v_mfma_f32_16x16x32_bf16 v[68:71], a[160:163], v[44:47], 0\n" \
  "ds_read_b128 v[72:75], v1 offset:6144\n" \
  "v_mfma_f32_16x16x32_bf16 v[76:79], a[240:243], v[44:47], 0\n" \
  "s_waitcnt lgkmcnt(5)\n" \
  "v_mfma_f32_16x16x32_bf16 v[68:71], a[164:167], v[48:51], v[68:71]\n" \
  "ds_read_b128 v[44:47], v1 offset:7168\n" \
  "s_add_u32 m0, s78, 0\n" \
  "s_add_u32 s87, s48, 0\n" \
  "buffer_load_dwordx4 v0, s[44:47], s87 offen sc1 lds\n" \
  "v_mfma_f32_16x16x32_bf16 v[76:79], a[244:247], v[48:51], v[76:79]\n" \
  "s_waitcnt lgkmcnt(5)\n" \
  "v_mfma_f32_16x16x32_bf16 v[68:71], a[168:171], v[52:55], v[68:71]\n" \
  "ds_read_b128 v[48:51], v1 offset:8192\n" \
  "v_mfma_f32_16x16x32_bf16 v[76:79], a[248:251], v[52:55], v[76:79]\n" \
  "buffer_load_dwordx4 v0, s[44:47], s87 offen offset:1024 sc1 lds\n" \
  "s_waitcnt lgkmcnt(5)\n" \
  "v_mfma_f32_16x16x32_bf16 v[68:71], a[172:175], v[56:59], v[68:71]\n" \
  "ds_read_b128 v[52:55], v1 offset:9216\n" \
  "v_mfma_f32_16x16x32_bf16 v[76:79], a[252:255], v[56:59], v[76:79]\n" \
  "s_waitcnt lgkmcnt(5)\n" \
  "v_mfma_f32_16x16x32_bf16 v[68:71], a[176:179], v[60:63], v[68:71]\n" \
  "ds_read_b128 v[56:59], v1 offset:10240\n" \
  "buffer_load_dwordx4 v0, s[44:47], s87 offen offset:2048 sc1 lds\n" \
  "v_mfma_f32_16x16x32_bf16 v[76:79], v[192:195], v[60:63], v[76:79]\n" \
  "s_waitcnt lgkmcnt(5)\n" \
  "v_mfma_f32_16x16x32_bf16 v[68:71], a[180:183], v[64:67], v[68:71]\n" \
  "ds_read_b128 v[60:63], v1 offset:11264\n" \
  "v_mfma_f32_16x16x32_bf16 v[76:79], v[196:199], v[64:67], v[76:79]\n" \
  "buffer_load_dwordx4 v0, s[44:47], s87 offen offset:3072 sc1 lds\n" \
  "s_waitcnt lgkmcnt(5)\n" \
  "v_mfma_f32_16x16x32_bf16 v[68:71], a[184:187], v[72:75], v[68:71]\n" \
  "ds_read_b128 v[64:67], v1 offset:12288\n" \
  "v_mfma_f32_16x16x32_bf16 v[76:79], v[200:203], v[72:75], v[76:79]\n" \
  "s_waitcnt lgkmcnt(5)\n" \
  "v_mfma_f32_16x16x32_bf16 v[68:71], a[188:191], v[44:47], v[68:71]\n" \
  "ds_read_b128 v[72:75], v1 offset:13312\n" \
  "s_add_u32 m0, s78, 4096\n" \
  "s_add_u32 s87, s48, 4096\n" \
  "buffer_load_dwordx4 v0, s[44:47], s87 offen sc1 lds\n" \
  "v_mfma_f32_16x16x32_bf16 v[76:79], v[204:207], v[44:47], v[76:79]\n" \
  "s_waitcnt lgkmcnt(5)\n" \
  "v_mfma_f32_16x16x32_bf16 v[68:71], a[192:195], v[48:51], v[68:71]\n" \
  "ds_read_b128 v[44:47], v1 offset:14336\n" \
  "v_mfma_f32_16x16x32_bf16 v[76:79], v[208:211], v[48:51], v[76:79]\n" \
  "buffer_load_dwordx4 v0, s[44:47], s87 offen offset:1024 sc1 lds\n" \
  "s_waitcnt lgkmcnt(5)\n" \
  "v_mfma_f32_16x16x32_bf16 v[68:71], a[196:199], v[52:55], v[68:71]\n" \
  "ds_read_b128 v[48:51], v1 offset:15360\n" \
  "v_mfma_f32_16x16x32_bf16 v[76:79], v[212:215], v[52:55], v[76:79]\n" \
  "s_waitcnt lgkmcnt(5)\n" \
  "v_mfma_f32_16x16x32_bf16 v[68:71], a[200:203], v[56:59], v[68:71]\n" \
  "ds_read_b128 v[52:55], v1 offset:16384\n" \
  "buffer_load_dwordx4 v0, s[44:47], s87 offen offset:2048 sc1 lds\n" \
  "v_mfma_f32_16x16x32_bf16 v[76:79], v[216:219], v[56:59], v[76:79]\n" \
  "s_waitcnt lgkmcnt(5)\n" \
  "v_mfma_f32_16x16x32_bf16 v[68:71], a[204:207], v[60:63], v[68:71]\n" \
  "ds_read_b128 v[56:59], v1 offset:17408\n" \
  "v_mfma_f32_16x16x32_bf16 v[76:79], v[220:223], v[60:63], v[76:79]\n" \
  "buffer_load_dwordx4 v0, s[44:47], s87 offen offset:3072 sc1 lds\n" \
  "s_waitcnt lgkmcnt(5)\n" \
  "v_mfma_f32_16x16x32_bf16 v[68:71], a[208:211], v[64:67], v[68:71]\n" \
  "ds_read_b128 v[60:63], v1 offset:18432\n" \
  "v_mfma_f32_16x16x32_bf16 v[76:79], v[224:227], v[64:67], v[76:79]\n" \
  "s_waitcnt lgkmcnt(5)\n" \
  "v_mfma_f32_16x16x32_bf16 v[68:71], a[212:215], v[72:75], v[68:71]\n" \
  "ds_read_b128 v[64:67], v1 offset:19456\n" \
  "s_add_u32 m0, s78, 8192\n" \
  "s_add_u32 s87, s48, 8192\n" \
  "buffer_load_dwordx4 v0, s[44:47], s87 offen sc1 lds\n" \
  "v_mfma_f32_16x16x32_bf16 v[76:79], v[228:231], v[72:75], v[76:79]\n" \
  "s_waitcnt lgkmcnt(5)\n" \
  "v_mfma_f32_16x16x32_bf16 v[68:71], a[216:219], v[44:47], v[68:71]\n" \
  "ds_read_b128 v[72:75], v1 offset:20480\n" \
  "v_mfma_f32_16x16x32_bf16 v[76:79], v[232:235], v[44:47], v[76:79]\n" \
  "buffer_load_dwordx4 v0, s[44:47], s87 offen offset:1024 sc1 lds\n" \
  "s_waitcnt lgkmcnt(5)\n" \
  "v_mfma_f32_16x16x32_bf16 v[68:71], a[220:223], v[48:51], v[68:71]\n" \
  "ds_read_b128 v[44:47], v1 offset:21504\n" \
  "v_mfma_f32_16x16x32_bf16 v[76:79], v[236:239], v[48:51], v[76:79]\n" \
  "s_waitcnt lgkmcnt(5)\n" \
  "v_mfma_f32_16x16x32_bf16 v[68:71], a[224:227], v[52:55], v[68:71]\n" \
  "ds_read_b128 v[48:51], v1 offset:22528\n" \
  "v_mfma_f32_16x16x32_bf16 v[76:79], v[240:243], v[52:55], v[76:79]\n" \
  "s_waitcnt lgkmcnt(5)\n" \
  "v_mfma_f32_16x16x32_bf16 v[68:71], a[228:231], v[56:59], v[68:71]\n" \
  "ds_read_b128 v[52:55], v1 offset:23552\n" \
  "v_mfma_f32_16x16x32_bf16 v[76:79], v[244:247], v[56:59], v[76:79]\n" \
  "s_waitcnt lgkmcnt(5)\n" \
  "v_mfma_f32_16x16x32_bf16 v[68:71], a[232:235], v[60:63], v[68:71]\n" \
  "ds_read_b128 v[56:59], v1 offset:24576\n" \
  "v_mfma_f32_16x16x32_bf16 v[76:79], v[248:251], v[60:63], v[76:79]\n" \
  "s_waitcnt lgkmcnt(5)\n" \
  "v_mfma_f32_16x16x32_bf16 v[68:71], a[236:239], v[64:67], v[68:71]\n" \
  "ds_read_b128 v[60:63], v1 offset:25600\n" \
  "v_mfma_f32_16x16x32_bf16 v[76:79], v[252:255], v[64:67], v[76:79]\n" \
  "s_waitcnt lgkmcnt(5)\n" \
  "v_mfma_f32_16x16x32_bf16 v[64:67], a[160:163], v[72:75], 0\n" \
  "ds_read_b128 v[80:83], v1 offset:26624\n" \
  "v_mfma_f32_16x16x32_bf16 v[84:87], a[240:243], v[72:75], 0\n" \
  "s_waitcnt lgkmcnt(5)\n" \
  "v_mfma_f32_16x16x32_bf16 v[64:67], a[164:167], v[44:47], v[64:67]\n" \
  "ds_read_b128 v[72:75], v1 offset:27648\n" \
  "v_mfma_f32_16x16x32_bf16 v[84:87], a[244:247], v[44:47], v[84:87]\n" \
  "s_waitcnt lgkmcnt(5)\n" \
  "v_mfma_f32_16x16x32_bf16 v[64:67], a[168:171], v[48:51], v[64:67]\n" \
  "ds_read_b128 v[44:47], v1 offset:28672\n" \
  "v_mfma_f32_16x16x32_bf16 v[84:87], a[248:251], v[48:51], v[84:87]\n" \
  "s_waitcnt lgkmcnt(5)\n" \
  "v_mfma_f32_16x16x32_bf16 v[64:67], a[172:175], v[52:55], v[64:67]\n" \
  "ds_read_b128 v[48:51], v1 offset:29696\n" \
  "v_mfma_f32_16x16x32_bf16 v[84:87], a[252:255], v[52:55], v[84:87]\n" \
  "s_waitcnt lgkmcnt(5)\n" \
  "v_mfma_f32_16x16x32_bf16 v[64:67], a[176:179], v[56:59], v[64:67]\n" \
  "ds_read_b128 v[52:55], v1 offset:30720\n" \
  "v_mfma_f32_16x16x32_bf16 v[84:87], v[192:195], v[56:59], v[84:87]\n" \
  "v_cvt_pk_bf16_f32 v56, v68, v69\n" \
  "s_waitcnt lgkmcnt(5)\n" \
  "v_mfma_f32_16x16x32_bf16 v[64:67], a[180:183], v[60:63], v[64:67]\n" \
  "ds_read_b128 v[88:91], v1 offset:31744\n" \
  "v_mfma_f32_16x16x32_bf16 v[84:87], v[196:199], v[60:63], v[84:87]\n" \
  "v_cvt_pk_bf16_f32 v57, v70, v71\n" \
  "s_waitcnt lgkmcnt(5)\n" \
  "v_mfma_f32_16x16x32_bf16 v[64:67], a[184:187], v[80:83], v[64:67]\n" \
  "ds_read_b128 v[60:63], v1 offset:32768\n" \
  "v_mfma_f32_16x16x32_bf16 v[84:87], v[200:203], v[80:83], v[84:87]\n" \
  "v_cvt_pk_bf16_f32 v58, v76, v77\n" \
  "s_waitcnt lgkmcnt(5)\n" \
  "v_mfma_f32_16x16x32_bf16 v[64:67], a[188:191], v[72:75], v[64:67]\n" \
  "ds_read_b128 v[68:71], v1 offset:33792\n" \
  "v_mfma_f32_16x16x32_bf16 v[84:87], v[204:207], v[72:75], v[84:87]\n" \
  "v_cvt_pk_bf16_f32 v59, v78, v79\n" \
  "s_waitcnt lgkmcnt(5)\n" \
  "v_mfma_f32_16x16x32_bf16 v[64:67], a[192:195], v[44:47], v[64:67]\n" \
  "ds_read_b128 v[72:75], v1 offset:34816\n" \
  "v_mfma_f32_16x16x32_bf16 v[84:87], v[208:211], v[44:47], v[84:87]\n" \
  "ds_write_b128 v2, v[56:59] offset:0\n" \
  "s_waitcnt lgkmcnt(6)\n" \
  "v_mfma_f32_16x16x32_bf16 v[64:67], a[196:199], v[48:51], v[64:67]\n" \
  "ds_read_b128 v[44:47], v1 offset:35840\n" \
  "v_mfma_f32_16x16x32_bf16 v[84:87], v[212:215], v[48:51], v[84:87]\n" \
  "s_waitcnt lgkmcnt(6)\n" \
  "v_mfma_f32_16x16x32_bf16 v[64:67], a[200:203], v[52:55], v[64:67]\n" \
  "ds_read_b128 v[48:51], v1 offset:36864\n" \
  "v_mfma_f32_16x16x32_bf16 v[84:87], v[216:219], v[52:55], v[84:87]\n" \
  "s_waitcnt lgkmcnt(6)\n" \
  "v_mfma_f32_16x16x32_bf16 v[64:67], a[204:207], v[88:91], v[64:67]\n" \
  "ds_read_b128 v[52:55], v1 offset:37888\n" \
  "v_mfma_f32_16x16x32_bf16 v[84:87], v[220:223], v[88:91], v[84:87]\n" \
  "s_waitcnt lgkmcnt(6)\n" \
  "v_mfma_f32_16x16x32_bf16 v[64:67], a[208:211], v[60:63], v[64:67]\n" \
  "ds_read_b128 v[56:59], v1 offset:38912\n" \
  "v_mfma_f32_16x16x32_bf16 v[84:87], v[224:227], v[60:63], v[84:87]\n" \
  "s_waitcnt lgkmcnt(6)\n" \
  "v_mfma_f32_16x16x32_bf16 v[64:67], a[212:215], v[68:71], v[64:67]\n" \
  "ds_read_b128 v[60:63], v1 offset:39936\n" \
  "v_mfma_f32_16x16x32_bf16 v[84:87], v[228:231], v[68:71], v[84:87]\n" \
  "s_waitcnt lgkmcnt(6)\n" \
  "v_mfma_f32_16x16x32_bf16 v[64:67], a[216:219], v[72:75], v[64:67]\n" \
  "v_mfma_f32_16x16x32_bf16 v[84:87], v[232:235], v[72:75], v[84:87]\n" \
  "s_waitcnt lgkmcnt(4)\n" \
  "v_mfma_f32_16x16x32_bf16 v[64:67], a[220:223], v[44:47], v[64:67]\n" \
  "v_mfma_f32_16x16x32_bf16 v[84:87], v[236:239], v[44:47], v[84:87]\n" \
  "s_waitcnt lgkmcnt(3)\n" \
  "v_mfma_f32_16x16x32_bf16 v[64:67], a[224:227], v[48:51], v[64:67]\n" \
  "v_mfma_f32_16x16x32_bf16 v[84:87], v[240:243], v[48:51], v[84:87]\n" \
  "s_waitcnt lgkmcnt(2)\n" \
  "v_mfma_f32_16x16x32_bf16 v[64:67], a[228:231], v[52:55], v[64:67]\n" \
  "v_mfma_f32_16x16x32_bf16 v[84:87], v[244:247], v[52:55], v[84:87]\n" \
  "s_waitcnt lgkmcnt(1)\n" \
  "v_mfma_f32_16x16x32_bf16 v[64:67], a[232:235], v[56:59], v[64:67]\n" \
  "v_mfma_f32_16x16x32_bf16 v[84:87], v[248:251], v[56:59], v[84:87]\n" \
  "s_waitcnt lgkmcnt(0)\n" \
  "v_mfma_f32_16x16x32_bf16 v[64:67], a[236:239], v[60:63], v[64:67]\n" \
  "v_mfma_f32_16x16x32_bf16 v[84:87], v[252:255], v[60:63], v[84:87]\n" \
  "s_nop 6\n" \
  "v_cvt_pk_bf16_f32 v44, v64, v65\n" \
  "v_cvt_pk_bf16_f32 v45, v66, v67\n" \
  "v_cvt_pk_bf16_f32 v46, v84, v85\n" \
  "v_cvt_pk_bf16_f32 v47, v86, v87\n" \
  "ds_write_b128 v2, v[44:47] offset:4096\n" \
  "s_add_u32 s78, s78, 40960\n" \
  "v_add_u32 v1, 0xa000, v1\n" \
  "s_add_u32 s79, s79, 1\n" \
  "s_cmp_eq_u32 s79, 1\n" \
  "s_cselect_b32 s85, 122880, 0\n" \
  "s_sub_u32 s78, s78, s85\n" \
  "s_cmp_eq_u32 s79, 3\n" \
  "s_cselect_b32 s85, 122880, 0\n" \
  "s_cselect_b32 s79, 0, s79\n" \
  "v_subrev_u32 v1, s85, v1\n" \
  "s_add_u32 s48, s48, 40960\n" \
  "v_xor_b32 v2, 0x2000, v2\n" \
  "s_mov_b32 s30, 0x80000000\n" \
  "s_waitcnt vmcnt(10) lgkmcnt(0)\n" \
  "s_barrier\n" \
  "ds_read_b128 v[16:19], v1 offset:0\n" \
  "ds_read_b128 v[20:23], v1 offset:1024\n" \
  "ds_read_b128 v[24:27], v1 offset:2048\n" \
  "ds_read_b128 v[28:31], v1 offset:3072\n" \
  "ds_read_b128 v[32:35], v1 offset:4096\n" \
  "s_waitcnt lgkmcnt(0)\n" \
  "s_memtime s[20:21]\n" \
  "s_memrealtime s[22:23]\n" \
  "s_mov_b32 s24, 0\n" \
  "s_waitcnt lgkmcnt(0)\n" \
  "LOOP%=:\n" \
  "v_mfma_f32_16x16x32_bf16 v[44:47], a[160:163], v[16:19], 0\n" \
  "ds_read_b128 v[48:51], v1 offset:5120\n" \
  "v_mfma_f32_16x16x32_bf16 v[52:55], a[240:243], v[16:19], 0\n" \
  "ds_read_b128 v[56:59], v3 offset:0\n" \
  "ds_read_b128 v[60:63], v3 offset:4096\n" \
  "v_mfma_f32_16x16x32_bf16 v[44:47], a[164:167], v[20:23], v[44:47]\n" \
  "ds_read_b128 v[16:19], v1 offset:6144\n" \
  "v_mfma_f32_16x16x32_bf16 v[52:55], a[244:247], v[20:23], v[52:55]\n" \
  "ds_read_b128 v[64:67], v3 offset:1024\n" \
  "ds_read_b128 v[68:71], v3 offset:5120\n" \
  "v_mfma_f32_16x16x32_bf16 v[44:47], a[168:171], v[24:27], v[44:47]\n" \
  "ds_read_b128 v[20:23], v1 offset:7168\n" \
  "v_mfma_f32_16x16x32_bf16 v[52:55], a[248:251], v[24:27], v[52:55]\n" \
  "ds_read_b128 v[72:75], v3 offset:2048\n" \
  "ds_read_b128 v[76:79], v3 offset:6144\n" \
  "v_mfma_f32_16x16x32_bf16 v[44:47], a[172:175], v[28:31], v[44:47]\n" \
  "ds_read_b128 v[24:27], v1 offset:8192\n" \
  "v_mfma_f32_16x16x32_bf16 v[52:55], a[252:255], v[28:31], v[52:55]\n" \
  "ds_read_b128 v[80:83], v3 offset:3072\n" \
  "ds_read_b128 v[84:87], v3 offset:7168\n" \
  "v_mfma_f32_16x16x32_bf16 v[44:47], a[176:179], v[32:35], v[44:47]\n" \
  "ds_read_b128 v[28:31], v1 offset:9216\n" \
  "v_mfma_f32_16x16x32_bf16 v[52:55], v[192:195], v[32:35], v[52:55]\n" \
  "buffer_load_dword v12, v9, s[36:39], 0 offen sc1\n" \
  "s_waitcnt lgkmcnt(11)\n" \
  "v_mfma_f32_16x16x32_bf16 v[32:35], v[56:59], a[0:3], 0\n" \
  "v_cvt_pk_bf16_f32 v88, v36, v37\n" \
  "v_cvt_pk_bf16_f32 v89, v38, v39\n" \
  "s_waitcnt lgkmcnt(10)\n" \
  "v_mfma_f32_16x16x32_bf16 v[92:95], v[60:63], a[0:3], 0\n" \
  "v_cvt_pk_bf16_f32 v90, v40, v41\n" \
  "v_cvt_pk_bf16_f32 v91, v42, v43\n" \
  "v_mfma_f32_16x16x32_bf16 v[44:47], a[180:183], v[48:51], v[44:47]\n" \
  "ds_read_b128 v[96:99], v1 offset:10240\n" \
  "v_mfma_f32_16x16x32_bf16 v[52:55], v[196:199], v[48:51], v[52:55]\n" \
  "buffer_store_dwordx4 v[88:91], v0, s[44:47], s30 offen sc1\n" \
  "s_waitcnt lgkmcnt(9)\n" \
  "v_mfma_f32_16x16x32_bf16 v[32:35], v[64:67], a[4:7], v[32:35]\n" \
  "s_cmp_lg_u32 s80, 12\n" \
  "s_cbranch_scc1 MY%=\n" \
  "s_mov_b32 s55, 0x10000\n" \
  "s_mov_b64 exec, 1\n" \
  "MW%=:\n" \
  "buffer_load_dword v11, off, s[36:39], s65 offset:2048 sc1\n" \
  "s_sub_u32 s55, s55, 1\n" \
  "s_cbranch_scc1 MX%=\n" \
  "s_waitcnt vmcnt(0)\n" \
  "v_readfirstlane_b32 s29, v11\n" \
  "s_cmp_lg_u32 s29, -1\n" \
  "s_cbranch_scc1 MX%=\n" \
  "s_sleep 3\n" \
  "s_branch MW%=\n" \
  "MX%=:\n" \
  "s_waitcnt vmcnt(0)\n" \
  "s_mov_b64 exec, -1\n" \
  "s_mov_b32 s55, 0x10000\n" \
  "MY%=:\n" \
  "s_waitcnt lgkmcnt(8)\n" \
  "v_mfma_f32_16x16x32_bf16 v[92:95], v[68:71], a[4:7], v[92:95]\n" \
  "s_add_u32 m0, s78, 0\n" \
  "s_add_u32 s87, s48, 0\n" \
  "buffer_load_dwordx4 v0, s[44:47], s87 offen sc1 lds\n" \
  "v_mfma_f32_16x16x32_bf16 v[44:47], a[184:187], v[16:19], v[44:47]\n" \
  "ds_read_b128 v[36:39], v1 offset:11264\n" \
  "v_mfma_f32_16x16x32_bf16 v[52:55], v[200:203], v[16:19], v[52:55]\n" \
  "s_waitcnt lgkmcnt(7)\n" \
  "v_mfma_f32_16x16x32_bf16 v[32:35], v[72:75], a[8:11], v[32:35]\n" \
  "s_waitcnt lgkmcnt(6)\n" \
  "v_mfma_f32_16x16x32_bf16 v[92:95], v[76:79], a[8:11], v[92:95]\n" \
  "v_mfma_f32_16x16x32_bf16 v[44:47], a[188:191], v[20:23], v[44:47]\n" \
  "ds_read_b128 v[16:19], v1 offset:12288\n" \
  "v_mfma_f32_16x16x32_bf16 v[52:55], v[204:207], v[20:23], v[52:55]\n" \
  "s_waitcnt lgkmcnt(5)\n" \
  "v_mfma_f32_16x16x32_bf16 v[32:35], v[80:83], a[12:15], v[32:35]\n" \
  "s_waitcnt lgkmcnt(4)\n" \
  "v_mfma_f32_16x16x32_bf16 v[92:95], v[84:87], a[12:15], v[92:95]\n" \
  "v_mfma_f32_16x16x32_bf16 v[44:47], a[192:195], v[24:27], v[44:47]\n" \
  "ds_read_b128 v[20:23], v1 offset:13312\n" \
  "v_mfma_f32_16x16x32_bf16 v[52:55], v[208:211], v[24:27], v[52:55]\n" \
  "buffer_load_dwordx4 v0, s[44:47], s87 offen offset:1024 sc1 lds\n" \
  "s_waitcnt lgkmcnt(4)\n" \
  "v_mfma_f32_16x16x32_bf16 v[44:47], a[196:199], v[28:31], v[44:47]\n" \
  "ds_read_b128 v[24:27], v1 offset:14336\n" \
  "v_mfma_f32_16x16x32_bf16 v[52:55], v[212:215], v[28:31], v[52:55]\n" \
  "v_mfma_f32_16x16x32_bf16 v[28:31], v[56:59], a[16:19], 0\n" \
  "v_mfma_f32_16x16x32_bf16 v[40:43], v[60:63], a[16:19], 0\n" \
  "s_waitcnt lgkmcnt(4)\n" \
  "v_mfma_f32_16x16x32_bf16 v[44:47], a[200:203], v[96:99], v[44:47]\n" \
  "ds_read_b128 v[48:51], v1 offset:15360\n" \
  "v_mfma_f32_16x16x32_bf16 v[52:55], v[216:219], v[96:99], v[52:55]\n" \
  "v_mfma_f32_16x16x32_bf16 v[28:31], v[64:67], a[20:23], v[28:31]\n" \
  "v_cvt_pk_bf16_f32 v88, v32, v33\n" \
  "v_cvt_pk_bf16_f32 v89, v34, v35\n" \
  "v_mfma_f32_16x16x32_bf16 v[40:43], v[68:71], a[20:23], v[40:43]\n" \
  "v_cvt_pk_bf16_f32 v90, v92, v93\n" \
  "v_cvt_pk_bf16_f32 v91, v94, v95\n" \
  "s_waitcnt lgkmcnt(4)\n" \
  "v_mfma_f32_16x16x32_bf16 v[44:47], a[204:207], v[36:39], v[44:47]\n" \
  "ds_read_b128 v[32:35], v1 offset:16384\n" \
  "v_mfma_f32_16x16x32_bf16 v[52:55], v[220:223], v[36:39], v[52:55]\n" \
  "s_add_u32 s86, s49, 0\n" \
  "buffer_store_dwordx4 v[88:91], v0, s[44:47], s86 offen sc1\n" \
  "buffer_load_dwordx4 v0, s[44:47], s87 offen offset:2048 sc1 lds\n" \
  "v_mfma_f32_16x16x32_bf16 v[28:31], v[72:75], a[24:27], v[28:31]\n" \
  "v_mfma_f32_16x16x32_bf16 v[40:43], v[76:79], a[24:27], v[40:43]\n" \
  "s_waitcnt lgkmcnt(4)\n" \
  "v_mfma_f32_16x16x32_bf16 v[44:47], a[208:211], v[16:19], v[44:47]\n" \
  "ds_read_b128 v[36:39], v1 offset:17408\n" \
  "v_mfma_f32_16x16x32_bf16 v[52:55], v[224:227], v[16:19], v[52:55]\n" \
  "v_mfma_f32_16x16x32_bf16 v[28:31], v[80:83], a[28:31], v[28:31]\n" \
  "v_mfma_f32_16x16x32_bf16 v[40:43], v[84:87], a[28:31], v[40:43]\n" \
  "s_waitcnt lgkmcnt(4)\n" \
  "v_mfma_f32_16x16x32_bf16 v[44:47], a[212:215], v[20:23], v[44:47]\n" \
  "ds_read_b128 v[16:19], v1 offset:18432\n" \
  "v_mfma_f32_16x16x32_bf16 v[52:55], v[228:231], v[20:23], v[52:55]\n" \
  "s_waitcnt lgkmcnt(4)\n" \
  "v_mfma_f32_16x16x32_bf16 v[44:47], a[216:219], v[24:27], v[44:47]\n" \
  "ds_read_b128 v[20:23], v1 offset:19456\n" \
  "v_mfma_f32_16x16x32_bf16 v[52:55], v[232:235], v[24:27], v[52:55]\n" \
  "buffer_load_dwordx4 v0, s[44:47], s87 offen offset:3072 sc1 lds\n" \
  "v_mfma_f32_16x16x32_bf16 v[24:27], v[56:59], a[32:35], 0\n" \
  "v_mfma_f32_16x16x32_bf16 v[88:91], v[60:63], a[32:35], 0\n" \
  "s_waitcnt lgkmcnt(4)\n" \
  "v_mfma_f32_16x16x32_bf16 v[44:47], a[220:223], v[48:51], v[44:47]\n" \
  "ds_read_b128 v[92:95], v1 offset:20480\n" \
  "v_mfma_f32_16x16x32_bf16 v[52:55], v[236:239], v[48:51], v[52:55]\n" \
  "v_mfma_f32_16x16x32_bf16 v[24:27], v[64:67], a[36:39], v[24:27]\n" \
  "v_cvt_pk_bf16_f32 v48, v28, v29\n" \
  "v_cvt_pk_bf16_f32 v49, v30, v31\n" \
  "v_mfma_f32_16x16x32_bf16 v[88:91], v[68:71], a[36:39], v[88:91]\n" \
  "v_cvt_pk_bf16_f32 v50, v40, v41\n" \
  "v_cvt_pk_bf16_f32 v51, v42, v43\n" \
  "s_waitcnt lgkmcnt(4)\n" \
  "v_mfma_f32_16x16x32_bf16 v[44:47], a[224:227], v[32:35], v[44:47]\n" \
  "ds_read_b128 v[28:31], v1 offset:21504\n" \
  "v_mfma_f32_16x16x32_bf16 v[52:55], v[240:243], v[32:35], v[52:55]\n" \
  "s_add_u32 s86, s49, 8388608\n" \
  "buffer_store_dwordx4 v[48:51], v0, s[44:47], s86 offen sc1\n" \
  "v_mfma_f32_16x16x32_bf16 v[24:27], v[72:75], a[40:43], v[24:27]\n" \
  "v_mfma_f32_16x16x32_bf16 v[88:91], v[76:79], a[40:43], v[88:91]\n" \
  "s_add_u32 m0, s78, 4096\n" \
  "s_add_u32 s87, s48, 4096\n" \
  "buffer_load_dwordx4 v0, s[44:47], s87 offen sc1 lds\n" \
  "s_waitcnt lgkmcnt(4)\n" \
  "v_mfma_f32_16x16x32_bf16 v[44:47], a[228:231], v[36:39], v[44:47]\n" \
  "ds_read_b128 v[32:35], v1 offset:22528\n" \
  "v_mfma_f32_16x16x32_bf16 v[52:55], v[244:247], v[36:39], v[52:55]\n" \
  "v_mfma_f32_16x16x32_bf16 v[24:27], v[80:83], a[44:47], v[24:27]\n" \
  "v_mfma_f32_16x16x32_bf16 v[88:91], v[84:87], a[44:47], v[88:91]\n" \
  "s_waitcnt lgkmcnt(4)\n" \
  "v_mfma_f32_16x16x32_bf16 v[44:47], a[232:235], v[16:19], v[44:47]\n" \
  "ds_read_b128 v[36:39], v1 offset:23552\n" \
  "v_mfma_f32_16x16x32_bf16 v[52:55], v[248:251], v[16:19], v[52:55]\n" \
  "s_waitcnt lgkmcnt(4)\n" \
  "v_mfma_f32_16x16x32_bf16 v[44:47], a[236:239], v[20:23], v[44:47]\n" \
  "ds_read_b128 v[16:19], v1 offset:24576\n" \
  "s_cmp_eq_u32 s80, 0\n" \
  "s_cbranch_scc1 NP%=\n" \
  "s_waitcnt vmcnt(7)\n" \
  "s_mov_b64 exec, 1\n" \
  "buffer_atomic_add v10, off, s[36:39], s52\n" \
  "s_mov_b64 exec, -1\n" \
  "NP%=:\n" \
  "v_mfma_f32_16x16x32_bf16 v[52:55], v[252:255], v[20:23], v[52:55]\n" \
  "v_mfma_f32_16x16x32_bf16 v[20:23], v[56:59], a[48:51], 0\n" \
  "v_mfma_f32_16x16x32_bf16 v[40:43], v[60:63], a[48:51], 0\n" \
  "buffer_load_dwordx4 v0, s[44:47], s87 offen offset:1024 sc1 lds\n" \
  "s_waitcnt lgkmcnt(4)\n" \
  "v_mfma_f32_16x16x32_bf16 v[48:51], a[160:163], v[92:95], 0\n" \
  "ds_read_b128 v[96:99], v1 offset:25600\n" \
  "v_mfma_f32_16x16x32_bf16 v[100:103], a[240:243], v[92:95], 0\n" \
  "v_mfma_f32_16x16x32_bf16 v[20:23], v[64:67], a[52:55], v[20:23]\n" \
  "v_cvt_pk_bf16_f32 v92, v24, v25\n" \
  "v_cvt_pk_bf16_f32 v93, v26, v27\n" \
  "v_mfma_f32_16x16x32_bf16 v[40:43], v[68:71], a[52:55], v[40:43]\n" \
  "v_cvt_pk_bf16_f32 v94, v88, v89\n" \
  "v_cvt_pk_bf16_f32 v95, v90, v91\n" \
  "v_cvt_pk_bf16_f32 v24, v44, v45\n" \
  "v_cvt_pk_bf16_f32 v25, v46, v47\n" \
  "s_waitcnt lgkmcnt(4)\n" \
  "v_mfma_f32_16x16x32_bf16 v[48:51], a[164:167], v[28:31], v[48:51]\n" \
  "ds_read_b128 v[44:47], v1 offset:26624\n" \
  "v_cvt_pk_bf16_f32 v26, v52, v53\n" \
  "v_cvt_pk_bf16_f32 v27, v54, v55\n" \
  "v_mfma_f32_16x16x32_bf16 v[100:103], a[244:247], v[28:31], v[100:103]\n" \
  "s_add_u32 s86, s49, 16777216\n" \
  "buffer_store_dwordx4 v[92:95], v0, s[44:47], s86 offen sc1\n" \
  "ds_write_b128 v2, v[24:27] offset:0\n" \
  "v_mfma_f32_16x16x32_bf16 v[20:23], v[72:75], a[56:59], v[20:23]\n" \
  "v_mfma_f32_16x16x32_bf16 v[40:43], v[76:79], a[56:59], v[40:43]\n" \
  "s_waitcnt lgkmcnt(5)\n" \
  "v_mfma_f32_16x16x32_bf16 v[48:51], a[168:171], v[32:35], v[48:51]\n" \
  "ds_read_b128 v[24:27], v1 offset:27648\n" \
  "v_mfma_f32_16x16x32_bf16 v[100:103], a[248:251], v[32:35], v[100:103]\n" \
  "buffer_load_dwordx4 v0, s[44:47], s87 offen offset:2048 sc1 lds\n" \
  "v_mfma_f32_16x16x32_bf16 v[20:23], v[80:83], a[60:63], v[20:23]\n" \
  "v_mfma_f32_16x16x32_bf16 v[40:43], v[84:87], a[60:63], v[40:43]\n" \
  "s_waitcnt lgkmcnt(5)\n" \
  "v_mfma_f32_16x16x32_bf16 v[48:51], a[172:175], v[36:39], v[48:51]\n" \
  "ds_read_b128 v[28:31], v1 offset:28672\n" \
  "v_mfma_f32_16x16x32_bf16 v[100:103], a[252:255], v[36:39], v[100:103]\n" \
  "s_waitcnt lgkmcnt(5)\n" \
  "v_mfma_f32_16x16x32_bf16 v[48:51], a[176:179], v[16:19], v[48:51]\n" \
  "ds_read_b128 v[32:35], v1 offset:29696\n" \
  "v_mfma_f32_16x16x32_bf16 v[100:103], v[192:195], v[16:19], v[100:103]\n" \
  "v_mfma_f32_16x16x32_bf16 v[16:19], v[56:59], a[64:67], 0\n" \
  "v_mfma_f32_16x16x32_bf16 v[36:39], v[60:63], a[64:67], 0\n" \
  "s_waitcnt lgkmcnt(5)\n" \
  "v_mfma_f32_16x16x32_bf16 v[48:51], a[180:183], v[96:99], v[48:51]\n" \
  "ds_read_b128 v[52:55], v1 offset:30720\n" \
  "v_mfma_f32_16x16x32_bf16 v[100:103], v[196:199], v[96:99], v[100:103]\n" \
  "buffer_load_dwordx4 v0, s[44:47], s87 offen offset:3072 sc1 lds\n" \
  "v_mfma_f32_16x16x32_bf16 v[16:19], v[64:67], a[68:71], v[16:19]\n" \
  "v_cvt_pk_bf16_f32 v88, v20, v21\n" \
  "v_cvt_pk_bf16_f32 v89, v22, v23\n" \
  "v_mfma_f32_16x16x32_bf16 v[36:39], v[68:71], a[68:71], v[36:39]\n" \
  "v_cvt_pk_bf16_f32 v90, v40, v41\n" \
  "v_cvt_pk_bf16_f32 v91, v42, v43\n" \
  "s_waitcnt lgkmcnt(5)\n" \
  "v_mfma_f32_16x16x32_bf16 v[48:51], a[184:187], v[44:47], v[48:51]\n" \
  "ds_read_b128 v[20:23], v1 offset:31744\n" \
  "v_mfma_f32_16x16x32_bf16 v[100:103], v[200:203], v[44:47], v[100:103]\n" \
  "s_add_u32 s86, s49, 25165824\n" \
  "buffer_store_dwordx4 v[88:91], v0, s[44:47], s86 offen sc1\n" \
  "v_mfma_f32_16x16x32_bf16 v[16:19], v[72:75], a[72:75], v[16:19]\n" \
  "v_mfma_f32_16x16x32_bf16 v[36:39], v[76:79], a[72:75], v[36:39]\n" \
  "s_waitcnt lgkmcnt(4)\n" \
  "v_mfma_f32_16x16x32_bf16 v[48:51], a[188:191], v[24:27], v[48:51]\n" \
  "ds_read_b128 v[40:43], v1 offset:32768\n" \
  "v_mfma_f32_16x16x32_bf16 v[100:103], v[204:207], v[24:27], v[100:103]\n" \
  "v_mfma_f32_16x16x32_bf16 v[16:19], v[80:83], a[76:79], v[16:19]\n" \
  "v_mfma_f32_16x16x32_bf16 v[36:39], v[84:87], a[76:79], v[36:39]\n" \
  "s_add_u32 m0, s78, 8192\n" \
  "s_add_u32 s87, s48, 8192\n" \
  "buffer_load_dwordx4 v0, s[44:47], s87 offen sc1 lds\n" \
  "s_waitcnt lgkmcnt(4)\n" \
  "v_mfma_f32_16x16x32_bf16 v[48:51], a[192:195], v[28:31], v[48:51]\n" \
  "ds_read_b128 v[24:27], v1 offset:33792\n" \
  "v_mfma_f32_16x16x32_bf16 v[100:103], v[208:211], v[28:31], v[100:103]\n" \
  "s_waitcnt lgkmcnt(4)\n" \
  "v_mfma_f32_16x16x32_bf16 v[48:51], a[196:199], v[32:35], v[48:51]\n" \
  "ds_read_b128 v[28:31], v1 offset:34816\n" \
  "v_mfma_f32_16x16x32_bf16 v[100:103], v[212:215], v[32:35], v[100:103]\n" \
  "v_mfma_f32_16x16x32_bf16 v[32:35], v[56:59], a[80:83], 0\n" \
  "v_mfma_f32_16x16x32_bf16 v[44:47], v[60:63], a[80:83], 0\n" \
  "s_waitcnt lgkmcnt(4)\n" \
  "v_mfma_f32_16x16x32_bf16 v[48:51], a[200:203], v[52:55], v[48:51]\n" \
  "ds_read_b128 v[88:91], v1 offset:35840\n" \
  "v_mfma_f32_16x16x32_bf16 v[100:103], v[216:219], v[52:55], v[100:103]\n" \
  "v_mfma_f32_16x16x32_bf16 v[32:35], v[64:67], a[84:87], v[32:35]\n" \
  "v_cvt_pk_bf16_f32 v52, v16, v17\n" \
  "v_cvt_pk_bf16_f32 v53, v18, v19\n" \
  "v_mfma_f32_16x16x32_bf16 v[44:47], v[68:71], a[84:87], v[44:47]\n" \
  "v_cvt_pk_bf16_f32 v54, v36, v37\n" \
  "v_cvt_pk_bf16_f32 v55, v38, v39\n" \
  "buffer_load_dwordx4 v0, s[44:47], s87 offen offset:1024 sc1 lds\n" \
  "s_waitcnt lgkmcnt(4)\n" \
  "v_mfma_f32_16x16x32_bf16 v[48:51], a[204:207], v[20:23], v[48:51]\n" \
  "ds_read_b128 v[16:19], v1 offset:36864\n" \
  "v_mfma_f32_16x16x32_bf16 v[100:103], v[220:223], v[20:23], v[100:103]\n" \
  "s_add_u32 s86, s49, 33554432\n" \
  "buffer_store_dwordx4 v[52:55], v0, s[44:47], s86 offen sc1\n" \
  "v_mfma_f32_16x16x32_bf16 v[32:35], v[72:75], a[88:91], v[32:35]\n" \
  "v_mfma_f32_16x16x32_bf16 v[44:47], v[76:79], a[88:91], v[44:47]\n" \
  "s_waitcnt lgkmcnt(4)\n" \
  "v_mfma_f32_16x16x32_bf16 v[48:51], a[208:211], v[40:43], v[48:51]\n" \
  "ds_read_b128 v[20:23], v1 offset:37888\n" \
  "v_mfma_f32_16x16x32_bf16 v[100:103], v[224:227], v[40:43], v[100:103]\n" \
  "v_mfma_f32_16x16x32_bf16 v[32:35], v[80:83], a[92:95], v[32:35]\n" \
  "v_mfma_f32_16x16x32_bf16 v[44:47], v[84:87], a[92:95], v[44:47]\n" \
  "s_waitcnt lgkmcnt(4)\n" \
  "v_mfma_f32_16x16x32_bf16 v[48:51], a[212:215], v[24:27], v[48:51]\n" \
  "ds_read_b128 v[36:39], v1 offset:38912\n" \
  "v_mfma_f32_16x16x32_bf16 v[100:103], v[228:231], v[24:27], v[100:103]\n" \
  "s_waitcnt lgkmcnt(4)\n" \
  "v_mfma_f32_16x16x32_bf16 v[48:51], a[216:219], v[28:31], v[48:51]\n" \
  "ds_read_b128 v[24:27], v1 offset:39936\n" \
  "v_mfma_f32_16x16x32_bf16 v[100:103], v[232:235], v[28:31], v[100:103]\n" \
  "s_add_u32 s78, s78, 40960\n" \
  "s_add_u32 s79, s79, 1\n" \
  "v_mfma_f32_16x16x32_bf16 v[28:31], v[56:59], a[96:99], 0\n" \
  "v_mfma_f32_16x16x32_bf16 v[40:43], v[60:63], a[96:99], 0\n" \
  "s_cmp_eq_u32 s79, 1\n" \
  "s_cselect_b32 s85, 122880, 0\n" \
  "s_waitcnt lgkmcnt(4)\n" \
  "v_mfma_f32_16x16x32_bf16 v[48:51], a[220:223], v[88:91], v[48:51]\n" \
  "v_mfma_f32_16x16x32_bf16 v[100:103], v[236:239], v[88:91], v[100:103]\n" \
  "s_sub_u32 s78, s78, s85\n" \
  "s_cmp_eq_u32 s79, 3\n" \
  "v_mfma_f32_16x16x32_bf16 v[28:31], v[64:67], a[100:103], v[28:31]\n" \
  "v_cvt_pk_bf16_f32 v52, v32, v33\n" \
  "v_cvt_pk_bf16_f32 v53, v34, v35\n" \
  "v_mfma_f32_16x16x32_bf16 v[40:43], v[68:71], a[100:103], v[40:43]\n" \
  "v_cvt_pk_bf16_f32 v54, v44, v45\n" \
  "v_cvt_pk_bf16_f32 v55, v46, v47\n" \
  "s_cselect_b32 s85, 122880, 0\n" \
  "s_cselect_b32 s79, 0, s79\n" \
  "s_waitcnt lgkmcnt(3)\n" \
  "v_mfma_f32_16x16x32_bf16 v[48:51], a[224:227], v[16:19], v[48:51]\n" \
  "v_mfma_f32_16x16x32_bf16 v[100:103], v[240:243], v[16:19], v[100:103]\n" \
  "s_add_u32 s86, s49, 41943040\n" \
  "buffer_store_dwordx4 v[52:55], v0, s[44:47], s86 offen sc1\n" \
  "v_mfma_f32_16x16x32_bf16 v[28:31], v[72:75], a[104:107], v[28:31]\n" \
  "v_mfma_f32_16x16x32_bf16 v[40:43], v[76:79], a[104:107], v[40:43]\n" \
  "s_waitcnt lgkmcnt(2)\n" \
  "v_mfma_f32_16x16x32_bf16 v[48:51], a[228:231], v[20:23], v[48:51]\n" \
  "v_mfma_f32_16x16x32_bf16 v[100:103], v[244:247], v[20:23], v[100:103]\n" \
  "v_mfma_f32_16x16x32_bf16 v[28:31], v[80:83], a[108:111], v[28:31]\n" \
  "v_mfma_f32_16x16x32_bf16 v[40:43], v[84:87], a[108:111], v[40:43]\n" \
  "s_waitcnt lgkmcnt(1)\n" \
  "v_mfma_f32_16x16x32_bf16 v[48:51], a[232:235], v[36:39], v[48:51]\n" \
  "v_mfma_f32_16x16x32_bf16 v[100:103], v[248:251], v[36:39], v[100:103]\n" \
  "s_waitcnt lgkmcnt(0)\n" \
  "v_mfma_f32_16x16x32_bf16 v[48:51], a[236:239], v[24:27], v[48:51]\n" \
  "v_mfma_f32_16x16x32_bf16 v[100:103], v[252:255], v[24:27], v[100:103]\n" \
  "v_mfma_f32_16x16x32_bf16 v[16:19], v[56:59], a[112:115], 0\n" \
  "v_add_u32 v1, 0xa000, v1\n" \
  "v_subrev_u32 v1, s85, v1\n" \
  "v_mfma_f32_16x16x32_bf16 v[20:23], v[60:63], a[112:115], 0\n" \
  "s_add_u32 s48, s48, 40960\n" \
  "v_xor_b32 v3, 0x2000, v3\n" \
  "v_mfma_f32_16x16x32_bf16 v[16:19], v[64:67], a[116:119], v[16:19]\n" \
  "v_mfma_f32_16x16x32_bf16 v[20:23], v[68:71], a[116:119], v[20:23]\n" \
  "v_mfma_f32_16x16x32_bf16 v[16:19], v[72:75], a[120:123], v[16:19]\n" \
  "v_cvt_pk_bf16_f32 v24, v28, v29\n" \
  "v_cvt_pk_bf16_f32 v25, v30, v31\n" \
  "v_mfma_f32_16x16x32_bf16 v[20:23], v[76:79], a[120:123], v[20:23]\n" \
  "v_cvt_pk_bf16_f32 v26, v40, v41\n" \
  "v_cvt_pk_bf16_f32 v27, v42, v43\n" \
  "v_cvt_pk_bf16_f32 v28, v48, v49\n" \
  "v_cvt_pk_bf16_f32 v29, v50, v51\n" \
  "v_mfma_f32_16x16x32_bf16 v[16:19], v[80:83], a[124:127], v[16:19]\n" \
  "v_cvt_pk_bf16_f32 v30, v100, v101\n" \
  "v_cvt_pk_bf16_f32 v31, v102, v103\n" \
  "v_mfma_f32_16x16x32_bf16 v[20:23], v[84:87], a[124:127], v[20:23]\n" \
  "s_add_u32 s86, s49, 50331648\n" \
  "buffer_store_dwordx4 v[24:27], v0, s[44:47], s86 offen sc1\n" \
  "ds_write_b128 v2, v[28:31] offset:4096\n" \
  "v_mfma_f32_16x16x32_bf16 v[44:47], v[56:59], a[128:131], 0\n" \
  "v_mfma_f32_16x16x32_bf16 v[48:51], v[60:63], a[128:131], 0\n" \
  "v_mfma_f32_16x16x32_bf16 v[44:47], v[64:67], a[132:135], v[44:47]\n" \
  "v_mfma_f32_16x16x32_bf16 v[48:51], v[68:71], a[132:135], v[48:51]\n" \
  "v_mfma_f32_16x16x32_bf16 v[44:47], v[72:75], a[136:139], v[44:47]\n" \
  "s_waitcnt vmcnt(18) lgkmcnt(0)\n" \
  "s_cmp_lg_u32 s64, 0\n" \
  "s_cbranch_scc1 NCb%=\n" \
  "v_cmp_gt_i32 vcc, s53, v12\n" \
  "s_cbranch_vccz NCb%=\n" \
  "NBb%=:\n" \
  "s_cmp_eq_u32 s55, 0\n" \
  "s_cbranch_scc1 NCb%=\n" \
  "s_sub_u32 s55, s55, 1\n" \
  "s_sleep 8\n" \
  "buffer_load_dword v12, v9, s[36:39], 0 offen sc1\n" \
  "s_waitcnt vmcnt(0)\n" \
  "v_cmp_gt_i32 vcc, s53, v12\n" \
  "s_cbranch_vccnz NBb%=\n" \
  "NCb%=:\n" \
  "s_add_u32 s53, s53, 1\n" \
  "s_barrier\n" \
  "v_xor_b32 v2, 0x2000, v2\n" \
  "v_mfma_f32_16x16x32_bf16 v[48:51], v[76:79], a[136:139], v[48:51]\n" \
  "v_cvt_pk_bf16_f32 v28, v16, v17\n" \
  "v_cvt_pk_bf16_f32 v29, v18, v19\n" \
  "ds_read_b128 v[16:19], v1 offset:0\n" \
  "v_mfma_f32_16x16x32_bf16 v[44:47], v[80:83], a[140:143], v[44:47]\n" \
  "v_cvt_pk_bf16_f32 v30, v20, v21\n" \
  "v_cvt_pk_bf16_f32 v31, v22, v23\n" \
  "ds_read_b128 v[20:23], v1 offset:1024\n" \
  "v_mfma_f32_16x16x32_bf16 v[48:51], v[84:87], a[140:143], v[48:51]\n" \
  "ds_read_b128 v[24:27], v1 offset:2048\n" \
  "v_mfma_f32_16x16x32_bf16 v[36:39], v[56:59], a[144:147], 0\n" \
  "s_add_u32 s86, s49, 58720256\n" \
  "buffer_store_dwordx4 v[28:31], v0, s[44:47], s86 offen sc1\n" \
  "s_nop 1\n" \
  "ds_read_b128 v[28:31], v1 offset:3072\n" \
  "v_mfma_f32_16x16x32_bf16 v[40:43], v[60:63], a[144:147], 0\n" \
  "ds_read_b128 v[32:35], v1 offset:4096\n" \
  "v_mfma_f32_16x16x32_bf16 v[36:39], v[64:67], a[148:151], v[36:39]\n" \
  "v_mfma_f32_16x16x32_bf16 v[40:43], v[68:71], a[148:151], v[40:43]\n" \
  "v_mfma_f32_16x16x32_bf16 v[36:39], v[72:75], a[152:155], v[36:39]\n" \
  "v_mfma_f32_16x16x32_bf16 v[40:43], v[76:79], a[152:155], v[40:43]\n" \
  "v_cvt_pk_bf16_f32 v52, v44, v45\n" \
  "v_cvt_pk_bf16_f32 v53, v46, v47\n" \
  "v_mfma_f32_16x16x32_bf16 v[36:39], v[80:83], a[156:159], v[36:39]\n" \
  "v_cvt_pk_bf16_f32 v54, v48, v49\n" \
  "v_cvt_pk_bf16_f32 v55, v50, v51\n" \
  "v_mfma_f32_16x16x32_bf16 v[40:43], v[84:87], a[156:159], v[40:43]\n" \
  "s_add_u32 s86, s49, 67108864\n" \
  "buffer_store_dwordx4 v[52:55], v0, s[44:47], s86 offen sc1\n" \
  "s_add_u32 s30, s49, 75497472\n" \
  "s_add_u32 s49, s49, 32768\n" \
  "s_bitset0_b32 s49, 19\n" \
  "s_add_u32 s80, s80, 1\n" \
  "s_cmp_lg_u32 s80, 64\n" \
  "s_waitcnt lgkmcnt(0)\n" \
  "s_cbranch_scc1 LOOP%=\n" \
  "v_cvt_pk_bf16_f32 v44, v36, v37\n" \
  "v_cvt_pk_bf16_f32 v45, v38, v39\n" \
  "v_cvt_pk_bf16_f32 v46, v40, v41\n" \
  "v_cvt_pk_bf16_f32 v47, v42, v43\n" \
  "buffer_store_dwordx4 v[44:47], v0, s[44:47], s30 offen sc1\n" \
  "s_waitcnt vmcnt(0)\n" \
  "s_mov_b64 exec, 1\n" \
  "buffer_atomic_add v10, off, s[36:39], s52\n" \
  "s_mov_b64 exec, -1\n" \
  "DONE%=:\n" \
  "s_waitcnt vmcnt(0) lgkmcnt(0)\n" \
  "s_cmp_eq_u32 s73, -1\n" \
  "s_cbranch_scc1 END%=\n" \
  "s_memrealtime vcc\n" \
  "s_waitcnt lgkmcnt(0)\n" \
  "v_mov_b32 v32, vcc_lo\n" \
  "s_sub_u32 s22, vcc_lo, s22\n" \
  "s_memtime vcc\n" \
  "s_waitcnt lgkmcnt(0)\n" \
  "s_sub_u32 s20, vcc_lo, s20\n" \
  "v_mov_b32 v33, s20\n" \
  "v_mov_b32 v34, s22\n" \
  "v_mov_b32 v35, s24\n" \
  "s_mov_b64 exec, 1\n" \
  "buffer_store_dword v32, off, s[44:47], s73\n" \
  "buffer_store_dword v33, off, s[44:47], s73 offset:1024\n" \
  "buffer_store_dword v34, off, s[44:47], s73 offset:2048\n" \
  "buffer_store_dword v35, off, s[44:47], s73 offset:3072\n" \
  "s_waitcnt vmcnt(0)\n" \
  "END%=:\n" \
  "s_endpgm\n"
#define REC_F_ASM \
  "s_lshl_b32 s86, s64, 12\n" \
  "s_add_u32 s86, s86, 32768\n" \
  "s_add_u32 m0, s86, 0\n" \
  "s_mov_b32 s85, 0\n" \
  "buffer_load_dwordx4 v16, s[56:59], s85 offen nt lds\n" \
  "s_add_u32 m0, s86, 1024\n" \
  "s_mov_b32 s85, 13824\n" \
  "buffer_load_dwordx4 v16, s[56:59], s85 offen nt lds\n" \
  "s_add_u32 m0, s86, 2048\n" \
  "s_mov_b32 s85, 27648\n" \
  "buffer_load_dwordx4 v17, s[56:59], s85 offen nt lds\n" \
  "s_add_u32 m0, s86, 3072\n" \
  "s_mov_b32 s85, 41472\n" \
  "buffer_load_dwordx4 v17, s[56:59], s85 offen nt lds\n" \
  "s_add_u32 m0, s86, 16384\n" \
  "s_mov_b32 s85, 221184\n" \
  "buffer_load_dwordx4 v16, s[56:59], s85 offen nt lds\n" \
  "s_add_u32 m0, s86, 17408\n" \
  "s_mov_b32 s85, 235008\n" \
  "buffer_load_dwordx4 v16, s[56:59], s85 offen nt lds\n" \
  "s_add_u32 m0, s86, 18432\n" \
  "s_mov_b32 s85, 248832\n" \
  "buffer_load_dwordx4 v17, s[56:59], s85 offen nt lds\n" \
  "s_add_u32 m0, s86, 19456\n" \
  "s_mov_b32 s85, 262656\n" \
  "buffer_load_dwordx4 v17, s[56:59], s85 offen nt lds\n" \
  "s_add_u32 m0, s86, 32768\n" \
  "s_mov_b32 s85, 442368\n" \
  "buffer_load_dwordx4 v16, s[56:59], s85 offen nt lds\n" \
  "s_add_u32 m0, s86, 33792\n" \
  "s_mov_b32 s85, 456192\n" \
  "buffer_load_dwordx4 v16, s[56:59], s85 offen nt lds\n" \
  "s_add_u32 m0, s86, 34816\n" \
  "s_mov_b32 s85, 470016\n" \
  "buffer_load_dwordx4 v17, s[56:59], s85 offen nt lds\n" \
  "s_add_u32 m0, s86, 35840\n" \
  "s_mov_b32 s85, 483840\n" \
  "buffer_load_dwordx4 v17, s[56:59], s85 offen nt lds\n" \
  "s_add_u32 m0, s86, 49152\n" \
  "s_mov_b32 s85, 663552\n" \
  "buffer_load_dwordx4 v16, s[56:59], s85 offen nt lds\n" \
  "s_add_u32 m0, s86, 50176\n" \
  "s_mov_b32 s85, 677376\n" \
  "buffer_load_dwordx4 v16, s[56:59], s85 offen nt lds\n" \
  "s_add_u32 m0, s86, 51200\n" \
  "s_mov_b32 s85, 691200\n" \
  "buffer_load_dwordx4 v17, s[56:59], s85 offen nt lds\n" \
  "s_add_u32 m0, s86, 52224\n" \
  "s_mov_b32 s85, 705024\n" \
  "buffer_load_dwordx4 v17, s[56:59], s85 offen nt lds\n" \
  "s_add_u32 m0, s86, 65536\n" \
  "s_mov_b32 s85, 884736\n" \
  "buffer_load_dwordx4 v16, s[56:59], s85 offen nt lds\n" \
  "s_add_u32 m0, s86, 66560\n" \
  "s_mov_b32 s85, 898560\n" \
  "buffer_load_dwordx4 v16, s[56:59], s85 offen nt lds\n" \
  "s_add_u32 m0, s86, 67584\n" \
  "s_mov_b32 s85, 912384\n" \
  "buffer_load_dwordx4 v17, s[56:59], s85 offen nt lds\n" \
  "s_add_u32 m0, s86, 68608\n" \
  "s_mov_b32 s85, 926208\n" \
  "buffer_load_dwordx4 v17, s[56:59], s85 offen nt lds\n" \
  "s_add_u32 m0, s86, 81920\n" \
  "s_mov_b32 s85, 1105920\n" \
  "buffer_load_dwordx4 v16, s[56:59], s85 offen nt lds\n" \
  "s_add_u32 m0, s86, 82944\n" \
  "s_mov_b32 s85, 1119744\n" \
  "buffer_load_dwordx4 v16, s[56:59], s85 offen nt lds\n" \
  "s_add_u32 m0, s86, 83968\n" \
  "s_mov_b32 s85, 1133568\n" \
  "buffer_load_dwordx4 v17, s[56:59], s85 offen nt lds\n" \
  "s_add_u32 m0, s86, 84992\n" \
  "s_mov_b32 s85, 1147392\n" \
  "buffer_load_dwordx4 v17, s[56:59], s85 offen nt lds\n" \
  "s_waitcnt vmcnt(16) lgkmcnt(0)\n" \
  "s_barrier\n" \
  "s_add_u32 m0, s86, 98304\n" \
  "s_mov_b32 s85, 1327104\n" \
  "buffer_load_dwordx4 v16, s[56:59], s85 offen nt lds\n" \
  "s_add_u32 m0, s86, 99328\n" \
  "s_mov_b32 s85, 1340928\n" \
  "buffer_load_dwordx4 v16, s[56:59], s85 offen nt lds\n" \
  "s_add_u32 m0, s86, 100352\n" \
  "s_mov_b32 s85, 1354752\n" \
  "buffer_load_dwordx4 v17, s[56:59], s85 offen nt lds\n" \
  "s_add_u32 m0, s86, 101376\n" \
  "s_mov_b32 s85, 1368576\n" \
  "buffer_load_dwordx4 v17, s[56:59], s85 offen nt lds\n" \
  "s_add_u32 m0, s86, 114688\n" \
  "s_mov_b32 s85, 1548288\n" \
  "buffer_load_dwordx4 v16, s[56:59], s85 offen nt lds\n" \
  "s_add_u32 m0, s86, 115712\n" \
  "s_mov_b32 s85, 1562112\n" \
  "buffer_load_dwordx4 v16, s[56:59], s85 offen nt lds\n" \
  "s_add_u32 m0, s86, 116736\n" \
  "s_mov_b32 s85, 1575936\n" \
  "buffer_load_dwordx4 v17, s[56:59], s85 offen nt lds\n" \
  "s_add_u32 m0, s86, 117760\n" \
  "s_mov_b32 s85, 1589760\n" \
  "buffer_load_dwordx4 v17, s[56:59], s85 offen nt lds\n" \
  "ds_read_b64_tr_b16 a[160:161], v18 offset:0\n" \
  "ds_read_b64_tr_b16 a[162:163], v18 offset:1024\n" \
  "ds_read_b64_tr_b16 a[164:165], v18 offset:8192\n" \
  "ds_read_b64_tr_b16 a[166:167], v18 offset:9216\n" \
  "ds_read_b64_tr_b16 a[240:241], v19 offset:0\n" \
  "ds_read_b64_tr_b16 a[242:243], v19 offset:1024\n" \
  "ds_read_b64_tr_b16 a[244:245], v19 offset:8192\n" \
  "ds_read_b64_tr_b16 a[246:247], v19 offset:9216\n" \
  "ds_read_b64_tr_b16 a[168:169], v18 offset:16384\n" \
  "ds_read_b64_tr_b16 a[170:171], v18 offset:17408\n" \
  "ds_read_b64_tr_b16 a[172:173], v18 offset:24576\n" \
  "ds_read_b64_tr_b16 a[174:175], v18 offset:25600\n" \
  "ds_read_b64_tr_b16 a[248:249], v19 offset:16384\n" \
  "ds_read_b64_tr_b16 a[250:251], v19 offset:17408\n" \
  "ds_read_b64_tr_b16 a[252:253], v19 offset:24576\n" \
  "ds_read_b64_tr_b16 a[254:255], v19 offset:25600\n" \
  "s_waitcnt vmcnt(16) lgkmcnt(0)\n" \
  "s_barrier\n" \
  "s_add_u32 m0, s86, 0\n" \
  "s_mov_b32 s85, 1769472\n" \
  "buffer_load_dwordx4 v16, s[56:59], s85 offen nt lds\n" \
  "s_add_u32 m0, s86, 1024\n" \
  "s_mov_b32 s85, 1783296\n" \
  "buffer_load_dwordx4 v16, s[56:59], s85 offen nt lds\n" \
  "s_add_u32 m0, s86, 2048\n" \
  "s_mov_b32 s85, 1797120\n" \
  "buffer_load_dwordx4 v17, s[56:59], s85 offen nt lds\n" \
  "s_add_u32 m0, s86, 3072\n" \
  "s_mov_b32 s85, 1810944\n" \
  "buffer_load_dwordx4 v17, s[56:59], s85 offen nt lds\n" \
  "s_add_u32 m0, s86, 16384\n" \
  "s_mov_b32 s85, 1990656\n" \
  "buffer_load_dwordx4 v16, s[56:59], s85 offen nt lds\n" \
  "s_add_u32 m0, s86, 17408\n" \
  "s_mov_b32 s85, 2004480\n" \
  "buffer_load_dwordx4 v16, s[56:59], s85 offen nt lds\n" \
  "s_add_u32 m0, s86, 18432\n" \
  "s_mov_b32 s85, 2018304\n" \
  "buffer_load_dwordx4 v17, s[56:59], s85 offen nt lds\n" \
  "s_add_u32 m0, s86, 19456\n" \
  "s_mov_b32 s85, 2032128\n" \
  "buffer_load_dwordx4 v17, s[56:59], s85 offen nt lds\n" \
  "ds_read_b64_tr_b16 a[176:177], v18 offset:32768\n" \
  "ds_read_b64_tr_b16 a[178:179], v18 offset:33792\n" \
  "ds_read_b64_tr_b16 a[180:181], v18 offset:40960\n" \
  "ds_read_b64_tr_b16 a[182:183], v18 offset:41984\n" \
  "ds_read_b64_tr_b16 v[192:193], v19 offset:32768\n" \
  "ds_read_b64_tr_b16 v[194:195], v19 offset:33792\n" \
  "ds_read_b64_tr_b16 v[196:197], v19 offset:40960\n" \
  "ds_read_b64_tr_b16 v[198:199], v19 offset:41984\n" \
  "ds_read_b64_tr_b16 a[184:185], v18 offset:49152\n" \
  "ds_read_b64_tr_b16 a[186:187], v18 offset:50176\n" \
  "ds_read_b64_tr_b16 a[188:189], v18 offset:57344\n" \
  "ds_read_b64_tr_b16 a[190:191], v18 offset:58368\n" \
  "ds_read_b64_tr_b16 v[200:201], v19 offset:49152\n" \
  "ds_read_b64_tr_b16 v[202:203], v19 offset:50176\n" \
  "ds_read_b64_tr_b16 v[204:205], v19 offset:57344\n" \
  "ds_read_b64_tr_b16 v[206:207], v19 offset:58368\n" \
  "s_waitcnt vmcnt(16) lgkmcnt(0)\n" \
  "s_barrier\n" \
  "s_add_u32 m0, s86, 32768\n" \
  "s_mov_b32 s85, 0\n" \
  "buffer_load_dwordx4 v20, s[60:63], s85 offen nt lds\n" \
  "s_add_u32 m0, s86, 33792\n" \
  "s_mov_b32 s85, 5120\n" \
  "buffer_load_dwordx4 v20, s[60:63], s85 offen nt lds\n" \
  "s_add_u32 m0, s86, 34816\n" \
  "s_mov_b32 s85, 10240\n" \
  "buffer_load_dwordx4 v21, s[60:63], s85 offen nt lds\n" \
  "s_add_u32 m0, s86, 35840\n" \
  "s_mov_b32 s85, 15360\n" \
  "buffer_load_dwordx4 v21, s[60:63], s85 offen nt lds\n" \
  "s_add_u32 m0, s86, 49152\n" \
  "s_mov_b32 s85, 81920\n" \
  "buffer_load_dwordx4 v20, s[60:63], s85 offen nt lds\n" \
  "s_add_u32 m0, s86, 50176\n" \
  "s_mov_b32 s85, 87040\n" \
  "buffer_load_dwordx4 v20, s[60:63], s85 offen nt lds\n" \
  "s_add_u32 m0, s86, 51200\n" \
  "s_mov_b32 s85, 92160\n" \
  "buffer_load_dwordx4 v21, s[60:63], s85 offen nt lds\n" \
  "s_add_u32 m0, s86, 52224\n" \
  "s_mov_b32 s85, 97280\n" \
  "buffer_load_dwordx4 v21, s[60:63], s85 offen nt lds\n" \
  "v_xor_b32 v18, 0x10000, v18\n" \
  "v_xor_b32 v19, 0x10000, v19\n" \
  "v_xor_b32 v22, 0x10000, v22\n" \
  "v_xor_b32 v23, 0x10000, v23\n" \
  "ds_read_b64_tr_b16 a[192:193], v18 offset:0\n" \
  "ds_read_b64_tr_b16 a[194:195], v18 offset:1024\n" \
  "ds_read_b64_tr_b16 a[196:197], v18 offset:8192\n" \
  "ds_read_b64_tr_b16 a[198:199], v18 offset:9216\n" \
  "ds_read_b64_tr_b16 v[208:209], v19 offset:0\n" \
  "ds_read_b64_tr_b16 v[210:211], v19 offset:1024\n" \
  "ds_read_b64_tr_b16 v[212:213], v19 offset:8192\n" \
  "ds_read_b64_tr_b16 v[214:215], v19 offset:9216\n" \
  "ds_read_b64_tr_b16 a[200:201], v18 offset:16384\n" \
  "ds_read_b64_tr_b16 a[202:203], v18 offset:17408\n" \
  "ds_read_b64_tr_b16 a[204:205], v18 offset:24576\n" \
  "ds_read_b64_tr_b16 a[206:207], v18 offset:25600\n" \
  "ds_read_b64_tr_b16 v[216:217], v19 offset:16384\n" \
  "ds_read_b64_tr_b16 v[218:219], v19 offset:17408\n" \
  "ds_read_b64_tr_b16 v[220:221], v19 offset:24576\n" \
  "ds_read_b64_tr_b16 v[222:223], v19 offset:25600\n" \
  "s_waitcnt vmcnt(16) lgkmcnt(0)\n" \
  "s_barrier\n" \
  "s_add_u32 m0, s86, 65536\n" \
  "s_mov_b32 s85, 256\n" \
  "buffer_load_dwordx4 v20, s[60:63], s85 offen nt lds\n" \
  "s_add_u32 m0, s86, 66560\n" \
  "s_mov_b32 s85, 5376\n" \
  "buffer_load_dwordx4 v20, s[60:63], s85 offen nt lds\n" \
  "s_add_u32 m0, s86, 67584\n" \
  "s_mov_b32 s85, 10496\n" \
  "buffer_load_dwordx4 v21, s[60:63], s85 offen nt lds\n" \
  "s_add_u32 m0, s86, 68608\n" \
  "s_mov_b32 s85, 15616\n" \
  "buffer_load_dwordx4 v21, s[60:63], s85 offen nt lds\n" \
  "s_add_u32 m0, s86, 81920\n" \
  "s_mov_b32 s85, 82176\n" \
  "buffer_load_dwordx4 v20, s[60:63], s85 offen nt lds\n" \
  "s_add_u32 m0, s86, 82944\n" \
  "s_mov_b32 s85, 87296\n" \
  "buffer_load_dwordx4 v20, s[60:63], s85 offen nt lds\n" \
  "s_add_u32 m0, s86, 83968\n" \
  "s_mov_b32 s85, 92416\n" \
  "buffer_load_dwordx4 v21, s[60:63], s85 offen nt lds\n" \
  "s_add_u32 m0, s86, 84992\n" \
  "s_mov_b32 s85, 97536\n" \
  "buffer_load_dwordx4 v21, s[60:63], s85 offen nt lds\n" \
  "ds_read_b64_tr_b16 a[208:209], v18 offset:32768\n" \
  "ds_read_b64_tr_b16 a[210:211], v18 offset:33792\n" \
  "ds_read_b64_tr_b16 a[212:213], v18 offset:40960\n" \
  "ds_read_b64_tr_b16 a[214:215], v18 offset:41984\n" \
  "ds_read_b64_tr_b16 v[224:225], v19 offset:32768\n" \
  "ds_read_b64_tr_b16 v[226:227], v19 offset:33792\n" \
  "ds_read_b64_tr_b16 v[228:229], v19 offset:40960\n" \
  "ds_read_b64_tr_b16 v[230:231], v19 offset:41984\n" \
  "ds_read_b64_tr_b16 a[216:217], v18 offset:49152\n" \
  "ds_read_b64_tr_b16 a[218:219], v18 offset:50176\n" \
  "ds_read_b64_tr_b16 a[220:221], v18 offset:57344\n" \
  "ds_read_b64_tr_b16 a[222:223], v18 offset:58368\n" \
  "ds_read_b64_tr_b16 v[232:233], v19 offset:49152\n" \
  "ds_read_b64_tr_b16 v[234:235], v19 offset:50176\n" \
  "ds_read_b64_tr_b16 v[236:237], v19 offset:57344\n" \
  "ds_read_b64_tr_b16 v[238:239], v19 offset:58368\n" \
  "s_waitcnt vmcnt(16) lgkmcnt(0)\n" \
  "s_barrier\n" \
  "s_add_u32 m0, s86, 98304\n" \
  "s_mov_b32 s85, 512\n" \
  "buffer_load_dwordx4 v20, s[60:63], s85 offen nt lds\n" \
  "s_add_u32 m0, s86, 99328\n" \
  "s_mov_b32 s85, 5632\n" \
  "buffer_load_dwordx4 v20, s[60:63], s85 offen nt lds\n" \
  "s_add_u32 m0, s86, 100352\n" \
  "s_mov_b32 s85, 10752\n" \
  "buffer_load_dwordx4 v21, s[60:63], s85 offen nt lds\n" \
  "s_add_u32 m0, s86, 101376\n" \
  "s_mov_b32 s85, 15872\n" \
  "buffer_load_dwordx4 v21, s[60:63], s85 offen nt lds\n" \
  "s_add_u32 m0, s86, 114688\n" \
  "s_mov_b32 s85, 82432\n" \
  "buffer_load_dwordx4 v20, s[60:63], s85 offen nt lds\n" \
  "s_add_u32 m0, s86, 115712\n" \
  "s_mov_b32 s85, 87552\n" \
  "buffer_load_dwordx4 v20, s[60:63], s85 offen nt lds\n" \
  "s_add_u32 m0, s86, 116736\n" \
  "s_mov_b32 s85, 92672\n" \
  "buffer_load_dwordx4 v21, s[60:63], s85 offen nt lds\n" \
  "s_add_u32 m0, s86, 117760\n" \
  "s_mov_b32 s85, 97792\n" \
  "buffer_load_dwordx4 v21, s[60:63], s85 offen nt lds\n" \
  "v_xor_b32 v18, 0x10000, v18\n" \
  "v_xor_b32 v19, 0x10000, v19\n" \
  "v_xor_b32 v22, 0x10000, v22\n" \
  "v_xor_b32 v23, 0x10000, v23\n" \
  "ds_read_b64_tr_b16 a[224:225], v18 offset:0\n" \
  "ds_read_b64_tr_b16 a[226:227], v18 offset:1024\n" \
  "ds_read_b64_tr_b16 a[228:229], v18 offset:8192\n" \
  "ds_read_b64_tr_b16 a[230:231], v18 offset:9216\n" \
  "ds_read_b64_tr_b16 v[240:241], v19 offset:0\n" \
  "ds_read_b64_tr_b16 v[242:243], v19 offset:1024\n" \
  "ds_read_b64_tr_b16 v[244:245], v19 offset:8192\n" \
  "ds_read_b64_tr_b16 v[246:247], v19 offset:9216\n" \
  "ds_read_b64_tr_b16 a[232:233], v18 offset:16384\n" \
  "ds_read_b64_tr_b16 a[234:235], v18 offset:17408\n" \
  "ds_read_b64_tr_b16 a[236:237], v18 offset:24576\n" \
  "ds_read_b64_tr_b16 a[238:239], v18 offset:25600\n" \
  "ds_read_b64_tr_b16 v[248:249], v19 offset:16384\n" \
  "ds_read_b64_tr_b16 v[250:251], v19 offset:17408\n" \
  "ds_read_b64_tr_b16 v[252:253], v19 offset:24576\n" \
  "ds_read_b64_tr_b16 v[254:255], v19 offset:25600\n" \
  "s_waitcnt vmcnt(16) lgkmcnt(0)\n" \
  "s_barrier\n" \
  "s_add_u32 m0, s86, 0\n" \
  "s_mov_b32 s85, 768\n" \
  "buffer_load_dwordx4 v20, s[60:63], s85 offen nt lds\n" \
  "s_add_u32 m0, s86, 1024\n" \
  "s_mov_b32 s85, 5888\n" \
  "buffer_load_dwordx4 v20, s[60:63], s85 offen nt lds\n" \
  "s_add_u32 m0, s86, 2048\n" \
  "s_mov_b32 s85, 11008\n" \
  "buffer_load_dwordx4 v21, s[60:63], s85 offen nt lds\n" \
  "s_add_u32 m0, s86, 3072\n" \
  "s_mov_b32 s85, 16128\n" \
  "buffer_load_dwordx4 v21, s[60:63], s85 offen nt lds\n" \
  "s_add_u32 m0, s86, 16384\n" \
  "s_mov_b32 s85, 82688\n" \
  "buffer_load_dwordx4 v20, s[60:63], s85 offen nt lds\n" \
  "s_add_u32 m0, s86, 17408\n" \
  "s_mov_b32 s85, 87808\n" \
  "buffer_load_dwordx4 v20, s[60:63], s85 offen nt lds\n" \
  "s_add_u32 m0, s86, 18432\n" \
  "s_mov_b32 s85, 92928\n" \
  "buffer_load_dwordx4 v21, s[60:63], s85 offen nt lds\n" \
  "s_add_u32 m0, s86, 19456\n" \
  "s_mov_b32 s85, 98048\n" \
  "buffer_load_dwordx4 v21, s[60:63], s85 offen nt lds\n" \
  "ds_read_b64_tr_b16 a[0:1], v22 offset:32768\n" \
  "ds_read_b64_tr_b16 a[2:3], v22 offset:33792\n" \
  "ds_read_b64_tr_b16 a[4:5], v22 offset:40960\n" \
  "ds_read_b64_tr_b16 a[6:7], v22 offset:41984\n" \
  "ds_read_b64_tr_b16 a[16:17], v23 offset:32768\n" \
  "ds_read_b64_tr_b16 a[18:19], v23 offset:33792\n" \
  "ds_read_b64_tr_b16 a[20:21], v23 offset:40960\n" \
  "ds_read_b64_tr_b16 a[22:23], v23 offset:41984\n" \
  "ds_read_b64_tr_b16 a[8:9], v22 offset:49152\n" \
  "ds_read_b64_tr_b16 a[10:11], v22 offset:50176\n" \
  "ds_read_b64_tr_b16 a[12:13], v22 offset:57344\n" \
  "ds_read_b64_tr_b16 a[14:15], v22 offset:58368\n" \
  "ds_read_b64_tr_b16 a[24:25], v23 offset:49152\n" \
  "ds_read_b64_tr_b16 a[26:27], v23 offset:50176\n" \
  "ds_read_b64_tr_b16 a[28:29], v23 offset:57344\n" \
  "ds_read_b64_tr_b16 a[30:31], v23 offset:58368\n" \
  "s_waitcnt vmcnt(16) lgkmcnt(0)\n" \
  "s_barrier\n" \
  "s_add_u32 m0, s86, 32768\n" \
  "s_mov_b32 s85, 1024\n" \
  "buffer_load_dwordx4 v20, s[60:63], s85 offen nt lds\n" \
  "s_add_u32 m0, s86, 33792\n" \
  "s_mov_b32 s85, 6144\n" \
  "buffer_load_dwordx4 v20, s[60:63], s85 offen nt lds\n" \
  "s_add_u32 m0, s86, 34816\n" \
  "s_mov_b32 s85, 11264\n" \
  "buffer_load_dwordx4 v21, s[60:63], s85 offen nt lds\n" \
  "s_add_u32 m0, s86, 35840\n" \
  "s_mov_b32 s85, 16384\n" \
  "buffer_load_dwordx4 v21, s[60:63], s85 offen nt lds\n" \
  "s_add_u32 m0, s86, 49152\n" \
  "s_mov_b32 s85, 82944\n" \
  "buffer_load_dwordx4 v20, s[60:63], s85 offen nt lds\n" \
  "s_add_u32 m0, s86, 50176\n" \
  "s_mov_b32 s85, 88064\n" \
  "buffer_load_dwordx4 v20, s[60:63], s85 offen nt lds\n" \
  "s_add_u32 m0, s86, 51200\n" \
  "s_mov_b32 s85, 93184\n" \
  "buffer_load_dwordx4 v21, s[60:63], s85 offen nt lds\n" \
  "s_add_u32 m0, s86, 52224\n" \
  "s_mov_b32 s85, 98304\n" \
  "buffer_load_dwordx4 v21, s[60:63], s85 offen nt lds\n" \
  "v_xor_b32 v18, 0x10000, v18\n" \
  "v_xor_b32 v19, 0x10000, v19\n" \
  "v_xor_b32 v22, 0x10000, v22\n" \
  "v_xor_b32 v23, 0x10000, v23\n" \
  "ds_read_b64_tr_b16 a[32:33], v22 offset:0\n" \
  "ds_read_b64_tr_b16 a[34:35], v22 offset:1024\n" \
  "ds_read_b64_tr_b16 a[36:37], v22 offset:8192\n" \
  "ds_read_b64_tr_b16 a[38:39], v22 offset:9216\n" \
  "ds_read_b64_tr_b16 a[48:49], v23 offset:0\n" \
  "ds_read_b64_tr_b16 a[50:51], v23 offset:1024\n" \
  "ds_read_b64_tr_b16 a[52:53], v23 offset:8192\n" \
  "ds_read_b64_tr_b16 a[54:55], v23 offset:9216\n" \
  "ds_read_b64_tr_b16 a[40:41], v22 offset:16384\n" \
  "ds_read_b64_tr_b16 a[42:43], v22 offset:17408\n" \
  "ds_read_b64_tr_b16 a[44:45], v22 offset:24576\n" \
  "ds_read_b64_tr_b16 a[46:47], v22 offset:25600\n" \
  "ds_read_b64_tr_b16 a[56:57], v23 offset:16384\n" \
  "ds_read_b64_tr_b16 a[58:59], v23 offset:17408\n" \
  "ds_read_b64_tr_b16 a[60:61], v23 offset:24576\n" \
  "ds_read_b64_tr_b16 a[62:63], v23 offset:25600\n" \
  "s_waitcnt vmcnt(16) lgkmcnt(0)\n" \
  "s_barrier\n" \
  "ds_read_b64_tr_b16 a[64:65], v22 offset:32768\n" \
  "ds_read_b64_tr_b16 a[66:67], v22 offset:33792\n" \
  "ds_read_b64_tr_b16 a[68:69], v22 offset:40960\n" \
  "ds_read_b64_tr_b16 a[70:71], v22 offset:41984\n" \
  "ds_read_b64_tr_b16 a[80:81], v23 offset:32768\n" \
  "ds_read_b64_tr_b16 a[82:83], v23 offset:33792\n" \
  "ds_read_b64_tr_b16 a[84:85], v23 offset:40960\n" \
  "ds_read_b64_tr_b16 a[86:87], v23 offset:41984\n" \
  "ds_read_b64_tr_b16 a[72:73], v22 offset:49152\n" \
  "ds_read_b64_tr_b16 a[74:75], v22 offset:50176\n" \
  "ds_read_b64_tr_b16 a[76:77], v22 offset:57344\n" \
  "ds_read_b64_tr_b16 a[78:79], v22 offset:58368\n" \
  "ds_read_b64_tr_b16 a[88:89], v23 offset:49152\n" \
  "ds_read_b64_tr_b16 a[90:91], v23 offset:50176\n" \
  "ds_read_b64_tr_b16 a[92:93], v23 offset:57344\n" \
  "ds_read_b64_tr_b16 a[94:95], v23 offset:58368\n" \
  "s_waitcnt vmcnt(8) lgkmcnt(0)\n" \
  "s_barrier\n" \
  "v_xor_b32 v18, 0x10000, v18\n" \
  "v_xor_b32 v19, 0x10000, v19\n" \
  "v_xor_b32 v22, 0x10000, v22\n" \
  "v_xor_b32 v23, 0x10000, v23\n" \
  "ds_read_b64_tr_b16 a[96:97], v22 offset:0\n" \
  "ds_read_b64_tr_b16 a[98:99], v22 offset:1024\n" \
  "ds_read_b64_tr_b16 a[100:101], v22 offset:8192\n" \
  "ds_read_b64_tr_b16 a[102:103], v22 offset:9216\n" \
  "ds_read_b64_tr_b16 a[112:113], v23 offset:0\n" \
  "ds_read_b64_tr_b16 a[114:115], v23 offset:1024\n" \
  "ds_read_b64_tr_b16 a[116:117], v23 offset:8192\n" \
  "ds_read_b64_tr_b16 a[118:119], v23 offset:9216\n" \
  "ds_read_b64_tr_b16 a[104:105], v22 offset:16384\n" \
  "ds_read_b64_tr_b16 a[106:107], v22 offset:17408\n" \
  "ds_read_b64_tr_b16 a[108:109], v22 offset:24576\n" \
  "ds_read_b64_tr_b16 a[110:111], v22 offset:25600\n" \
  "ds_read_b64_tr_b16 a[120:121], v23 offset:16384\n" \
  "ds_read_b64_tr_b16 a[122:123], v23 offset:17408\n" \
  "ds_read_b64_tr_b16 a[124:125], v23 offset:24576\n" \
  "ds_read_b64_tr_b16 a[126:127], v23 offset:25600\n" \
  "s_waitcnt vmcnt(0) lgkmcnt(0)\n" \
  "s_barrier\n" \
  "ds_read_b64_tr_b16 a[128:129], v22 offset:32768\n" \
  "ds_read_b64_tr_b16 a[130:131], v22 offset:33792\n" \
  "ds_read_b64_tr_b16 a[132:133], v22 offset:40960\n" \
  "ds_read_b64_tr_b16 a[134:135], v22 offset:41984\n" \
  "ds_read_b64_tr_b16 a[144:145], v23 offset:32768\n" \
  "ds_read_b64_tr_b16 a[146:147], v23 offset:33792\n" \
  "ds_read_b64_tr_b16 a[148:149], v23 offset:40960\n" \
  "ds_read_b64_tr_b16 a[150:151], v23 offset:41984\n" \
  "ds_read_b64_tr_b16 a[136:137], v22 offset:49152\n" \
  "ds_read_b64_tr_b16 a[138:139], v22 offset:50176\n" \
  "ds_read_b64_tr_b16 a[140:141], v22 offset:57344\n" \
  "ds_read_b64_tr_b16 a[142:143], v22 offset:58368\n" \
  "ds_read_b64_tr_b16 a[152:153], v23 offset:49152\n" \
  "ds_read_b64_tr_b16 a[154:155], v23 offset:50176\n" \
  "ds_read_b64_tr_b16 a[156:157], v23 offset:57344\n" \
  "ds_read_b64_tr_b16 a[158:159], v23 offset:58368\n" \
  "s_waitcnt lgkmcnt(0)\n" \
  "s_barrier\n" \
  "s_mov_b32 s55, 0x10000\n" \
  "s_mov_b64 exec, 1\n" \
  "LW%=:\n" \
  "buffer_load_dword v191, off, s[36:39], s65 offset:2048 sc1\n" \
  "s_sub_u32 s55, s55, 1\n" \
  "s_cbranch_scc1 LX%=\n" \
  "s_waitcnt vmcnt(0)\n" \
  "v_readfirstlane_b32 s85, v191\n" \
  "s_cmp_lg_u32 s85, -1\n" \
  "s_cbranch_scc1 LX%=\n" \
  "s_sleep 3\n" \
  "s_branch LW%=\n" \
  "LX%=:\n" \
  "s_waitcnt vmcnt(0)\n" \
  "s_mov_b64 exec, -1\n" \
  "s_mov_b32 s55, 0x10000\n" \
  "s_lshl_b32 s85, s64, 10\n" \
  "s_add_u32 m0, s85, 16384\n" \
  "s_nop 0\n" \
  "buffer_load_dwordx4 v0, s[44:47], s50 offen sc1 lds\n" \
  "s_waitcnt vmcnt(0)\n" \
  "s_barrier\n" \
  "s_mov_b32 s55, 0x40000\n" \
  "s_mul_i32 s78, s64, 7168\n" \
  "s_add_u32 s78, s78, 32768\n" \
  "s_mov_b32 s91, 0xbfb8aa3b\n" \
  "s_mov_b32 s80, 0\n" \
  "ds_read_b128 v[32:35], v3 offset:0\n" \
  "ds_read_b128 v[36:39], v3 offset:64\n" \
  "ds_read_b128 v[40:43], v3 offset:128\n" \
  "ds_read_b128 v[44:47], v3 offset:192\n" \
  "s_waitcnt lgkmcnt(3)\n" \
  "v_mfma_f32_16x16x32_bf16 v[24:27], a[160:163], v[32:35], 0\n" \
  "ds_read_b128 v[48:51], v3 offset:256\n" \
  "v_mfma_f32_16x16x32_bf16 v[28:31], a[240:243], v[32:35], 0\n" \
  "s_waitcnt lgkmcnt(3)\n" \
  "v_mfma_f32_16x16x32_bf16 v[24:27], a[164:167], v[36:39], v[24:27]\n" \
  "ds_read_b128 v[32:35], v3 offset:320\n" \
  "v_mfma_f32_16x16x32_bf16 v[28:31], a[244:247], v[36:39], v[28:31]\n" \
  "s_waitcnt lgkmcnt(3)\n" \
  "v_mfma_f32_16x16x32_bf16 v[24:27], a[168:171], v[40:43], v[24:27]\n" \
  "ds_read_b128 v[36:39], v3 offset:384\n" \
  "v_mfma_f32_16x16x32_bf16 v[28:31], a[248:251], v[40:43], v[28:31]\n" \
  "s_waitcnt lgkmcnt(3)\n" \
  "v_mfma_f32_16x16x32_bf16 v[24:27], a[172:175], v[44:47], v[24:27]\n" \
  "ds_read_b128 v[40:43], v3 offset:448\n" \
  "v_mfma_f32_16x16x32_bf16 v[28:31], a[252:255], v[44:47], v[28:31]\n" \
  "s_waitcnt lgkmcnt(3)\n" \
  "v_mfma_f32_16x16x32_bf16 v[24:27], a[176:179], v[48:51], v[24:27]\n" \
  "ds_read_b128 v[44:47], v3 offset:512\n" \
  "v_mfma_f32_16x16x32_bf16 v[28:31], v[192:195], v[48:51], v[28:31]\n" \
  "s_waitcnt lgkmcnt(3)\n" \
  "v_mfma_f32_16x16x32_bf16 v[24:27], a[180:183], v[32:35], v[24:27]\n" \
  "ds_read_b128 v[48:51], v3 offset:576\n" \
  "v_mfma_f32_16x16x32_bf16 v[28:31], v[196:199], v[32:35], v[28:31]\n" \
  "s_waitcnt lgkmcnt(3)\n" \
  "v_mfma_f32_16x16x32_bf16 v[24:27], a[184:187], v[36:39], v[24:27]\n" \
  "ds_read_b128 v[32:35], v3 offset:640\n" \
  "v_mfma_f32_16x16x32_bf16 v[28:31], v[200:203], v[36:39], v[28:31]\n" \
  "s_waitcnt lgkmcnt(3)\n" \
  "v_mfma_f32_16x16x32_bf16 v[24:27], a[188:191], v[40:43], v[24:27]\n" \
  "ds_read_b128 v[36:39], v3 offset:704\n" \
  "v_mfma_f32_16x16x32_bf16 v[28:31], v[204:207], v[40:43], v[28:31]\n" \
  "s_waitcnt lgkmcnt(3)\n" \
  "v_mfma_f32_16x16x32_bf16 v[24:27], a[192:195], v[44:47], v[24:27]\n" \
  "ds_read_b128 v[40:43], v3 offset:768\n" \
  "v_mfma_f32_16x16x32_bf16 v[28:31], v[208:211], v[44:47], v[28:31]\n" \
  "s_waitcnt lgkmcnt(3)\n" \
  "v_mfma_f32_16x16x32_bf16 v[24:27], a[196:199], v[48:51], v[24:27]\n" \
  "ds_read_b128 v[44:47], v3 offset:832\n" \
  "v_mfma_f32_16x16x32_bf16 v[28:31], v[212:215], v[48:51], v[28:31]\n" \
  "s_waitcnt lgkmcnt(3)\n" \
  "v_mfma_f32_16x16x32_bf16 v[24:27], a[200:203], v[32:35], v[24:27]\n" \
  "ds_read_b128 v[48:51], v3 offset:896\n" \
  "v_mfma_f32_16x16x32_bf16 v[28:31], v[216:219], v[32:35], v[28:31]\n" \
  "s_waitcnt lgkmcnt(3)\n" \
  "v_mfma_f32_16x16x32_bf16 v[24:27], a[204:207], v[36:39], v[24:27]\n" \
  "ds_read_b128 v[32:35], v3 offset:960\n" \
  "v_mfma_f32_16x16x32_bf16 v[28:31], v[220:223], v[36:39], v[28:31]\n" \
  "s_waitcnt lgkmcnt(3)\n" \
  "v_mfma_f32_16x16x32_bf16 v[24:27], a[208:211], v[40:43], v[24:27]\n" \
  "ds_read_b128 v[36:39], v3 offset:1024\n" \
  "v_mfma_f32_16x16x32_bf16 v[28:31], v[224:227], v[40:43], v[28:31]\n" \
  "s_waitcnt lgkmcnt(3)\n" \
  "v_mfma_f32_16x16x32_bf16 v[24:27], a[212:215], v[44:47], v[24:27]\n" \
  "ds_read_b128 v[40:43], v3 offset:1088\n" \
  "v_mfma_f32_16x16x32_bf16 v[28:31], v[228:231], v[44:47], v[28:31]\n" \
  "s_waitcnt lgkmcnt(3)\n" \
  "v_mfma_f32_16x16x32_bf16 v[24:27], a[216:219], v[48:51], v[24:27]\n" \
  "ds_read_b128 v[44:47], v3 offset:1152\n" \
  "v_mfma_f32_16x16x32_bf16 v[28:31], v[232:235], v[48:51], v[28:31]\n" \
  "s_waitcnt lgkmcnt(3)\n" \
  "v_mfma_f32_16x16x32_bf16 v[24:27], a[220:223], v[32:35], v[24:27]\n" \
  "ds_read_b128 v[48:51], v3 offset:1216\n" \
  "v_mfma_f32_16x16x32_bf16 v[28:31], v[236:239], v[32:35], v[28:31]\n" \
  "s_waitcnt lgkmcnt(3)\n" \
  "v_mfma_f32_16x16x32_bf16 v[24:27], a[224:227], v[36:39], v[24:27]\n" \
  "ds_read_b128 v[32:35], v3 offset:1280\n" \
  "v_mfma_f32_16x16x32_bf16 v[28:31], v[240:243], v[36:39], v[28:31]\n" \
  "s_waitcnt lgkmcnt(3)\n" \
  "v_mfma_f32_16x16x32_bf16 v[24:27], a[228:231], v[40:43], v[24:27]\n" \
  "ds_read_b128 v[36:39], v3 offset:1344\n" \
  "v_mfma_f32_16x16x32_bf16 v[28:31], v[244:247], v[40:43], v[28:31]\n" \
  "s_waitcnt lgkmcnt(3)\n" \
  "v_mfma_f32_16x16x32_bf16 v[24:27], a[232:235], v[44:47], v[24:27]\n" \
  "ds_read_b128 v[40:43], v3 offset:1408\n" \
  "v_mfma_f32_16x16x32_bf16 v[28:31], v[248:251], v[44:47], v[28:31]\n" \
  "s_waitcnt lgkmcnt(3)\n" \
  "v_mfma_f32_16x16x32_bf16 v[24:27], a[236:239], v[48:51], v[24:27]\n" \
  "ds_read_b128 v[44:47], v3 offset:1472\n" \
  "v_mfma_f32_16x16x32_bf16 v[28:31], v[252:255], v[48:51], v[28:31]\n" \
  "s_waitcnt lgkmcnt(3)\n" \
  "v_mfma_f32_16x16x32_bf16 v[16:19], a[160:163], v[32:35], 0\n" \
  "ds_read_b128 v[48:51], v3 offset:1536\n" \
  "v_mfma_f32_16x16x32_bf16 v[20:23], a[240:243], v[32:35], 0\n" \
  "s_waitcnt lgkmcnt(3)\n" \
  "v_mfma_f32_16x16x32_bf16 v[16:19], a[164:167], v[36:39], v[16:19]\n" \
  "ds_read_b128 v[32:35], v3 offset:1600\n" \
  "v_mfma_f32_16x16x32_bf16 v[20:23], a[244:247], v[36:39], v[20:23]\n" \
  "s_waitcnt lgkmcnt(3)\n" \
  "v_mfma_f32_16x16x32_bf16 v[16:19], a[168:171], v[40:43], v[16:19]\n" \
  "ds_read_b128 v[36:39], v3 offset:1664\n" \
  "v_mfma_f32_16x16x32_bf16 v[20:23], a[248:251], v[40:43], v[20:23]\n" \
  "s_waitcnt lgkmcnt(3)\n" \
  "v_mfma_f32_16x16x32_bf16 v[16:19], a[172:175], v[44:47], v[16:19]\n" \
  "ds_read_b128 v[40:43], v3 offset:1728\n" \
  "v_mfma_f32_16x16x32_bf16 v[20:23], a[252:255], v[44:47], v[20:23]\n" \
  "s_waitcnt lgkmcnt(3)\n" \
  "v_mfma_f32_16x16x32_bf16 v[16:19], a[176:179], v[48:51], v[16:19]\n" \
  "ds_read_b128 v[44:47], v3 offset:1792\n" \
  "v_mfma_f32_16x16x32_bf16 v[20:23], v[192:195], v[48:51], v[20:23]\n" \
  "s_waitcnt lgkmcnt(3)\n" \
  "v_mfma_f32_16x16x32_bf16 v[16:19], a[180:183], v[32:35], v[16:19]\n" \
  "ds_read_b128 v[48:51], v3 offset:1856\n" \
  "v_mfma_f32_16x16x32_bf16 v[20:23], v[196:199], v[32:35], v[20:23]\n" \
  "s_waitcnt lgkmcnt(3)\n" \
  "v_mfma_f32_16x16x32_bf16 v[16:19], a[184:187], v[36:39], v[16:19]\n" \
  "ds_read_b128 v[32:35], v3 offset:1920\n" \
  "v_mfma_f32_16x16x32_bf16 v[20:23], v[200:203], v[36:39], v[20:23]\n" \
  "s_waitcnt lgkmcnt(3)\n" \
  "v_mfma_f32_16x16x32_bf16 v[16:19], a[188:191], v[40:43], v[16:19]\n" \
  "ds_read_b128 v[36:39], v3 offset:1984\n" \
  "v_mfma_f32_16x16x32_bf16 v[20:23], v[204:207], v[40:43], v[20:23]\n" \
  "s_waitcnt lgkmcnt(3)\n" \
  "v_mfma_f32_16x16x32_bf16 v[16:19], a[192:195], v[44:47], v[16:19]\n" \
  "ds_read_b128 v[40:43], v3 offset:2048\n" \
  "v_mfma_f32_16x16x32_bf16 v[20:23], v[208:211], v[44:47], v[20:23]\n" \
  "s_waitcnt lgkmcnt(3)\n" \
  "v_mfma_f32_16x16x32_bf16 v[16:19], a[196:199], v[48:51], v[16:19]\n" \
  "ds_read_b128 v[44:47], v3 offset:2112\n" \
  "v_mfma_f32_16x16x32_bf16 v[20:23], v[212:215], v[48:51], v[20:23]\n" \
  "s_waitcnt lgkmcnt(3)\n" \
  "v_mfma_f32_16x16x32_bf16 v[16:19], a[200:203], v[32:35], v[16:19]\n" \
  "ds_read_b128 v[48:51], v3 offset:2176\n" \
  "v_mfma_f32_16x16x32_bf16 v[20:23], v[216:219], v[32:35], v[20:23]\n" \
  "s_waitcnt lgkmcnt(3)\n" \
  "v_mfma_f32_16x16x32_bf16 v[16:19], a[204:207], v[36:39], v[16:19]\n" \
  "ds_read_b128 v[32:35], v3 offset:2240\n" \
  "v_mfma_f32_16x16x32_bf16 v[20:23], v[220:223], v[36:39], v[20:23]\n" \
  "s_waitcnt lgkmcnt(3)\n" \
  "v_mfma_f32_16x16x32_bf16 v[16:19], a[208:211], v[40:43], v[16:19]\n" \
  "ds_read_b128 v[36:39], v3 offset:2304\n" \
  "v_mfma_f32_16x16x32_bf16 v[20:23], v[224:227], v[40:43], v[20:23]\n" \
  "s_waitcnt lgkmcnt(3)\n" \
  "v_mfma_f32_16x16x32_bf16 v[16:19], a[212:215], v[44:47], v[16:19]\n" \
  "ds_read_b128 v[40:43], v3 offset:2368\n" \
  "v_mfma_f32_16x16x32_bf16 v[20:23], v[228:231], v[44:47], v[20:23]\n" \
  "s_waitcnt lgkmcnt(3)\n" \
  "v_mfma_f32_16x16x32_bf16 v[16:19], a[216:219], v[48:51], v[16:19]\n" \
  "ds_read_b128 v[44:47], v3 offset:2432\n" \
  "v_mfma_f32_16x16x32_bf16 v[20:23], v[232:235], v[48:51], v[20:23]\n" \
  "s_waitcnt lgkmcnt(3)\n" \
  "v_mfma_f32_16x16x32_bf16 v[16:19], a[220:223], v[32:35], v[16:19]\n" \
  "ds_read_b128 v[48:51], v3 offset:2496\n" \
  "v_mfma_f32_16x16x32_bf16 v[20:23], v[236:239], v[32:35], v[20:23]\n" \
  "s_waitcnt lgkmcnt(3)\n" \
  "v_mfma_f32_16x16x32_bf16 v[16:19], a[224:227], v[36:39], v[16:19]\n" \
  "v_mfma_f32_16x16x32_bf16 v[20:23], v[240:243], v[36:39], v[20:23]\n" \
  "s_waitcnt lgkmcnt(2)\n" \
  "v_mfma_f32_16x16x32_bf16 v[16:19], a[228:231], v[40:43], v[16:19]\n" \
  "v_mfma_f32_16x16x32_bf16 v[20:23], v[244:247], v[40:43], v[20:23]\n" \
  "s_waitcnt lgkmcnt(1)\n" \
  "v_mfma_f32_16x16x32_bf16 v[16:19], a[232:235], v[44:47], v[16:19]\n" \
  "v_mfma_f32_16x16x32_bf16 v[20:23], v[248:251], v[44:47], v[20:23]\n" \
  "s_waitcnt lgkmcnt(0)\n" \
  "v_mfma_f32_16x16x32_bf16 v[16:19], a[236:239], v[48:51], v[16:19]\n" \
  "v_mfma_f32_16x16x32_bf16 v[20:23], v[252:255], v[48:51], v[20:23]\n" \
  "s_waitcnt lgkmcnt(0)\n" \
  "s_memtime s[20:21]\n" \
  "s_memrealtime s[22:23]\n" \
  "s_mov_b32 s24, 0\n" \
  "s_waitcnt lgkmcnt(0)\n" \
  "WAITX%=:\n" \
  "buffer_load_dword v191, v9, s[36:39], 0 offen sc1\n" \
  "s_waitcnt vmcnt(0)\n" \
  "WAITY%=:\n" \
  "s_cmp_lg_u32 s64, 0\n" \
  "s_cbranch_scc1 WB%=\n" \
  "v_cmp_gt_i32 vcc, s53, v191\n" \
  "s_cbranch_vccz NCx%=\n" \
  "NBx%=:\n" \
  "s_cmp_eq_u32 s55, 0\n" \
  "s_cbranch_scc1 NCx%=\n" \
  "s_sub_u32 s55, s55, 1\n" \
  "s_sleep 3\n" \
  "buffer_load_dword v191, v9, s[36:39], 0 offen sc1\n" \
  "s_waitcnt vmcnt(0)\n" \
  "v_cmp_gt_i32 vcc, s53, v191\n" \
  "s_cbranch_vccnz NBx%=\n" \
  "NCx%=:\n" \
  "WB%=:\n" \
  "s_barrier\n" \
  "s_add_u32 m0, s78, 0\n" \
  "s_add_u32 s87, s48, 0\n" \
  "buffer_load_dwordx4 v0, s[44:47], s87 offen sc1 lds\n" \
  "buffer_load_dwordx4 v0, s[44:47], s87 offen offset:1024 sc1 lds\n" \
  "buffer_load_dwordx4 v0, s[44:47], s87 offen offset:2048 sc1 lds\n" \
  "buffer_load_dwordx4 v0, s[44:47], s87 offen offset:3072 sc1 lds\n" \
  "s_add_u32 m0, s78, 4096\n" \
  "s_add_u32 s87, s48, 4096\n" \
  "buffer_load_dwordx4 v0, s[44:47], s87 offen sc1 lds\n" \
  "buffer_load_dwordx4 v0, s[44:47], s87 offen offset:1024 sc1 lds\n" \
  "buffer_load_dwordx4 v0, s[44:47], s87 offen offset:2048 sc1 lds\n" \
  "s_brev_b32 s30, 1\n" \
  "s_waitcnt vmcnt(0)\n" \
  "LOOP%=:\n" \
  "s_barrier\n" \
  "ds_read_b128 v[32:35], v1 offset:0\n" \
  "ds_read_b128 v[36:39], v1 offset:1024\n" \
  "ds_read_b128 v[40:43], v1 offset:2048\n" \
  "ds_read_b128 v[44:47], v1 offset:20480\n" \
  "ds_read_b128 v[48:51], v1 offset:21504\n" \
  "ds_read_b128 v[52:55], v1 offset:22528\n" \
  "ds_read_b128 v[56:59], v1 offset:23552\n" \
  "ds_read_b128 v[60:63], v1 offset:24576\n" \
  "ds_read_b128 v[64:67], v1 offset:3072\n" \
  "ds_read_b128 v[68:71], v1 offset:4096\n" \
  "ds_read_b128 v[72:75], v1 offset:5120\n" \
  "s_add_u32 s48, s48, 57344\n" \
  "s_add_u32 s80, s80, 1\n" \
  "s_and_b32 s85, s80, 3\n" \
  "s_cselect_b32 s86, 1, -3\n" \
  "s_cselect_b32 s85, 0, 4\n" \
  "s_lshl_b32 s86, s86, 11\n" \
  "v_add_u32 v9, s86, v9\n" \
  "s_add_u32 s53, s53, s85\n" \
  "buffer_load_dword v191, v9, s[36:39], 0 offen sc1\n" \
  "s_waitcnt lgkmcnt(10)\n" \
  "v_mfma_f32_16x16x32_bf16 v[76:79], a[160:163], v[32:35], 0\n" \
  "ds_read_b128 v[80:83], v1 offset:6144\n" \
  "v_mfma_f32_16x16x32_bf16 v[84:87], a[240:243], v[32:35], 0\n" \
  "s_waitcnt lgkmcnt(10)\n" \
  "v_mfma_f32_16x16x32_bf16 v[76:79], a[164:167], v[36:39], v[76:79]\n" \
  "ds_read_b128 v[32:35], v1 offset:7168\n" \
  "v_mfma_f32_16x16x32_bf16 v[84:87], a[244:247], v[36:39], v[84:87]\n" \
  "s_waitcnt lgkmcnt(8)\n" \
  "v_add_f32 v44, v44, v48\n" \
  "v_add_f32 v45, v45, v49\n" \
  "v_mfma_f32_16x16x32_bf16 v[76:79], a[168:171], v[40:43], v[76:79]\n" \
  "ds_read_b128 v[36:39], v1 offset:8192\n" \
  "v_add_f32 v46, v46, v50\n" \
  "v_add_f32 v47, v47, v51\n" \
  "v_mfma_f32_16x16x32_bf16 v[84:87], a[248:251], v[40:43], v[84:87]\n" \
  "s_waitcnt lgkmcnt(8)\n" \
  "v_add_f32 v44, v44, v52\n" \
  "v_add_f32 v45, v45, v53\n" \
  "s_waitcnt lgkmcnt(5)\n" \
  "v_mfma_f32_16x16x32_bf16 v[76:79], a[172:175], v[64:67], v[76:79]\n" \
  "ds_read_b128 v[40:43], v1 offset:9216\n" \
  "v_add_f32 v46, v46, v54\n" \
  "v_add_f32 v47, v47, v55\n" \
  "v_mfma_f32_16x16x32_bf16 v[84:87], a[252:255], v[64:67], v[84:87]\n" \
  "v_add_f32 v44, v44, v56\n" \
  "v_add_f32 v45, v45, v57\n" \
  "s_waitcnt lgkmcnt(5)\n" \
  "v_mfma_f32_16x16x32_bf16 v[76:79], a[176:179], v[68:71], v[76:79]\n" \
  "ds_read_b128 v[48:51], v1 offset:10240\n" \
  "v_add_f32 v46, v46, v58\n" \
  "v_add_f32 v47, v47, v59\n" \
  "v_mfma_f32_16x16x32_bf16 v[84:87], v[192:195], v[68:71], v[84:87]\n" \
  "v_add_f32 v44, v44, v60\n" \
  "v_add_f32 v45, v45, v61\n" \
  "s_waitcnt lgkmcnt(5)\n" \
  "v_mfma_f32_16x16x32_bf16 v[76:79], a[180:183], v[72:75], v[76:79]\n" \
  "ds_read_b128 v[52:55], v1 offset:11264\n" \
  "v_add_f32 v46, v46, v62\n" \
  "v_add_f32 v47, v47, v63\n" \
  "v_mfma_f32_16x16x32_bf16 v[84:87], v[196:199], v[72:75], v[84:87]\n" \
  "v_add_f32 v44, v44, v46\n" \
  "v_add_f32 v45, v45, v47\n" \
  "s_waitcnt lgkmcnt(5)\n" \
  "v_mfma_f32_16x16x32_bf16 v[76:79], a[184:187], v[80:83], v[76:79]\n" \
  "ds_read_b128 v[56:59], v1 offset:12288\n" \
  "v_mov_b32 v190, v44\n" \
  "s_nop 0\n" \
  "v_mfma_f32_16x16x32_bf16 v[84:87], v[200:203], v[80:83], v[84:87]\n" \
  "v_permlane32_swap_b32 v44, v190\n" \
  "v_add_f32 v44, v44, v190\n" \
  "s_waitcnt lgkmcnt(5)\n" \
  "v_mfma_f32_16x16x32_bf16 v[76:79], a[188:191], v[32:35], v[76:79]\n" \
  "ds_read_b128 v[60:63], v1 offset:13312\n" \
  "v_mov_b32 v190, v44\n" \
  "s_nop 0\n" \
  "v_mfma_f32_16x16x32_bf16 v[84:87], v[204:207], v[32:35], v[84:87]\n" \
  "v_permlane16_swap_b32 v44, v190\n" \
  "v_add_f32 v44, v44, v190\n" \
  "s_waitcnt lgkmcnt(5)\n" \
  "v_mfma_f32_16x16x32_bf16 v[76:79], a[192:195], v[36:39], v[76:79]\n" \
  "ds_read_b128 v[32:35], v1 offset:14336\n" \
  "v_mov_b32 v190, v45\n" \
  "s_nop 0\n" \
  "v_mfma_f32_16x16x32_bf16 v[84:87], v[208:211], v[36:39], v[84:87]\n" \
  "v_permlane32_swap_b32 v45, v190\n" \
  "v_add_f32 v45, v45, v190\n" \
  "s_waitcnt lgkmcnt(5)\n" \
  "v_mfma_f32_16x16x32_bf16 v[76:79], a[196:199], v[40:43], v[76:79]\n" \
  "ds_read_b128 v[36:39], v1 offset:15360\n" \
  "v_mov_b32 v190, v45\n" \
  "s_nop 0\n" \
  "v_mfma_f32_16x16x32_bf16 v[84:87], v[212:215], v[40:43], v[84:87]\n" \
  "v_permlane16_swap_b32 v45, v190\n" \
  "v_add_f32 v45, v45, v190\n" \
  "s_waitcnt lgkmcnt(5)\n" \
  "v_mfma_f32_16x16x32_bf16 v[76:79], a[200:203], v[48:51], v[76:79]\n" \
  "ds_read_b128 v[40:43], v1 offset:16384\n" \
  "v_mul_f32 v44, 0x3acccccd, v44\n" \
  "v_mul_f32 v45, 0x3acccccd, v45\n" \
  "v_mfma_f32_16x16x32_bf16 v[84:87], v[216:219], v[48:51], v[84:87]\n" \
  "v_fma_f32 v45, -v44, v44, v45\n" \
  "v_max_f32 v45, 0, v45\n" \
  "s_waitcnt lgkmcnt(5)\n" \
  "v_mfma_f32_16x16x32_bf16 v[76:79], a[204:207], v[52:55], v[76:79]\n" \
  "ds_read_b128 v[48:51], v1 offset:17408\n" \
  "v_cvt_pk_bf16_f32 v45, v45, v45\n" \
  "v_and_b32 v45, 0xffff0000, v45\n" \
  "v_mfma_f32_16x16x32_bf16 v[84:87], v[220:223], v[52:55], v[84:87]\n" \
  "v_add_f32 v45, 0x3727c5ac, v45\n" \
  "v_cvt_pk_bf16_f32 v45, v45, v45\n" \
  "s_waitcnt lgkmcnt(5)\n" \
  "v_mfma_f32_16x16x32_bf16 v[76:79], a[208:211], v[56:59], v[76:79]\n" \
  "ds_read_b128 v[52:55], v1 offset:18432\n" \
  "v_and_b32 v45, 0xffff0000, v45\n" \
  "v_sqrt_f32 v45, v45\n" \
  "v_mfma_f32_16x16x32_bf16 v[84:87], v[224:227], v[56:59], v[84:87]\n" \
  "v_cvt_pk_bf16_f32 v45, v45, v45\n" \
  "v_and_b32 v45, 0xffff0000, v45\n" \
  "s_waitcnt lgkmcnt(5)\n" \
  "v_mfma_f32_16x16x32_bf16 v[76:79], a[212:215], v[60:63], v[76:79]\n" \
  "ds_read_b128 v[56:59], v1 offset:19456\n" \
  "v_rcp_f32 v190, v45\n" \
  "v_cvt_pk_bf16_f32 v44, v44, v44\n" \
  "v_mfma_f32_16x16x32_bf16 v[84:87], v[228:231], v[60:63], v[84:87]\n" \
  "v_and_b32 v44, 0xffff0000, v44\n" \
  "v_mul_f32 v44, v190, v44\n" \
  "s_waitcnt lgkmcnt(5)\n" \
  "v_mfma_f32_16x16x32_bf16 v[76:79], a[216:219], v[32:35], v[76:79]\n" \
  "v_fma_f32 v60, -v44, v16, v24\n" \
  "v_fma_f32 v61, -v44, v17, v25\n" \
  "v_mfma_f32_16x16x32_bf16 v[84:87], v[232:235], v[32:35], v[84:87]\n" \
  "v_fma_f32 v62, -v44, v18, v26\n" \
  "v_fma_f32 v63, -v44, v19, v27\n" \
  "s_waitcnt lgkmcnt(4)\n" \
  "v_mfma_f32_16x16x32_bf16 v[76:79], a[220:223], v[36:39], v[76:79]\n" \
  "v_fma_f32 v64, -v44, v20, v28\n" \
  "v_fma_f32 v65, -v44, v21, v29\n" \
  "v_mfma_f32_16x16x32_bf16 v[84:87], v[236:239], v[36:39], v[84:87]\n" \
  "v_fma_f32 v66, -v44, v22, v30\n" \
  "v_fma_f32 v67, -v44, v23, v31\n" \
  "s_waitcnt lgkmcnt(3)\n" \
  "v_mfma_f32_16x16x32_bf16 v[76:79], a[224:227], v[40:43], v[76:79]\n" \
  "v_mfma_f32_16x16x32_bf16 v[84:87], v[240:243], v[40:43], v[84:87]\n" \
  "s_waitcnt lgkmcnt(2)\n" \
  "v_mfma_f32_16x16x32_bf16 v[76:79], a[228:231], v[48:51], v[76:79]\n" \
  "v_mfma_f32_16x16x32_bf16 v[84:87], v[244:247], v[48:51], v[84:87]\n" \
  "s_waitcnt lgkmcnt(1)\n" \
  "v_mfma_f32_16x16x32_bf16 v[76:79], a[232:235], v[52:55], v[76:79]\n" \
  "v_mfma_f32_16x16x32_bf16 v[84:87], v[248:251], v[52:55], v[84:87]\n" \
  "s_waitcnt lgkmcnt(0)\n" \
  "v_mfma_f32_16x16x32_bf16 v[76:79], a[236:239], v[56:59], v[76:79]\n" \
  "v_mfma_f32_16x16x32_bf16 v[84:87], v[252:255], v[56:59], v[84:87]\n" \
  "s_nop 6\n" \
  "v_fma_f32 v76, v190, v76, v60\n" \
  "v_fma_f32 v77, v190, v77, v61\n" \
  "v_fma_f32 v78, v190, v78, v62\n" \
  "v_fma_f32 v79, v190, v79, v63\n" \
  "v_fma_f32 v84, v190, v84, v64\n" \
  "v_fma_f32 v85, v190, v85, v65\n" \
  "v_fma_f32 v86, v190, v86, v66\n" \
  "v_fma_f32 v87, v190, v87, v67\n" \
  "v_mul_f32 v32, s91, v76\n" \
  "v_mul_f32 v33, s91, v77\n" \
  "v_mul_f32 v34, s91, v78\n" \
  "v_mul_f32 v35, s91, v79\n" \
  "v_mul_f32 v36, s91, v84\n" \
  "v_mul_f32 v37, s91, v85\n" \
  "v_mul_f32 v38, s91, v86\n" \
  "v_mul_f32 v39, s91, v87\n" \
  "v_exp_f32 v32, v32\n" \
  "v_exp_f32 v33, v33\n" \
  "v_exp_f32 v34, v34\n" \
  "v_exp_f32 v35, v35\n" \
  "v_exp_f32 v36, v36\n" \
  "v_exp_f32 v37, v37\n" \
  "v_exp_f32 v38, v38\n" \
  "v_exp_f32 v39, v39\n" \
  "v_add_f32 v32, 1.0, v32\n" \
  "v_add_f32 v33, 1.0, v33\n" \
  "v_add_f32 v34, 1.0, v34\n" \
  "v_add_f32 v35, 1.0, v35\n" \
  "v_add_f32 v36, 1.0, v36\n" \
  "v_add_f32 v37, 1.0, v37\n" \
  "v_add_f32 v38, 1.0, v38\n" \
  "v_add_f32 v39, 1.0, v39\n" \
  "v_rcp_f32 v32, v32\n" \
  "v_rcp_f32 v33, v33\n" \
  "v_rcp_f32 v34, v34\n" \
  "v_rcp_f32 v35, v35\n" \
  "v_rcp_f32 v36, v36\n" \
  "v_rcp_f32 v37, v37\n" \
  "v_rcp_f32 v38, v38\n" \
  "v_rcp_f32 v39, v39\n" \
  "v_mul_f32 v76, v76, v32\n" \
  "v_mul_f32 v77, v77, v33\n" \
  "v_mul_f32 v78, v78, v34\n" \
  "v_mul_f32 v79, v79, v35\n" \
  "v_mul_f32 v84, v84, v36\n" \
  "v_mul_f32 v85, v85, v37\n" \
  "v_mul_f32 v86, v86, v38\n" \
  "v_mul_f32 v87, v87, v39\n" \
  "v_cvt_pk_bf16_f32 v32, v76, v77\n" \
  "v_cvt_pk_bf16_f32 v33, v78, v79\n" \
  "v_cvt_pk_bf16_f32 v34, v84, v85\n" \
  "v_cvt_pk_bf16_f32 v35, v86, v87\n" \
  "ds_write_b128 v2, v[32:35]\n" \
  "s_sub_u32 s85, s80, 2\n" \
  "v_mov_b32 v190, s85\n" \
  "s_waitcnt vmcnt(0)\n" \
  "ds_write_b32 v2, v191 offset:8192\n" \
  "s_waitcnt lgkmcnt(0)\n" \
  "s_barrier\n" \
  "ds_read_b32 v191, v0 offset:8192\n" \
  "ds_read_b128 v[32:35], v0 offset:0\n" \
  "ds_read_b128 v[36:39], v0 offset:1024\n" \
  "ds_read_b128 v[40:43], v0 offset:2048\n" \
  "ds_read_b128 v[44:47], v0 offset:3072\n" \
  "s_waitcnt lgkmcnt(4)\n" \
  "v_cmp_gt_i32 vcc, s53, v191\n" \
  "s_cbranch_vccnz ND%=\n" \
  "s_waitcnt lgkmcnt(3)\n" \
  "v_mfma_f32_16x16x32_bf16 v[48:51], a[0:3], v[32:35], 0\n" \
  "s_add_u32 m0, s78, 0\n" \
  "s_add_u32 s87, s48, 0\n" \
  "buffer_load_dwordx4 v0, s[44:47], s87 offen sc1 lds\n" \
  "v_mfma_f32_16x16x32_bf16 v[52:55], a[16:19], v[32:35], 0\n" \
  "s_waitcnt lgkmcnt(2)\n" \
  "v_mfma_f32_16x16x32_bf16 v[48:51], a[4:7], v[36:39], v[48:51]\n" \
  "buffer_load_dwordx4 v0, s[44:47], s87 offen offset:1024 sc1 lds\n" \
  "v_mfma_f32_16x16x32_bf16 v[52:55], a[20:23], v[36:39], v[52:55]\n" \
  "s_waitcnt lgkmcnt(1)\n" \
  "v_mfma_f32_16x16x32_bf16 v[48:51], a[8:11], v[40:43], v[48:51]\n" \
  "buffer_load_dwordx4 v0, s[44:47], s87 offen offset:2048 sc1 lds\n" \
  "v_mfma_f32_16x16x32_bf16 v[52:55], a[24:27], v[40:43], v[52:55]\n" \
  "s_waitcnt lgkmcnt(0)\n" \
  "v_mfma_f32_16x16x32_bf16 v[48:51], a[12:15], v[44:47], v[48:51]\n" \
  "buffer_load_dwordx4 v0, s[44:47], s87 offen offset:3072 sc1 lds\n" \
  "v_mfma_f32_16x16x32_bf16 v[52:55], a[28:31], v[44:47], v[52:55]\n" \
  "v_mfma_f32_16x16x32_bf16 v[56:59], a[32:35], v[32:35], 0\n" \
  "s_add_u32 m0, s78, 4096\n" \
  "s_add_u32 s87, s48, 4096\n" \
  "buffer_load_dwordx4 v0, s[44:47], s87 offen sc1 lds\n" \
  "v_mfma_f32_16x16x32_bf16 v[60:63], a[48:51], v[32:35], 0\n" \
  "v_mfma_f32_16x16x32_bf16 v[56:59], a[36:39], v[36:39], v[56:59]\n" \
  "buffer_load_dwordx4 v0, s[44:47], s87 offen offset:1024 sc1 lds\n" \
  "v_mfma_f32_16x16x32_bf16 v[60:63], a[52:55], v[36:39], v[60:63]\n" \
  "v_mfma_f32_16x16x32_bf16 v[56:59], a[40:43], v[40:43], v[56:59]\n" \
  "buffer_load_dwordx4 v0, s[44:47], s87 offen offset:2048 sc1 lds\n" \
  "s_mov_b64 exec, 1\n" \
  "buffer_store_dword v190, off, s[36:39], s30 sc1\n" \
  "s_mov_b64 exec, -1\n" \
  "v_mfma_f32_16x16x32_bf16 v[60:63], a[56:59], v[40:43], v[60:63]\n" \
  "v_cvt_pk_bf16_f32 v64, v48, v49\n" \
  "v_cvt_pk_bf16_f32 v65, v50, v51\n" \
  "v_mfma_f32_16x16x32_bf16 v[56:59], a[44:47], v[44:47], v[56:59]\n" \
  "v_cvt_pk_bf16_f32 v66, v52, v53\n" \
  "v_cvt_pk_bf16_f32 v67, v54, v55\n" \
  "v_mfma_f32_16x16x32_bf16 v[60:63], a[60:63], v[44:47], v[60:63]\n" \
  "s_add_u32 s85, s49, 0\n" \
  "buffer_store_dwordx4 v[64:67], v0, s[44:47], s85 offen sc1\n" \
  "v_mfma_f32_16x16x32_bf16 v[48:51], a[64:67], v[32:35], 0\n" \
  "v_mfma_f32_16x16x32_bf16 v[52:55], a[80:83], v[32:35], 0\n" \
  "v_mfma_f32_16x16x32_bf16 v[48:51], a[68:71], v[36:39], v[48:51]\n" \
  "v_mfma_f32_16x16x32_bf16 v[52:55], a[84:87], v[36:39], v[52:55]\n" \
  "v_mfma_f32_16x16x32_bf16 v[48:51], a[72:75], v[40:43], v[48:51]\n" \
  "v_mfma_f32_16x16x32_bf16 v[52:55], a[88:91], v[40:43], v[52:55]\n" \
  "v_cvt_pk_bf16_f32 v64, v56, v57\n" \
  "v_cvt_pk_bf16_f32 v65, v58, v59\n" \
  "v_mfma_f32_16x16x32_bf16 v[48:51], a[76:79], v[44:47], v[48:51]\n" \
  "v_cvt_pk_bf16_f32 v66, v60, v61\n" \
  "v_cvt_pk_bf16_f32 v67, v62, v63\n" \
  "v_mfma_f32_16x16x32_bf16 v[52:55], a[92:95], v[44:47], v[52:55]\n" \
  "s_add_u32 s85, s49, 114688\n" \
  "buffer_store_dwordx4 v[64:67], v0, s[44:47], s85 offen sc1\n" \
  "v_mfma_f32_16x16x32_bf16 v[56:59], a[96:99], v[32:35], 0\n" \
  "v_mfma_f32_16x16x32_bf16 v[60:63], a[112:115], v[32:35], 0\n" \
  "v_mfma_f32_16x16x32_bf16 v[56:59], a[100:103], v[36:39], v[56:59]\n" \
  "v_mfma_f32_16x16x32_bf16 v[60:63], a[116:119], v[36:39], v[60:63]\n" \
  "v_mfma_f32_16x16x32_bf16 v[56:59], a[104:107], v[40:43], v[56:59]\n" \
  "v_mfma_f32_16x16x32_bf16 v[60:63], a[120:123], v[40:43], v[60:63]\n" \
  "v_cvt_pk_bf16_f32 v64, v48, v49\n" \
  "v_cvt_pk_bf16_f32 v65, v50, v51\n" \
  "v_mfma_f32_16x16x32_bf16 v[56:59], a[108:111], v[44:47], v[56:59]\n" \
  "v_cvt_pk_bf16_f32 v66, v52, v53\n" \
  "v_cvt_pk_bf16_f32 v67, v54, v55\n" \
  "v_mfma_f32_16x16x32_bf16 v[60:63], a[124:127], v[44:47], v[60:63]\n" \
  "s_add_u32 s85, s49, 229376\n" \
  "buffer_store_dwordx4 v[64:67], v0, s[44:47], s85 offen sc1\n" \
  "v_mfma_f32_16x16x32_bf16 v[48:51], a[128:131], v[32:35], 0\n" \
  "v_mfma_f32_16x16x32_bf16 v[52:55], a[144:147], v[32:35], 0\n" \
  "v_mfma_f32_16x16x32_bf16 v[48:51], a[132:135], v[36:39], v[48:51]\n" \
  "v_mfma_f32_16x16x32_bf16 v[52:55], a[148:151], v[36:39], v[52:55]\n" \
  "v_mfma_f32_16x16x32_bf16 v[48:51], a[136:139], v[40:43], v[48:51]\n" \
  "v_mfma_f32_16x16x32_bf16 v[52:55], a[152:155], v[40:43], v[52:55]\n" \
  "v_cvt_pk_bf16_f32 v64, v56, v57\n" \
  "v_cvt_pk_bf16_f32 v65, v58, v59\n" \
  "v_mfma_f32_16x16x32_bf16 v[48:51], a[140:143], v[44:47], v[48:51]\n" \
  "v_cvt_pk_bf16_f32 v66, v60, v61\n" \
  "v_cvt_pk_bf16_f32 v67, v62, v63\n" \
  "v_mfma_f32_16x16x32_bf16 v[52:55], a[156:159], v[44:47], v[52:55]\n" \
  "s_add_u32 s85, s49, 344064\n" \
  "buffer_store_dwordx4 v[64:67], v0, s[44:47], s85 offen sc1\n" \
  "s_nop 2\n" \
  "v_cvt_pk_bf16_f32 v56, v48, v49\n" \
  "v_cvt_pk_bf16_f32 v57, v50, v51\n" \
  "s_nop 0\n" \
  "v_cvt_pk_bf16_f32 v58, v52, v53\n" \
  "v_cvt_pk_bf16_f32 v59, v54, v55\n" \
  "s_add_u32 s85, s49, 458752\n" \
  "buffer_store_dwordx4 v[56:59], v0, s[44:47], s85 offen sc1\n" \
  "s_add_u32 s49, s49, 0x100000\n" \
  "s_bitset0_b32 s49, 24\n" \
  "s_mov_b32 s30, s52\n" \
  "s_waitcnt vmcnt(6)\n" \
  "s_branch LOOP%=\n" \
  "ND%=:\n" \
  "s_waitcnt lgkmcnt(3)\n" \
  "v_mfma_f32_16x16x32_bf16 v[48:51], a[0:3], v[32:35], 0\n" \
  "s_mov_b64 exec, 1\n" \
  "buffer_store_dword v190, off, s[36:39], s30 sc1\n" \
  "s_mov_b64 exec, -1\n" \
  "v_mfma_f32_16x16x32_bf16 v[52:55], a[16:19], v[32:35], 0\n" \
  "s_waitcnt lgkmcnt(2)\n" \
  "v_mfma_f32_16x16x32_bf16 v[48:51], a[4:7], v[36:39], v[48:51]\n" \
  "v_mfma_f32_16x16x32_bf16 v[52:55], a[20:23], v[36:39], v[52:55]\n" \
  "s_waitcnt lgkmcnt(1)\n" \
  "v_mfma_f32_16x16x32_bf16 v[48:51], a[8:11], v[40:43], v[48:51]\n" \
  "v_mfma_f32_16x16x32_bf16 v[52:55], a[24:27], v[40:43], v[52:55]\n" \
  "s_waitcnt lgkmcnt(0)\n" \
  "v_mfma_f32_16x16x32_bf16 v[48:51], a[12:15], v[44:47], v[48:51]\n" \
  "v_mfma_f32_16x16x32_bf16 v[52:55], a[28:31], v[44:47], v[52:55]\n" \
  "v_mfma_f32_16x16x32_bf16 v[56:59], a[32:35], v[32:35], 0\n" \
  "v_mfma_f32_16x16x32_bf16 v[60:63], a[48:51], v[32:35], 0\n" \
  "v_mfma_f32_16x16x32_bf16 v[56:59], a[36:39], v[36:39], v[56:59]\n" \
  "v_mfma_f32_16x16x32_bf16 v[60:63], a[52:55], v[36:39], v[60:63]\n" \
  "v_mfma_f32_16x16x32_bf16 v[56:59], a[40:43], v[40:43], v[56:59]\n" \
  "v_mfma_f32_16x16x32_bf16 v[60:63], a[56:59], v[40:43], v[60:63]\n" \
  "s_nop 0\n" \
  "v_cvt_pk_bf16_f32 v64, v48, v49\n" \
  "v_cvt_pk_bf16_f32 v65, v50, v51\n" \
  "v_mfma_f32_16x16x32_bf16 v[56:59], a[44:47], v[44:47], v[56:59]\n" \
  "v_cvt_pk_bf16_f32 v66, v52, v53\n" \
  "v_cvt_pk_bf16_f32 v67, v54, v55\n" \
  "v_mfma_f32_16x16x32_bf16 v[60:63], a[60:63], v[44:47], v[60:63]\n" \
  "s_add_u32 s85, s49, 0\n" \
  "buffer_store_dwordx4 v[64:67], v0, s[44:47], s85 offen sc1\n" \
  "v_mfma_f32_16x16x32_bf16 v[48:51], a[64:67], v[32:35], 0\n" \
  "v_mfma_f32_16x16x32_bf16 v[52:55], a[80:83], v[32:35], 0\n" \
  "v_mfma_f32_16x16x32_bf16 v[48:51], a[68:71], v[36:39], v[48:51]\n" \
  "v_mfma_f32_16x16x32_bf16 v[52:55], a[84:87], v[36:39], v[52:55]\n" \
  "v_mfma_f32_16x16x32_bf16 v[48:51], a[72:75], v[40:43], v[48:51]\n" \
  "v_mfma_f32_16x16x32_bf16 v[52:55], a[88:91], v[40:43], v[52:55]\n" \
  "v_cvt_pk_bf16_f32 v64, v56, v57\n" \
  "v_cvt_pk_bf16_f32 v65, v58, v59\n" \
  "v_mfma_f32_16x16x32_bf16 v[48:51], a[76:79], v[44:47], v[48:51]\n" \
  "v_cvt_pk_bf16_f32 v66, v60, v61\n" \
  "v_cvt_pk_bf16_f32 v67, v62, v63\n" \
  "v_mfma_f32_16x16x32_bf16 v[52:55], a[92:95], v[44:47], v[52:55]\n" \
  "s_add_u32 s85, s49, 114688\n" \
  "buffer_store_dwordx4 v[64:67], v0, s[44:47], s85 offen sc1\n" \
  "v_mfma_f32_16x16x32_bf16 v[56:59], a[96:99], v[32:35], 0\n" \
  "v_mfma_f32_16x16x32_bf16 v[60:63], a[112:115], v[32:35], 0\n" \
  "v_mfma_f32_16x16x32_bf16 v[56:59], a[100:103], v[36:39], v[56:59]\n" \
  "v_mfma_f32_16x16x32_bf16 v[60:63], a[116:119], v[36:39], v[60:63]\n" \
  "v_mfma_f32_16x16x32_bf16 v[56:59], a[104:107], v[40:43], v[56:59]\n" \
  "v_mfma_f32_16x16x32_bf16 v[60:63], a[120:123], v[40:43], v[60:63]\n" \
  "v_cvt_pk_bf16_f32 v64, v48, v49\n" \
  "v_cvt_pk_bf16_f32 v65, v50, v51\n" \
  "v_mfma_f32_16x16x32_bf16 v[56:59], a[108:111], v[44:47], v[56:59]\n" \
  "v_cvt_pk_bf16_f32 v66, v52, v53\n" \
  "v_cvt_pk_bf16_f32 v67, v54, v55\n" \
  "v_mfma_f32_16x16x32_bf16 v[60:63], a[124:127], v[44:47], v[60:63]\n" \
  "s_add_u32 s85, s49, 229376\n" \
  "buffer_store_dwordx4 v[64:67], v0, s[44:47], s85 offen sc1\n" \
  "v_mfma_f32_16x16x32_bf16 v[48:51], a[128:131], v[32:35], 0\n" \
  "v_mfma_f32_16x16x32_bf16 v[52:55], a[144:147], v[32:35], 0\n" \
  "v_mfma_f32_16x16x32_bf16 v[48:51], a[132:135], v[36:39], v[48:51]\n" \
  "v_mfma_f32_16x16x32_bf16 v[52:55], a[148:151], v[36:39], v[52:55]\n" \
  "v_mfma_f32_16x16x32_bf16 v[48:51], a[136:139], v[40:43], v[48:51]\n" \
  "v_mfma_f32_16x16x32_bf16 v[52:55], a[152:155], v[40:43], v[52:55]\n" \
  "v_cvt_pk_bf16_f32 v32, v56, v57\n" \
  "v_cvt_pk_bf16_f32 v33, v58, v59\n" \
  "v_mfma_f32_16x16x32_bf16 v[48:51], a[140:143], v[44:47], v[48:51]\n" \
  "v_cvt_pk_bf16_f32 v34, v60, v61\n" \
  "v_cvt_pk_bf16_f32 v35, v62, v63\n" \
  "v_mfma_f32_16x16x32_bf16 v[52:55], a[156:159], v[44:47], v[52:55]\n" \
  "s_add_u32 s85, s49, 344064\n" \
  "buffer_store_dwordx4 v[32:35], v0, s[44:47], s85 offen sc1\n" \
  "s_nop 2\n" \
  "v_cvt_pk_bf16_f32 v32, v48, v49\n" \
  "v_cvt_pk_bf16_f32 v33, v50, v51\n" \
  "s_nop 0\n" \
  "v_cvt_pk_bf16_f32 v34, v52, v53\n" \
  "v_cvt_pk_bf16_f32 v35, v54, v55\n" \
  "s_add_u32 s85, s49, 458752\n" \
  "buffer_store_dwordx4 v[32:35], v0, s[44:47], s85 offen sc1\n" \
  "s_add_u32 s49, s49, 0x100000\n" \
  "s_bitset0_b32 s49, 24\n" \
  "buffer_load_dword v191, v9, s[36:39], 0 offen sc1\n" \
  "v_add_u32 v190, 1, v190\n" \
  "s_waitcnt vmcnt(1)\n" \
  "s_mov_b64 exec, 1\n" \
  "buffer_store_dword v190, off, s[36:39], s52 sc1\n" \
  "s_mov_b64 exec, -1\n" \
  "s_waitcnt vmcnt(1)\n" \
  "s_cmp_lt_u32 s80, 64\n" \
  "s_cbranch_scc1 WAITY%=\n" \
  "DONE%=:\n" \
  "s_waitcnt vmcnt(0) lgkmcnt(0)\n" \
  "s_cmp_eq_u32 s73, -1\n" \
  "s_cbranch_scc1 END%=\n" \
  "s_memrealtime vcc\n" \
  "s_waitcnt lgkmcnt(0)\n" \
  "v_mov_b32 v32, vcc_lo\n" \
  "s_sub_u32 s22, vcc_lo, s22\n" \
  "s_memtime vcc\n" \
  "s_waitcnt lgkmcnt(0)\n" \
  "s_sub_u32 s20, vcc_lo, s20\n" \
  "v_mov_b32 v33, s20\n" \
  "v_mov_b32 v34, s22\n" \
  "v_mov_b32 v35, s24\n" \
  "s_mov_b64 exec, 1\n" \
  "buffer_store_dword v32, off, s[44:47], s73\n" \
  "buffer_store_dword v33, off, s[44:47], s73 offset:1024\n" \
  "buffer_store_dword v34, off, s[44:47], s73 offset:2048\n" \
  "buffer_store_dword v35, off, s[44:47], s73 offset:3072\n" \
  "s_waitcnt vmcnt(0)\n" \
  "END%=:\n" \
  "s_endpgm\n"
#define REC_X_ASM \
  "s_mov_b32 s80, s53\n" \
  "s_mov_b32 s55, 0x40000\n" \
  "s_lshl_b32 s78, s64, 12\n" \
  "s_cmp_lg_u32 s64, 1\n" \
  "s_cbranch_scc1 I0%=\n" \
  "buffer_load_dword v255, v9, s[36:39], 0 offen sc1\n" \
  "s_waitcnt vmcnt(0)\n" \
  "v_cmp_gt_i32 vcc, v6, v255\n" \
  "s_cbranch_vccz NCi%=\n" \
  "NBi%=:\n" \
  "s_cmp_eq_u32 s55, 0\n" \
  "s_cbranch_scc1 NCi%=\n" \
  "s_sub_u32 s55, s55, 1\n" \
  "s_sleep 4\n" \
  "buffer_load_dword v255, v9, s[36:39], 0 offen sc1\n" \
  "s_waitcnt vmcnt(0)\n" \
  "v_cmp_gt_i32 vcc, v6, v255\n" \
  "s_cbranch_vccnz NBi%=\n" \
  "NCi%=:\n" \
  "v_add_u32 v6, v7, v6\n" \
  "I0%=:\n" \
  "s_barrier\n" \
  "s_add_u32 s85, s33, 0\n" \
  "buffer_load_dwordx4 a[0:3], v0, s[44:47], s85 offen offset:0 sc1\n" \
  "s_sleep 3\n" \
  "buffer_load_dwordx4 a[4:7], v0, s[44:47], s85 offen offset:1024 sc1\n" \
  "s_sleep 3\n" \
  "buffer_load_dwordx4 a[8:11], v0, s[44:47], s85 offen offset:2048 sc1\n" \
  "s_sleep 3\n" \
  "buffer_load_dwordx4 a[12:15], v0, s[44:47], s85 offen offset:3072 sc1\n" \
  "s_sleep 3\n" \
  "s_add_u32 s85, s33, 4096\n" \
  "buffer_load_dwordx4 a[16:19], v0, s[44:47], s85 offen offset:0 sc1\n" \
  "s_sleep 3\n" \
  "buffer_load_dwordx4 a[20:23], v0, s[44:47], s85 offen offset:1024 sc1\n" \
  "s_sleep 3\n" \
  "buffer_load_dwordx4 a[24:27], v0, s[44:47], s85 offen offset:2048 sc1\n" \
  "s_sleep 3\n" \
  "buffer_load_dwordx4 a[28:31], v0, s[44:47], s85 offen offset:3072 sc1\n" \
  "s_sleep 3\n" \
  "s_add_u32 s85, s33, 8192\n" \
  "buffer_load_dwordx4 a[32:35], v0, s[44:47], s85 offen offset:0 sc1\n" \
  "s_sleep 3\n" \
  "buffer_load_dwordx4 a[36:39], v0, s[44:47], s85 offen offset:1024 sc1\n" \
  "s_sleep 3\n" \
  "buffer_load_dwordx4 a[40:43], v0, s[44:47], s85 offen offset:2048 sc1\n" \
  "s_sleep 3\n" \
  "buffer_load_dwordx4 a[44:47], v0, s[44:47], s85 offen offset:3072 sc1\n" \
  "s_sleep 3\n" \
  "s_add_u32 s85, s33, 12288\n" \
  "buffer_load_dwordx4 a[48:51], v0, s[44:47], s85 offen offset:0 sc1\n" \
  "s_sleep 3\n" \
  "buffer_load_dwordx4 a[52:55], v0, s[44:47], s85 offen offset:1024 sc1\n" \
  "s_sleep 3\n" \
  "buffer_load_dwordx4 a[56:59], v0, s[44:47], s85 offen offset:2048 sc1\n" \
  "s_sleep 3\n" \
  "buffer_load_dwordx4 a[60:63], v0, s[44:47], s85 offen offset:3072 sc1\n" \
  "s_sleep 3\n" \
  "s_add_u32 s85, s33, 16384\n" \
  "buffer_load_dwordx4 a[64:67], v0, s[44:47], s85 offen offset:0 sc1\n" \
  "s_sleep 3\n" \
  "buffer_load_dwordx4 a[68:71], v0, s[44:47], s85 offen offset:1024 sc1\n" \
  "s_sleep 3\n" \
  "buffer_load_dwordx4 a[72:75], v0, s[44:47], s85 offen offset:2048 sc1\n" \
  "s_sleep 3\n" \
  "buffer_load_dwordx4 a[76:79], v0, s[44:47], s85 offen offset:3072 sc1\n" \
  "s_sleep 3\n" \
  "s_add_u32 s85, s33, 20480\n" \
  "buffer_load_dwordx4 a[80:83], v0, s[44:47], s85 offen offset:0 sc1\n" \
  "s_sleep 3\n" \
  "buffer_load_dwordx4 a[84:87], v0, s[44:47], s85 offen offset:1024 sc1\n" \
  "s_sleep 3\n" \
  "buffer_load_dwordx4 a[88:91], v0, s[44:47], s85 offen offset:2048 sc1\n" \
  "s_sleep 3\n" \
  "buffer_load_dwordx4 a[92:95], v0, s[44:47], s85 offen offset:3072 sc1\n" \
  "s_sleep 3\n" \
  "s_add_u32 s85, s33, 24576\n" \
  "buffer_load_dwordx4 a[96:99], v0, s[44:47], s85 offen offset:0 sc1\n" \
  "s_sleep 3\n" \
  "buffer_load_dwordx4 a[100:103], v0, s[44:47], s85 offen offset:1024 sc1\n" \
  "s_sleep 3\n" \
  "buffer_load_dwordx4 a[104:107], v0, s[44:47], s85 offen offset:2048 sc1\n" \
  "s_sleep 3\n" \
  "buffer_load_dwordx4 a[108:111], v0, s[44:47], s85 offen offset:3072 sc1\n" \
  "s_sleep 3\n" \
  "s_add_u32 s85, s33, 28672\n" \
  "buffer_load_dwordx4 a[112:115], v0, s[44:47], s85 offen offset:0 sc1\n" \
  "s_sleep 3\n" \
  "buffer_load_dwordx4 a[116:119], v0, s[44:47], s85 offen offset:1024 sc1\n" \
  "s_sleep 3\n" \
  "buffer_load_dwordx4 a[120:123], v0, s[44:47], s85 offen offset:2048 sc1\n" \
  "s_sleep 3\n" \
  "buffer_load_dwordx4 a[124:127], v0, s[44:47], s85 offen offset:3072 sc1\n" \
  "s_sleep 3\n" \
  "s_waitcnt lgkmcnt(0)\n" \
  "s_memtime s[20:21]\n" \
  "s_memrealtime s[22:23]\n" \
  "s_mov_b32 s24, 0\n" \
  "s_waitcnt lgkmcnt(0)\n" \
  "LOOP%=:\n" \
  "s_cmp_lt_u32 s80, 60\n" \
  "s_cbranch_scc1 L0%=\n" \
  "v_mov_b32 v9, v8\n" \
  "v_mov_b32 v6, v11\n" \
  "s_add_u32 s49, s49, s54\n" \
  "L0%=:\n" \
  "s_cmp_ge_u32 s64, 2\n" \
  "s_cbranch_scc1 L1%=\n" \
  "buffer_load_dword v255, v9, s[36:39], 0 offen sc1\n" \
  "s_waitcnt vmcnt(0)\n" \
  "v_cmp_gt_i32 vcc, v6, v255\n" \
  "s_cbranch_vccz NCp%=\n" \
  "NBp%=:\n" \
  "s_cmp_eq_u32 s55, 0\n" \
  "s_cbranch_scc1 NCp%=\n" \
  "s_sub_u32 s55, s55, 1\n" \
  "s_sleep 3\n" \
  "buffer_load_dword v255, v9, s[36:39], 0 offen sc1\n" \
  "s_waitcnt vmcnt(0)\n" \
  "v_cmp_gt_i32 vcc, v6, v255\n" \
  "s_cbranch_vccnz NBp%=\n" \
  "NCp%=:\n" \
  "L1%=:\n" \
  "s_barrier\n" \
  "s_add_u32 s86, s49, 4096\n" \
  "s_add_u32 m0, s78, 0\n" \
  "s_add_u32 s85, s48, 0\n" \
  "buffer_load_dwordx4 v0, s[44:47], s85 offen sc1 lds\n" \
  "buffer_load_dwordx4 v0, s[44:47], s85 offen offset:1024 sc1 lds\n" \
  "s_add_u32 m0, s78, 2048\n" \
  "s_add_u32 s85, s48, 2097152\n" \
  "buffer_load_dwordx4 v0, s[44:47], s85 offen sc1 lds\n" \
  "buffer_load_dwordx4 v0, s[44:47], s85 offen offset:1024 sc1 lds\n" \
  "buffer_load_dwordx4 v[24:27], v0, s[44:47], s49 offen offset:0 sc1\n" \
  "buffer_load_dwordx4 v[28:31], v0, s[44:47], s49 offen offset:1024 sc1\n" \
  "s_add_u32 m0, s78, 16384\n" \
  "s_add_u32 s85, s48, 16777216\n" \
  "buffer_load_dwordx4 v0, s[44:47], s85 offen sc1 lds\n" \
  "buffer_load_dwordx4 v0, s[44:47], s85 offen offset:1024 sc1 lds\n" \
  "s_add_u32 m0, s78, 18432\n" \
  "s_add_u32 s85, s48, 18874368\n" \
  "buffer_load_dwordx4 v0, s[44:47], s85 offen sc1 lds\n" \
  "buffer_load_dwordx4 v0, s[44:47], s85 offen offset:1024 sc1 lds\n" \
  "buffer_load_dwordx4 v[32:35], v0, s[44:47], s49 offen offset:2048 sc1\n" \
  "buffer_load_dwordx4 v[36:39], v0, s[44:47], s49 offen offset:3072 sc1\n" \
  "s_add_u32 m0, s78, 32768\n" \
  "s_add_u32 s85, s48, 33554432\n" \
  "buffer_load_dwordx4 v0, s[44:47], s85 offen sc1 lds\n" \
  "buffer_load_dwordx4 v0, s[44:47], s85 offen offset:1024 sc1 lds\n" \
  "s_add_u32 m0, s78, 34816\n" \
  "s_add_u32 s85, s48, 35651584\n" \
  "buffer_load_dwordx4 v0, s[44:47], s85 offen sc1 lds\n" \
  "buffer_load_dwordx4 v0, s[44:47], s85 offen offset:1024 sc1 lds\n" \
  "buffer_load_dwordx4 v[40:43], v0, s[44:47], s86 offen offset:0 sc1\n" \
  "buffer_load_dwordx4 v[44:47], v0, s[44:47], s86 offen offset:1024 sc1\n" \
  "s_add_u32 m0, s78, 49152\n" \
  "s_add_u32 s85, s48, 50331648\n" \
  "buffer_load_dwordx4 v0, s[44:47], s85 offen sc1 lds\n" \
  "buffer_load_dwordx4 v0, s[44:47], s85 offen offset:1024 sc1 lds\n" \
  "s_add_u32 m0, s78, 51200\n" \
  "s_add_u32 s85, s48, 52428800\n" \
  "buffer_load_dwordx4 v0, s[44:47], s85 offen sc1 lds\n" \
  "buffer_load_dwordx4 v0, s[44:47], s85 offen offset:1024 sc1 lds\n" \
  "buffer_load_dwordx4 v[48:51], v0, s[44:47], s86 offen offset:2048 sc1\n" \
  "buffer_load_dwordx4 v[52:55], v0, s[44:47], s86 offen offset:3072 sc1\n" \
  "s_add_u32 s85, s79, 20480\n" \
  "buffer_load_dwordx2 v[56:57], v3, s[56:59], s79 offen\n" \
  "buffer_load_dwordx2 v[58:59], v3, s[56:59], s85 offen\n" \
  "s_waitcnt vmcnt(20)\n" \
  "s_barrier\n" \
  "ds_read_b128 v[60:63], v0 offset:0\n" \
  "ds_read_b64 v[64:65], v1 offset:1024\n" \
  "ds_read_b128 v[68:71], v0 offset:2048\n" \
  "ds_read_b64 v[66:67], v1 offset:3072\n" \
  "ds_read_b128 v[72:75], v0 offset:4096\n" \
  "ds_read_b64 v[76:77], v1 offset:5120\n" \
  "ds_read_b128 v[80:83], v0 offset:6144\n" \
  "ds_read_b64 v[78:79], v1 offset:7168\n" \
  "s_waitcnt lgkmcnt(7)\n" \
  "v_mfma_f32_16x16x32_bf16 v[84:87], a[0:3], v[60:63], 0\n" \
  "ds_read_b128 v[60:63], v0 offset:8192\n" \
  "s_waitcnt lgkmcnt(7)\n" \
  "v_mfma_f32_16x16x16_bf16 v[88:91], a[0:1], v[64:65], 0\n" \
  "ds_read_b64 v[64:65], v1 offset:9216\n" \
  "s_waitcnt lgkmcnt(7)\n" \
  "v_mfma_f32_16x16x32_bf16 v[84:87], a[4:7], v[68:71], v[84:87]\n" \
  "ds_read_b128 v[68:71], v0 offset:10240\n" \
  "s_waitcnt lgkmcnt(7)\n" \
  "v_mfma_f32_16x16x16_bf16 v[88:91], a[4:5], v[66:67], v[88:91]\n" \
  "ds_read_b64 v[66:67], v1 offset:11264\n" \
  "s_waitcnt lgkmcnt(7)\n" \
  "v_mfma_f32_16x16x32_bf16 v[84:87], a[8:11], v[72:75], v[84:87]\n" \
  "ds_read_b128 v[72:75], v0 offset:12288\n" \
  "s_waitcnt lgkmcnt(7)\n" \
  "v_mfma_f32_16x16x16_bf16 v[88:91], a[8:9], v[76:77], v[88:91]\n" \
  "ds_read_b64 v[76:77], v1 offset:13312\n" \
  "s_waitcnt lgkmcnt(7)\n" \
  "v_mfma_f32_16x16x32_bf16 v[84:87], a[12:15], v[80:83], v[84:87]\n" \
  "ds_read_b128 v[80:83], v0 offset:14336\n" \
  "s_waitcnt lgkmcnt(7)\n" \
  "v_mfma_f32_16x16x16_bf16 v[88:91], a[12:13], v[78:79], v[88:91]\n" \
  "ds_read_b64 v[78:79], v1 offset:15360\n" \
  "s_waitcnt lgkmcnt(7)\n" \
  "v_mfma_f32_16x16x32_bf16 v[84:87], a[16:19], v[60:63], v[84:87]\n" \
  "ds_read_b128 v[60:63], v2 offset:0\n" \
  "s_waitcnt lgkmcnt(7)\n" \
  "v_mfma_f32_16x16x16_bf16 v[88:91], a[16:17], v[64:65], v[88:91]\n" \
  "ds_read_b128 v[92:95], v2 offset:256\n" \
  "s_waitcnt lgkmcnt(7)\n" \
  "v_mfma_f32_16x16x32_bf16 v[84:87], a[20:23], v[68:71], v[84:87]\n" \
  "ds_read_b128 v[68:71], v2 offset:8192\n" \
  "s_waitcnt lgkmcnt(7)\n" \
  "v_mfma_f32_16x16x16_bf16 v[88:91], a[20:21], v[66:67], v[88:91]\n" \
  "ds_read_b128 v[64:67], v2 offset:8448\n" \
  "s_waitcnt lgkmcnt(7)\n" \
  "v_mfma_f32_16x16x32_bf16 v[84:87], a[24:27], v[72:75], v[84:87]\n" \
  "s_waitcnt lgkmcnt(6)\n" \
  "v_mfma_f32_16x16x16_bf16 v[88:91], a[24:25], v[76:77], v[88:91]\n" \
  "s_waitcnt lgkmcnt(5)\n" \
  "v_mfma_f32_16x16x32_bf16 v[84:87], a[28:31], v[80:83], v[84:87]\n" \
  "s_waitcnt lgkmcnt(4)\n" \
  "v_mfma_f32_16x16x16_bf16 v[88:91], a[28:29], v[78:79], v[88:91]\n" \
  "s_waitcnt lgkmcnt(3)\n" \
  "v_mfma_f32_16x16x32_bf16 v[72:75], v[24:27], v[60:63], 0\n" \
  "s_waitcnt lgkmcnt(2)\n" \
  "v_mfma_f32_16x16x32_bf16 v[84:87], v[24:27], v[92:95], v[84:87]\n" \
  "s_waitcnt lgkmcnt(1)\n" \
  "v_mfma_f32_16x16x32_bf16 v[72:75], v[28:31], v[68:71], v[72:75]\n" \
  "s_waitcnt lgkmcnt(0)\n" \
  "v_mfma_f32_16x16x32_bf16 v[84:87], v[28:31], v[64:67], v[84:87]\n" \
  "s_waitcnt vmcnt(14)\n" \
  "s_barrier\n" \
  "ds_read_b128 v[24:27], v0 offset:16384\n" \
  "ds_read_b64 v[28:29], v1 offset:17408\n" \
  "ds_read_b128 v[60:63], v0 offset:18432\n" \
  "ds_read_b64 v[30:31], v1 offset:19456\n" \
  "ds_read_b128 v[64:67], v0 offset:20480\n" \
  "ds_read_b64 v[68:69], v1 offset:21504\n" \
  "ds_read_b128 v[76:79], v0 offset:22528\n" \
  "ds_read_b64 v[70:71], v1 offset:23552\n" \
  "s_waitcnt lgkmcnt(7)\n" \
  "v_mfma_f32_16x16x32_bf16 v[84:87], a[32:35], v[24:27], v[84:87]\n" \
  "ds_read_b128 v[24:27], v0 offset:24576\n" \
  "s_waitcnt lgkmcnt(7)\n" \
  "v_mfma_f32_16x16x16_bf16 v[88:91], a[32:33], v[28:29], v[88:91]\n" \
  "ds_read_b64 v[28:29], v1 offset:25600\n" \
  "s_waitcnt lgkmcnt(7)\n" \
  "v_mfma_f32_16x16x32_bf16 v[84:87], a[36:39], v[60:63], v[84:87]\n" \
  "ds_read_b128 v[60:63], v0 offset:26624\n" \
  "s_waitcnt lgkmcnt(7)\n" \
  "v_mfma_f32_16x16x16_bf16 v[88:91], a[36:37], v[30:31], v[88:91]\n" \
  "ds_read_b64 v[30:31], v1 offset:27648\n" \
  "s_waitcnt lgkmcnt(7)\n" \
  "v_mfma_f32_16x16x32_bf16 v[84:87], a[40:43], v[64:67], v[84:87]\n" \
  "ds_read_b128 v[64:67], v0 offset:28672\n" \
  "s_waitcnt lgkmcnt(7)\n" \
  "v_mfma_f32_16x16x16_bf16 v[88:91], a[40:41], v[68:69], v[88:91]\n" \
  "ds_read_b64 v[68:69], v1 offset:29696\n" \
  "s_waitcnt lgkmcnt(7)\n" \
  "v_mfma_f32_16x16x32_bf16 v[84:87], a[44:47], v[76:79], v[84:87]\n" \
  "ds_read_b128 v[76:79], v0 offset:30720\n" \
  "s_waitcnt lgkmcnt(7)\n" \
  "v_mfma_f32_16x16x16_bf16 v[88:91], a[44:45], v[70:71], v[88:91]\n" \
  "ds_read_b64 v[70:71], v1 offset:31744\n" \
  "s_waitcnt lgkmcnt(7)\n" \
  "v_mfma_f32_16x16x32_bf16 v[84:87], a[48:51], v[24:27], v[84:87]\n" \
  "ds_read_b128 v[24:27], v2 offset:16384\n" \
  "s_waitcnt lgkmcnt(7)\n" \
  "v_mfma_f32_16x16x16_bf16 v[88:91], a[48:49], v[28:29], v[88:91]\n" \
  "ds_read_b128 v[80:83], v2 offset:16640\n" \
  "s_waitcnt lgkmcnt(7)\n" \
  "v_mfma_f32_16x16x32_bf16 v[84:87], a[52:55], v[60:63], v[84:87]\n" \
  "ds_read_b128 v[60:63], v2 offset:24576\n" \
  "s_waitcnt lgkmcnt(7)\n" \
  "v_mfma_f32_16x16x16_bf16 v[88:91], a[52:53], v[30:31], v[88:91]\n" \
  "ds_read_b128 v[28:31], v2 offset:24832\n" \
  "s_waitcnt lgkmcnt(7)\n" \
  "v_mfma_f32_16x16x32_bf16 v[84:87], a[56:59], v[64:67], v[84:87]\n" \
  "s_waitcnt lgkmcnt(6)\n" \
  "v_mfma_f32_16x16x16_bf16 v[88:91], a[56:57], v[68:69], v[88:91]\n" \
  "s_waitcnt lgkmcnt(5)\n" \
  "v_mfma_f32_16x16x32_bf16 v[84:87], a[60:63], v[76:79], v[84:87]\n" \
  "s_waitcnt lgkmcnt(4)\n" \
  "v_mfma_f32_16x16x16_bf16 v[88:91], a[60:61], v[70:71], v[88:91]\n" \
  "s_waitcnt lgkmcnt(3)\n" \
  "v_mfma_f32_16x16x32_bf16 v[72:75], v[32:35], v[24:27], v[72:75]\n" \
  "s_waitcnt lgkmcnt(2)\n" \
  "v_mfma_f32_16x16x32_bf16 v[84:87], v[32:35], v[80:83], v[84:87]\n" \
  "s_waitcnt lgkmcnt(1)\n" \
  "v_mfma_f32_16x16x32_bf16 v[72:75], v[36:39], v[60:63], v[72:75]\n" \
  "s_waitcnt lgkmcnt(0)\n" \
  "v_mfma_f32_16x16x32_bf16 v[84:87], v[36:39], v[28:31], v[84:87]\n" \
  "s_waitcnt vmcnt(8)\n" \
  "s_barrier\n" \
  "ds_read_b128 v[24:27], v0 offset:32768\n" \
  "ds_read_b64 v[28:29], v1 offset:33792\n" \
  "ds_read_b128 v[32:35], v0 offset:34816\n" \
  "ds_read_b64 v[30:31], v1 offset:35840\n" \
  "ds_read_b128 v[36:39], v0 offset:36864\n" \
  "ds_read_b64 v[60:61], v1 offset:37888\n" \
  "ds_read_b128 v[64:67], v0 offset:38912\n" \
  "ds_read_b64 v[62:63], v1 offset:39936\n" \
  "s_waitcnt lgkmcnt(7)\n" \
  "v_mfma_f32_16x16x32_bf16 v[84:87], a[64:67], v[24:27], v[84:87]\n" \
  "ds_read_b128 v[24:27], v0 offset:40960\n" \
  "s_waitcnt lgkmcnt(7)\n" \
  "v_mfma_f32_16x16x16_bf16 v[88:91], a[64:65], v[28:29], v[88:91]\n" \
  "ds_read_b64 v[28:29], v1 offset:41984\n" \
  "s_waitcnt lgkmcnt(7)\n" \
  "v_mfma_f32_16x16x32_bf16 v[84:87], a[68:71], v[32:35], v[84:87]\n" \
  "ds_read_b128 v[32:35], v0 offset:43008\n" \
  "s_waitcnt lgkmcnt(7)\n" \
  "v_mfma_f32_16x16x16_bf16 v[88:91], a[68:69], v[30:31], v[88:91]\n" \
  "ds_read_b64 v[30:31], v1 offset:44032\n" \
  "s_waitcnt lgkmcnt(7)\n" \
  "v_mfma_f32_16x16x32_bf16 v[84:87], a[72:75], v[36:39], v[84:87]\n" \
  "ds_read_b128 v[36:39], v0 offset:45056\n" \
  "s_waitcnt lgkmcnt(7)\n" \
  "v_mfma_f32_16x16x16_bf16 v[88:91], a[72:73], v[60:61], v[88:91]\n" \
  "ds_read_b64 v[60:61], v1 offset:46080\n" \
  "s_waitcnt lgkmcnt(7)\n" \
  "v_mfma_f32_16x16x32_bf16 v[84:87], a[76:79], v[64:67], v[84:87]\n" \
  "ds_read_b128 v[64:67], v0 offset:47104\n" \
  "s_waitcnt lgkmcnt(7)\n" \
  "v_mfma_f32_16x16x16_bf16 v[88:91], a[76:77], v[62:63], v[88:91]\n" \
  "ds_read_b64 v[62:63], v1 offset:48128\n" \
  "s_waitcnt lgkmcnt(7)\n" \
  "v_mfma_f32_16x16x32_bf16 v[84:87], a[80:83], v[24:27], v[84:87]\n" \
  "ds_read_b128 v[24:27], v2 offset:32768\n" \
  "s_waitcnt lgkmcnt(7)\n" \
  "v_mfma_f32_16x16x16_bf16 v[88:91], a[80:81], v[28:29], v[88:91]\n" \
  "ds_read_b128 v[68:71], v2 offset:33024\n" \
  "s_waitcnt lgkmcnt(7)\n" \
  "v_mfma_f32_16x16x32_bf16 v[84:87], a[84:87], v[32:35], v[84:87]\n" \
  "ds_read_b128 v[32:35], v2 offset:40960\n" \
  "s_waitcnt lgkmcnt(7)\n" \
  "v_mfma_f32_16x16x16_bf16 v[88:91], a[84:85], v[30:31], v[88:91]\n" \
  "ds_read_b128 v[28:31], v2 offset:41216\n" \
  "s_waitcnt lgkmcnt(7)\n" \
  "v_mfma_f32_16x16x32_bf16 v[84:87], a[88:91], v[36:39], v[84:87]\n" \
  "s_waitcnt lgkmcnt(6)\n" \
  "v_mfma_f32_16x16x16_bf16 v[88:91], a[88:89], v[60:61], v[88:91]\n" \
  "s_waitcnt lgkmcnt(5)\n" \
  "v_mfma_f32_16x16x32_bf16 v[84:87], a[92:95], v[64:67], v[84:87]\n" \
  "s_waitcnt lgkmcnt(4)\n" \
  "v_mfma_f32_16x16x16_bf16 v[88:91], a[92:93], v[62:63], v[88:91]\n" \
  "s_waitcnt lgkmcnt(3)\n" \
  "v_mfma_f32_16x16x32_bf16 v[72:75], v[40:43], v[24:27], v[72:75]\n" \
  "s_waitcnt lgkmcnt(2)\n" \
  "v_mfma_f32_16x16x32_bf16 v[84:87], v[40:43], v[68:71], v[84:87]\n" \
  "s_waitcnt lgkmcnt(1)\n" \
  "v_mfma_f32_16x16x32_bf16 v[72:75], v[44:47], v[32:35], v[72:75]\n" \
  "s_waitcnt lgkmcnt(0)\n" \
  "v_mfma_f32_16x16x32_bf16 v[84:87], v[44:47], v[28:31], v[84:87]\n" \
  "s_waitcnt vmcnt(2)\n" \
  "s_barrier\n" \
  "ds_read_b128 v[24:27], v0 offset:49152\n" \
  "ds_read_b64 v[28:29], v1 offset:50176\n" \
  "ds_read_b128 v[32:35], v0 offset:51200\n" \
  "ds_read_b64 v[30:31], v1 offset:52224\n" \
  "ds_read_b128 v[36:39], v0 offset:53248\n" \
  "ds_read_b64 v[40:41], v1 offset:54272\n" \
  "ds_read_b128 v[44:47], v0 offset:55296\n" \
  "ds_read_b64 v[42:43], v1 offset:56320\n" \
  "s_waitcnt lgkmcnt(7)\n" \
  "v_mfma_f32_16x16x32_bf16 v[84:87], a[96:99], v[24:27], v[84:87]\n" \
  "ds_read_b128 v[24:27], v0 offset:57344\n" \
  "s_waitcnt lgkmcnt(7)\n" \
  "v_mfma_f32_16x16x16_bf16 v[88:91], a[96:97], v[28:29], v[88:91]\n" \
  "ds_read_b64 v[28:29], v1 offset:58368\n" \
  "s_waitcnt lgkmcnt(7)\n" \
  "v_mfma_f32_16x16x32_bf16 v[84:87], a[100:103], v[32:35], v[84:87]\n" \
  "ds_read_b128 v[32:35], v0 offset:59392\n" \
  "s_waitcnt lgkmcnt(7)\n" \
  "v_mfma_f32_16x16x16_bf16 v[88:91], a[100:101], v[30:31], v[88:91]\n" \
  "ds_read_b64 v[30:31], v1 offset:60416\n" \
  "s_waitcnt lgkmcnt(7)\n" \
  "v_mfma_f32_16x16x32_bf16 v[84:87], a[104:107], v[36:39], v[84:87]\n" \
  "ds_read_b128 v[36:39], v0 offset:61440\n" \
  "s_waitcnt lgkmcnt(7)\n" \
  "v_mfma_f32_16x16x16_bf16 v[88:91], a[104:105], v[40:41], v[88:91]\n" \
  "ds_read_b64 v[40:41], v1 offset:62464\n" \
  "s_waitcnt lgkmcnt(7)\n" \
  "v_mfma_f32_16x16x32_bf16 v[84:87], a[108:111], v[44:47], v[84:87]\n" \
  "ds_read_b128 v[44:47], v0 offset:63488\n" \
  "s_waitcnt lgkmcnt(7)\n" \
  "v_mfma_f32_16x16x16_bf16 v[88:91], a[108:109], v[42:43], v[88:91]\n" \
  "ds_read_b64 v[42:43], v1 offset:64512\n" \
  "s_waitcnt lgkmcnt(7)\n" \
  "v_mfma_f32_16x16x32_bf16 v[84:87], a[112:115], v[24:27], v[84:87]\n" \
  "ds_read_b128 v[24:27], v2 offset:49152\n" \
  "s_waitcnt lgkmcnt(7)\n" \
  "v_mfma_f32_16x16x16_bf16 v[88:91], a[112:113], v[28:29], v[88:91]\n" \
  "ds_read_b128 v[60:63], v2 offset:49408\n" \
  "s_waitcnt lgkmcnt(7)\n" \
  "v_mfma_f32_16x16x32_bf16 v[84:87], a[116:119], v[32:35], v[84:87]\n" \
  "ds_read_b128 v[32:35], v2 offset:57344\n" \
  "s_waitcnt lgkmcnt(7)\n" \
  "v_mfma_f32_16x16x16_bf16 v[88:91], a[116:117], v[30:31], v[88:91]\n" \
  "ds_read_b128 v[28:31], v2 offset:57600\n" \
  "s_waitcnt lgkmcnt(7)\n" \
  "v_mfma_f32_16x16x32_bf16 v[84:87], a[120:123], v[36:39], v[84:87]\n" \
  "s_waitcnt lgkmcnt(6)\n" \
  "v_mfma_f32_16x16x16_bf16 v[88:91], a[120:121], v[40:41], v[88:91]\n" \
  "s_waitcnt lgkmcnt(5)\n" \
  "v_mfma_f32_16x16x32_bf16 v[84:87], a[124:127], v[44:47], v[84:87]\n" \
  "s_waitcnt lgkmcnt(4)\n" \
  "v_mfma_f32_16x16x16_bf16 v[88:91], a[124:125], v[42:43], v[88:91]\n" \
  "s_waitcnt lgkmcnt(3)\n" \
  "v_mfma_f32_16x16x32_bf16 v[72:75], v[48:51], v[24:27], v[72:75]\n" \
  "s_waitcnt lgkmcnt(2)\n" \
  "v_mfma_f32_16x16x32_bf16 v[84:87], v[48:51], v[60:63], v[84:87]\n" \
  "s_waitcnt lgkmcnt(1)\n" \
  "v_mfma_f32_16x16x32_bf16 v[72:75], v[52:55], v[32:35], v[72:75]\n" \
  "s_waitcnt lgkmcnt(0)\n" \
  "v_mfma_f32_16x16x32_bf16 v[84:87], v[52:55], v[28:31], v[84:87]\n" \
  "s_nop 5\n" \
  "v_add_f32 v72, v72, v88\n" \
  "v_add_f32 v73, v73, v89\n" \
  "v_add_f32 v74, v74, v90\n" \
  "v_add_f32 v75, v75, v91\n" \
  "s_add_u32 s85, s50, 28672\n" \
  "s_add_u32 s86, s51, 20480\n" \
  "v_cvt_pk_bf16_f32 v24, v84, v85\n" \
  "v_cvt_pk_bf16_f32 v26, v72, v73\n" \
  "v_cvt_pk_bf16_f32 v25, v86, v87\n" \
  "v_cvt_pk_bf16_f32 v27, v74, v75\n" \
  "s_waitcnt vmcnt(0)\n" \
  "v_lshlrev_b32 v28, 16, v58\n" \
  "v_lshlrev_b32 v32, 16, v56\n" \
  "v_and_b32 v29, 0xffff0000, v58\n" \
  "v_and_b32 v33, 0xffff0000, v56\n" \
  "v_lshlrev_b32 v30, 16, v59\n" \
  "v_lshlrev_b32 v34, 16, v57\n" \
  "v_and_b32 v31, 0xffff0000, v59\n" \
  "v_and_b32 v35, 0xffff0000, v57\n" \
  "v_lshlrev_b32 v84, 16, v24\n" \
  "v_lshlrev_b32 v72, 16, v26\n" \
  "v_and_b32 v85, 0xffff0000, v24\n" \
  "v_and_b32 v73, 0xffff0000, v26\n" \
  "v_lshlrev_b32 v86, 16, v25\n" \
  "v_lshlrev_b32 v74, 16, v27\n" \
  "v_and_b32 v87, 0xffff0000, v25\n" \
  "v_and_b32 v75, 0xffff0000, v27\n" \
  "v_mul_f32 v84, v84, v16\n" \
  "v_mul_f32 v72, v72, v16\n" \
  "v_mul_f32 v85, v85, v17\n" \
  "v_mul_f32 v73, v73, v17\n" \
  "v_mul_f32 v86, v86, v18\n" \
  "v_mul_f32 v74, v74, v18\n" \
  "v_mul_f32 v87, v87, v19\n" \
  "v_mul_f32 v75, v75, v19\n" \
  "v_cvt_pk_bf16_f32 v24, v84, v85\n" \
  "v_cvt_pk_bf16_f32 v26, v72, v73\n" \
  "v_cvt_pk_bf16_f32 v25, v86, v87\n" \
  "v_cvt_pk_bf16_f32 v27, v74, v75\n" \
  "v_lshlrev_b32 v84, 16, v24\n" \
  "v_lshlrev_b32 v72, 16, v26\n" \
  "v_and_b32 v85, 0xffff0000, v24\n" \
  "v_and_b32 v73, 0xffff0000, v26\n" \
  "v_lshlrev_b32 v86, 16, v25\n" \
  "v_lshlrev_b32 v74, 16, v27\n" \
  "v_and_b32 v87, 0xffff0000, v25\n" \
  "v_and_b32 v75, 0xffff0000, v27\n" \
  "v_add_f32 v84, v84, v28\n" \
  "v_add_f32 v72, v72, v32\n" \
  "v_add_f32 v85, v85, v29\n" \
  "v_add_f32 v73, v73, v33\n" \
  "v_add_f32 v86, v86, v30\n" \
  "v_add_f32 v74, v74, v34\n" \
  "v_add_f32 v87, v87, v31\n" \
  "v_add_f32 v75, v75, v35\n" \
  "v_cvt_pk_bf16_f32 v36, v84, v85\n" \
  "v_cvt_pk_bf16_f32 v38, v72, v73\n" \
  "v_cvt_pk_bf16_f32 v37, v86, v87\n" \
  "v_cvt_pk_bf16_f32 v39, v74, v75\n" \
  "v_lshlrev_b32 v84, 16, v36\n" \
  "v_lshlrev_b32 v72, 16, v38\n" \
  "v_and_b32 v85, 0xffff0000, v36\n" \
  "v_and_b32 v73, 0xffff0000, v38\n" \
  "v_lshlrev_b32 v86, 16, v37\n" \
  "v_lshlrev_b32 v74, 16, v39\n" \
  "v_and_b32 v87, 0xffff0000, v37\n" \
  "v_and_b32 v75, 0xffff0000, v39\n" \
  "v_mul_f32 v28, v84, v20\n" \
  "v_mul_f32 v32, v72, v20\n" \
  "v_mul_f32 v29, v85, v21\n" \
  "v_mul_f32 v33, v73, v21\n" \
  "v_mul_f32 v30, v86, v22\n" \
  "v_mul_f32 v34, v74, v22\n" \
  "v_mul_f32 v31, v87, v23\n" \
  "v_mul_f32 v35, v75, v23\n" \
  "v_cvt_pk_bf16_f32 v24, v28, v29\n" \
  "v_cvt_pk_bf16_f32 v26, v32, v33\n" \
  "v_cvt_pk_bf16_f32 v25, v30, v31\n" \
  "v_cvt_pk_bf16_f32 v27, v34, v35\n" \
  "buffer_store_dwordx2 v[24:25], v4, s[44:47], s85 offen sc1\n" \
  "buffer_store_dwordx2 v[26:27], v4, s[44:47], s50 offen sc1\n" \
  "buffer_store_dwordx2 v[36:37], v4, s[44:47], s86 offen sc1\n" \
  "buffer_store_dwordx2 v[38:39], v4, s[44:47], s51 offen sc1\n" \
  "v_add_f32 v24, v84, v85\n" \
  "v_add_f32 v26, v72, v73\n" \
  "v_mul_f32 v25, v84, v84\n" \
  "v_mul_f32 v27, v72, v72\n" \
  "v_add_f32 v24, v24, v86\n" \
  "v_add_f32 v26, v26, v74\n" \
  "v_fmac_f32 v25, v85, v85\n" \
  "v_fmac_f32 v27, v73, v73\n" \
  "v_add_f32 v24, v24, v87\n" \
  "v_add_f32 v26, v26, v75\n" \
  "v_fmac_f32 v25, v86, v86\n" \
  "v_fmac_f32 v27, v74, v74\n" \
  "v_fmac_f32 v25, v87, v87\n" \
  "v_fmac_f32 v27, v75, v75\n" \
  "v_mov_b32 v255, v24\n" \
  "v_mov_b32 v254, v26\n" \
  "s_nop 0\n" \
  "v_permlane32_swap_b32 v24, v255\n" \
  "v_permlane32_swap_b32 v26, v254\n" \
  "v_add_f32 v24, v24, v255\n" \
  "v_add_f32 v26, v26, v254\n" \
  "v_mov_b32 v255, v24\n" \
  "v_mov_b32 v254, v26\n" \
  "s_nop 0\n" \
  "v_permlane16_swap_b32 v24, v255\n" \
  "v_permlane16_swap_b32 v26, v254\n" \
  "v_add_f32 v24, v24, v255\n" \
  "v_add_f32 v26, v26, v254\n" \
  "v_mov_b32 v255, v25\n" \
  "v_mov_b32 v254, v27\n" \
  "s_nop 0\n" \
  "v_permlane32_swap_b32 v25, v255\n" \
  "v_permlane32_swap_b32 v27, v254\n" \
  "v_add_f32 v25, v25, v255\n" \
  "v_add_f32 v27, v27, v254\n" \
  "v_mov_b32 v255, v25\n" \
  "v_mov_b32 v254, v27\n" \
  "s_nop 0\n" \
  "v_permlane16_swap_b32 v25, v255\n" \
  "v_permlane16_swap_b32 v27, v254\n" \
  "v_add_f32 v25, v25, v255\n" \
  "v_add_f32 v27, v27, v254\n" \
  "buffer_store_dwordx2 v[24:25], v5, s[44:47], s85 offen sc1\n" \
  "buffer_store_dwordx2 v[26:27], v5, s[44:47], s50 offen sc1\n" \
  "s_add_u32 s80, s80, 4\n" \
  "s_cmp_ge_u32 s80, 64\n" \
  "s_cbranch_scc1 FIN%=\n" \
  "s_add_u32 s33, s33, 131072\n" \
  "s_bitset0_b32 s33, 19\n" \
  "s_add_u32 s85, s33, 0\n" \
  "buffer_load_dwordx4 a[0:3], v0, s[44:47], s85 offen offset:0 sc1\n" \
  "s_sleep 3\n" \
  "buffer_load_dwordx4 a[4:7], v0, s[44:47], s85 offen offset:1024 sc1\n" \
  "s_sleep 3\n" \
  "buffer_load_dwordx4 a[8:11], v0, s[44:47], s85 offen offset:2048 sc1\n" \
  "s_sleep 3\n" \
  "buffer_load_dwordx4 a[12:15], v0, s[44:47], s85 offen offset:3072 sc1\n" \
  "s_sleep 3\n" \
  "s_add_u32 s48, s48, 8192\n" \
  "s_add_u32 s49, s49, 32768\n" \
  "s_bitset0_b32 s49, 17\n" \
  "s_add_u32 s79, s79, 163840\n" \
  "s_add_u32 s50, s50, 229376\n" \
  "s_add_u32 s51, s51, 163840\n" \
  "v_add_u32 v6, v7, v6\n" \
  "s_waitcnt vmcnt(4)\n" \
  "s_mov_b64 exec, 1\n" \
  "buffer_atomic_add v10, off, s[36:39], s52\n" \
  "s_mov_b64 exec, -1\n" \
  "s_add_u32 s85, s33, 4096\n" \
  "buffer_load_dwordx4 a[16:19], v0, s[44:47], s85 offen offset:0 sc1\n" \
  "s_sleep 3\n" \
  "buffer_load_dwordx4 a[20:23], v0, s[44:47], s85 offen offset:1024 sc1\n" \
  "s_sleep 3\n" \
  "buffer_load_dwordx4 a[24:27], v0, s[44:47], s85 offen offset:2048 sc1\n" \
  "s_sleep 3\n" \
  "buffer_load_dwordx4 a[28:31], v0, s[44:47], s85 offen offset:3072 sc1\n" \
  "s_sleep 3\n" \
  "s_add_u32 s85, s33, 8192\n" \
  "buffer_load_dwordx4 a[32:35], v0, s[44:47], s85 offen offset:0 sc1\n" \
  "s_sleep 3\n" \
  "buffer_load_dwordx4 a[36:39], v0, s[44:47], s85 offen offset:1024 sc1\n" \
  "s_sleep 3\n" \
  "buffer_load_dwordx4 a[40:43], v0, s[44:47], s85 offen offset:2048 sc1\n" \
  "s_sleep 3\n" \
  "buffer_load_dwordx4 a[44:47], v0, s[44:47], s85 offen offset:3072 sc1\n" \
  "s_sleep 3\n" \
  "s_add_u32 s85, s33, 12288\n" \
  "buffer_load_dwordx4 a[48:51], v0, s[44:47], s85 offen offset:0 sc1\n" \
  "s_sleep 3\n" \
  "buffer_load_dwordx4 a[52:55], v0, s[44:47], s85 offen offset:1024 sc1\n" \
  "s_sleep 3\n" \
  "buffer_load_dwordx4 a[56:59], v0, s[44:47], s85 offen offset:2048 sc1\n" \
  "s_sleep 3\n" \
  "buffer_load_dwordx4 a[60:63], v0, s[44:47], s85 offen offset:3072 sc1\n" \
  "s_sleep 3\n" \
  "s_add_u32 s85, s33, 16384\n" \
  "buffer_load_dwordx4 a[64:67], v0, s[44:47], s85 offen offset:0 sc1\n" \
  "s_sleep 3\n" \
  "buffer_load_dwordx4 a[68:71], v0, s[44:47], s85 offen offset:1024 sc1\n" \
  "s_sleep 3\n" \
  "buffer_load_dwordx4 a[72:75], v0, s[44:47], s85 offen offset:2048 sc1\n" \
  "s_sleep 3\n" \
  "buffer_load_dwordx4 a[76:79], v0, s[44:47], s85 offen offset:3072 sc1\n" \
  "s_sleep 3\n" \
  "s_add_u32 s85, s33, 20480\n" \
  "buffer_load_dwordx4 a[80:83], v0, s[44:47], s85 offen offset:0 sc1\n" \
  "s_sleep 3\n" \
  "buffer_load_dwordx4 a[84:87], v0, s[44:47], s85 offen offset:1024 sc1\n" \
  "s_sleep 3\n" \
  "buffer_load_dwordx4 a[88:91], v0, s[44:47], s85 offen offset:2048 sc1\n" \
  "s_sleep 3\n" \
  "buffer_load_dwordx4 a[92:95], v0, s[44:47], s85 offen offset:3072 sc1\n" \
  "s_sleep 3\n" \
  "s_add_u32 s85, s33, 24576\n" \
  "buffer_load_dwordx4 a[96:99], v0, s[44:47], s85 offen offset:0 sc1\n" \
  "s_sleep 3\n" \
  "buffer_load_dwordx4 a[100:103], v0, s[44:47], s85 offen offset:1024 sc1\n" \
  "s_sleep 3\n" \
  "buffer_load_dwordx4 a[104:107], v0, s[44:47], s85 offen offset:2048 sc1\n" \
  "s_sleep 3\n" \
  "buffer_load_dwordx4 a[108:111], v0, s[44:47], s85 offen offset:3072 sc1\n" \
  "s_sleep 3\n" \
  "s_add_u32 s85, s33, 28672\n" \
  "buffer_load_dwordx4 a[112:115], v0, s[44:47], s85 offen offset:0 sc1\n" \
  "s_sleep 3\n" \
  "buffer_load_dwordx4 a[116:119], v0, s[44:47], s85 offen offset:1024 sc1\n" \
  "s_sleep 3\n" \
  "buffer_load_dwordx4 a[120:123], v0, s[44:47], s85 offen offset:2048 sc1\n" \
  "s_sleep 3\n" \
  "buffer_load_dwordx4 a[124:127], v0, s[44:47], s85 offen offset:3072 sc1\n" \
  "s_sleep 3\n" \
  "s_branch LOOP%=\n" \
  "FIN%=:\n" \
  "s_waitcnt vmcnt(0)\n" \
  "s_mov_b64 exec, 1\n" \
  "buffer_atomic_add v10, off, s[36:39], s52\n" \
  "s_mov_b64 exec, -1\n" \
  "DONE%=:\n" \
  "s_waitcnt vmcnt(0) lgkmcnt(0)\n" \
  "s_cmp_eq_u32 s73, -1\n" \
  "s_cbranch_scc1 END%=\n" \
  "s_memrealtime vcc\n" \
  "s_waitcnt lgkmcnt(0)\n" \
  "v_mov_b32 v32, vcc_lo\n" \
  "s_sub_u32 s22, vcc_lo, s22\n" \
  "s_memtime vcc\n" \
  "s_waitcnt lgkmcnt(0)\n" \
  "s_sub_u32 s20, vcc_lo, s20\n" \
  "v_mov_b32 v33, s20\n" \
  "v_mov_b32 v34, s22\n" \
  "v_mov_b32 v35, s24\n" \
  "s_mov_b64 exec, 1\n" \
  "buffer_store_dword v32, off, s[44:47], s73\n" \
  "buffer_store_dword v33, off, s[44:47], s73 offset:1024\n" \
  "buffer_store_dword v34, off, s[44:47], s73 offset:2048\n" \
  "buffer_store_dword v35, off, s[44:47], s73 offset:3072\n" \
  "s_waitcnt vmcnt(0)\n" \
  "END%=:\n" \
  "s_endpgm\n"
#define REC_R_ASM \
  "s_mov_b32 s55, 0x40000\n" \
  "s_mov_b32 s80, 0\n" \
  "s_waitcnt lgkmcnt(0)\n" \
  "s_memtime s[20:21]\n" \
  "s_memrealtime s[22:23]\n" \
  "s_mov_b32 s24, 0\n" \
  "s_waitcnt lgkmcnt(0)\n" \
  "buffer_load_dword v255, v9, s[36:39], 0 offen sc1\n" \
  "s_waitcnt vmcnt(0)\n" \
  "v_cmp_gt_i32 vcc, s53, v255\n" \
  "s_cbranch_vccz NCi%=\n" \
  "NBi%=:\n" \
  "s_cmp_eq_u32 s55, 0\n" \
  "s_cbranch_scc1 NCi%=\n" \
  "s_sub_u32 s55, s55, 1\n" \
  "s_sleep 8\n" \
  "buffer_load_dword v255, v9, s[36:39], 0 offen sc1\n" \
  "s_waitcnt vmcnt(0)\n" \
  "v_cmp_gt_i32 vcc, s53, v255\n" \
  "s_cbranch_vccnz NBi%=\n" \
  "NCi%=:\n" \
  "s_add_u32 s85, s48, 0\n" \
  "buffer_load_dwordx4 v[28:31], v1, s[44:47], s85 offen offset:0 sc1\n" \
  "buffer_load_dwordx4 v[32:35], v1, s[44:47], s85 offen offset:1024 sc1\n" \
  "buffer_load_dwordx4 v[36:39], v1, s[44:47], s85 offen offset:2048 sc1\n" \
  "buffer_load_dwordx4 v[40:43], v1, s[44:47], s85 offen offset:3072 sc1\n" \
  "s_add_u32 s85, s48, 4096\n" \
  "buffer_load_dwordx4 v[44:47], v1, s[44:47], s85 offen offset:0 sc1\n" \
  "buffer_load_dwordx4 v[48:51], v1, s[44:47], s85 offen offset:1024 sc1\n" \
  "buffer_load_dwordx4 v[52:55], v1, s[44:47], s85 offen offset:2048 sc1\n" \
  "buffer_load_dwordx4 v[56:59], v1, s[44:47], s85 offen offset:3072 sc1\n" \
  "s_add_u32 s85, s48, 8192\n" \
  "buffer_load_dwordx4 v[60:63], v1, s[44:47], s85 offen offset:0 sc1\n" \
  "buffer_load_dwordx4 v[64:67], v1, s[44:47], s85 offen offset:1024 sc1\n" \
  "buffer_load_dwordx4 v[68:71], v1, s[44:47], s85 offen offset:2048 sc1\n" \
  "buffer_load_dwordx4 v[72:75], v1, s[44:47], s85 offen offset:3072 sc1\n" \
  "s_add_u32 s85, s48, 12288\n" \
  "buffer_load_dwordx4 v[76:79], v1, s[44:47], s85 offen offset:0 sc1\n" \
  "buffer_load_dwordx4 v[80:83], v1, s[44:47], s85 offen offset:1024 sc1\n" \
  "buffer_load_dwordx4 v[84:87], v2, s[44:47], s49 offen sc1\n" \
  "s_add_u32 s48, s48, 0x100000\n" \
  "s_bitset0_b32 s48, 24\n" \
  "s_add_u32 s49, s49, 40960\n" \
  "s_add_u32 s53, s53, 1\n" \
  "buffer_load_dword v255, v9, s[36:39], 0 offen sc1\n" \
  "s_waitcnt vmcnt(0)\n" \
  "LOOP%=:\n" \
  "s_waitcnt vmcnt(2)\n" \
  "v_cmp_gt_i32 vcc, s53, v255\n" \
  "s_cbranch_vccnz NA0%=\n" \
  "s_add_u32 s85, s48, 0\n" \
  "buffer_load_dwordx4 v[88:91], v1, s[44:47], s85 offen offset:0 sc1\n" \
  "buffer_load_dwordx4 v[92:95], v1, s[44:47], s85 offen offset:1024 sc1\n" \
  "buffer_load_dwordx4 v[96:99], v1, s[44:47], s85 offen offset:2048 sc1\n" \
  "buffer_load_dwordx4 v[100:103], v1, s[44:47], s85 offen offset:3072 sc1\n" \
  "s_add_u32 s85, s48, 4096\n" \
  "buffer_load_dwordx4 v[104:107], v1, s[44:47], s85 offen offset:0 sc1\n" \
  "buffer_load_dwordx4 v[108:111], v1, s[44:47], s85 offen offset:1024 sc1\n" \
  "buffer_load_dwordx4 v[112:115], v1, s[44:47], s85 offen offset:2048 sc1\n" \
  "buffer_load_dwordx4 v[116:119], v1, s[44:47], s85 offen offset:3072 sc1\n" \
  "s_add_u32 s85, s48, 8192\n" \
  "buffer_load_dwordx4 v[120:123], v1, s[44:47], s85 offen offset:0 sc1\n" \
  "buffer_load_dwordx4 v[124:127], v1, s[44:47], s85 offen offset:1024 sc1\n" \
  "buffer_load_dwordx4 v[128:131], v1, s[44:47], s85 offen offset:2048 sc1\n" \
  "buffer_load_dwordx4 v[132:135], v1, s[44:47], s85 offen offset:3072 sc1\n" \
  "s_add_u32 s85, s48, 12288\n" \
  "buffer_load_dwordx4 v[136:139], v1, s[44:47], s85 offen offset:0 sc1\n" \
  "buffer_load_dwordx4 v[140:143], v1, s[44:47], s85 offen offset:1024 sc1\n" \
  "buffer_load_dwordx4 v[144:147], v2, s[44:47], s49 offen sc1\n" \
  "s_add_u32 s48, s48, 0x100000\n" \
  "s_bitset0_b32 s48, 24\n" \
  "s_add_u32 s49, s49, 40960\n" \
  "s_add_u32 s53, s53, 1\n" \
  "buffer_load_dword v255, v9, s[36:39], 0 offen sc1\n" \
  "s_waitcnt vmcnt(55)\n" \
  "v_lshlrev_b32 v148, 16, v28\n" \
  "v_and_b32 v149, 0xffff0000, v28\n" \
  "v_lshlrev_b32 v150, 16, v29\n" \
  "v_and_b32 v151, 0xffff0000, v29\n" \
  "v_lshlrev_b32 v152, 16, v30\n" \
  "v_and_b32 v153, 0xffff0000, v30\n" \
  "v_lshlrev_b32 v154, 16, v31\n" \
  "v_and_b32 v155, 0xffff0000, v31\n" \
  "s_waitcnt vmcnt(54)\n" \
  "v_dot2c_f32_bf16 v148, v32, v24\n" \
  "v_dot2c_f32_bf16 v149, v32, v25\n" \
  "v_dot2c_f32_bf16 v150, v33, v24\n" \
  "v_dot2c_f32_bf16 v151, v33, v25\n" \
  "v_dot2c_f32_bf16 v152, v34, v24\n" \
  "v_dot2c_f32_bf16 v153, v34, v25\n" \
  "v_dot2c_f32_bf16 v154, v35, v24\n" \
  "v_dot2c_f32_bf16 v155, v35, v25\n" \
  "s_waitcnt vmcnt(53)\n" \
  "v_dot2c_f32_bf16 v148, v36, v24\n" \
  "v_dot2c_f32_bf16 v149, v36, v25\n" \
  "v_dot2c_f32_bf16 v150, v37, v24\n" \
  "v_dot2c_f32_bf16 v151, v37, v25\n" \
  "v_dot2c_f32_bf16 v152, v38, v24\n" \
  "v_dot2c_f32_bf16 v153, v38, v25\n" \
  "v_dot2c_f32_bf16 v154, v39, v24\n" \
  "v_dot2c_f32_bf16 v155, v39, v25\n" \
  "s_waitcnt vmcnt(52)\n" \
  "v_dot2c_f32_bf16 v148, v40, v24\n" \
  "v_dot2c_f32_bf16 v149, v40, v25\n" \
  "v_dot2c_f32_bf16 v150, v41, v24\n" \
  "v_dot2c_f32_bf16 v151, v41, v25\n" \
  "v_dot2c_f32_bf16 v152, v42, v24\n" \
  "v_dot2c_f32_bf16 v153, v42, v25\n" \
  "v_dot2c_f32_bf16 v154, v43, v24\n" \
  "v_dot2c_f32_bf16 v155, v43, v25\n" \
  "s_waitcnt vmcnt(51)\n" \
  "v_dot2c_f32_bf16 v148, v44, v24\n" \
  "v_dot2c_f32_bf16 v149, v44, v25\n" \
  "v_dot2c_f32_bf16 v150, v45, v24\n" \
  "v_dot2c_f32_bf16 v151, v45, v25\n" \
  "v_dot2c_f32_bf16 v152, v46, v24\n" \
  "v_dot2c_f32_bf16 v153, v46, v25\n" \
  "v_dot2c_f32_bf16 v154, v47, v24\n" \
  "v_dot2c_f32_bf16 v155, v47, v25\n" \
  "s_waitcnt vmcnt(50)\n" \
  "v_dot2c_f32_bf16 v148, v48, v24\n" \
  "v_dot2c_f32_bf16 v149, v48, v25\n" \
  "v_dot2c_f32_bf16 v150, v49, v24\n" \
  "v_dot2c_f32_bf16 v151, v49, v25\n" \
  "v_dot2c_f32_bf16 v152, v50, v24\n" \
  "v_dot2c_f32_bf16 v153, v50, v25\n" \
  "v_dot2c_f32_bf16 v154, v51, v24\n" \
  "v_dot2c_f32_bf16 v155, v51, v25\n" \
  "s_waitcnt vmcnt(49)\n" \
  "v_dot2c_f32_bf16 v148, v52, v24\n" \
  "v_dot2c_f32_bf16 v149, v52, v25\n" \
  "v_dot2c_f32_bf16 v150, v53, v24\n" \
  "v_dot2c_f32_bf16 v151, v53, v25\n" \
  "v_dot2c_f32_bf16 v152, v54, v24\n" \
  "v_dot2c_f32_bf16 v153, v54, v25\n" \
  "v_dot2c_f32_bf16 v154, v55, v24\n" \
  "v_dot2c_f32_bf16 v155, v55, v25\n" \
  "s_waitcnt vmcnt(48)\n" \
  "v_dot2c_f32_bf16 v148, v56, v24\n" \
  "v_dot2c_f32_bf16 v149, v56, v25\n" \
  "v_dot2c_f32_bf16 v150, v57, v24\n" \
  "v_dot2c_f32_bf16 v151, v57, v25\n" \
  "v_dot2c_f32_bf16 v152, v58, v24\n" \
  "v_dot2c_f32_bf16 v153, v58, v25\n" \
  "v_dot2c_f32_bf16 v154, v59, v24\n" \
  "v_dot2c_f32_bf16 v155, v59, v25\n" \
  "s_waitcnt vmcnt(47)\n" \
  "v_dot2c_f32_bf16 v148, v60, v24\n" \
  "v_dot2c_f32_bf16 v149, v60, v25\n" \
  "v_dot2c_f32_bf16 v150, v61, v24\n" \
  "v_dot2c_f32_bf16 v151, v61, v25\n" \
  "v_dot2c_f32_bf16 v152, v62, v24\n" \
  "v_dot2c_f32_bf16 v153, v62, v25\n" \
  "v_dot2c_f32_bf16 v154, v63, v24\n" \
  "v_dot2c_f32_bf16 v155, v63, v25\n" \
  "s_waitcnt vmcnt(46)\n" \
  "v_dot2c_f32_bf16 v148, v64, v24\n" \
  "v_dot2c_f32_bf16 v149, v64, v25\n" \
  "v_dot2c_f32_bf16 v150, v65, v24\n" \
  "v_dot2c_f32_bf16 v151, v65, v25\n" \
  "v_dot2c_f32_bf16 v152, v66, v24\n" \
  "v_dot2c_f32_bf16 v153, v66, v25\n" \
  "v_dot2c_f32_bf16 v154, v67, v24\n" \
  "v_dot2c_f32_bf16 v155, v67, v25\n" \
  "s_waitcnt vmcnt(45)\n" \
  "v_dot2c_f32_bf16 v148, v68, v24\n" \
  "v_dot2c_f32_bf16 v149, v68, v25\n" \
  "v_dot2c_f32_bf16 v150, v69, v24\n" \
  "v_dot2c_f32_bf16 v151, v69, v25\n" \
  "v_dot2c_f32_bf16 v152, v70, v24\n" \
  "v_dot2c_f32_bf16 v153, v70, v25\n" \
  "v_dot2c_f32_bf16 v154, v71, v24\n" \
  "v_dot2c_f32_bf16 v155, v71, v25\n" \
  "s_waitcnt vmcnt(44)\n" \
  "v_dot2c_f32_bf16 v148, v72, v24\n" \
  "v_dot2c_f32_bf16 v149, v72, v25\n" \
  "v_dot2c_f32_bf16 v150, v73, v24\n" \
  "v_dot2c_f32_bf16 v151, v73, v25\n" \
  "v_dot2c_f32_bf16 v152, v74, v24\n" \
  "v_dot2c_f32_bf16 v153, v74, v25\n" \
  "v_dot2c_f32_bf16 v154, v75, v24\n" \
  "v_dot2c_f32_bf16 v155, v75, v25\n" \
  "s_waitcnt vmcnt(43)\n" \
  "v_dot2c_f32_bf16 v148, v76, v24\n" \
  "v_dot2c_f32_bf16 v149, v76, v25\n" \
  "v_dot2c_f32_bf16 v150, v77, v24\n" \
  "v_dot2c_f32_bf16 v151, v77, v25\n" \
  "v_dot2c_f32_bf16 v152, v78, v24\n" \
  "v_dot2c_f32_bf16 v153, v78, v25\n" \
  "v_dot2c_f32_bf16 v154, v79, v24\n" \
  "v_dot2c_f32_bf16 v155, v79, v25\n" \
  "s_waitcnt vmcnt(42)\n" \
  "v_dot2c_f32_bf16 v148, v80, v24\n" \
  "v_dot2c_f32_bf16 v149, v80, v25\n" \
  "v_dot2c_f32_bf16 v150, v81, v24\n" \
  "v_dot2c_f32_bf16 v151, v81, v25\n" \
  "v_dot2c_f32_bf16 v152, v82, v24\n" \
  "v_dot2c_f32_bf16 v153, v82, v25\n" \
  "v_dot2c_f32_bf16 v154, v83, v24\n" \
  "v_dot2c_f32_bf16 v155, v83, v25\n" \
  "s_waitcnt vmcnt(41)\n" \
  "v_lshlrev_b32 v156, 16, v84\n" \
  "v_and_b32 v157, 0xffff0000, v84\n" \
  "v_lshlrev_b32 v158, 16, v85\n" \
  "v_and_b32 v159, 0xffff0000, v85\n" \
  "v_lshlrev_b32 v160, 16, v86\n" \
  "v_and_b32 v161, 0xffff0000, v86\n" \
  "v_lshlrev_b32 v162, 16, v87\n" \
  "v_and_b32 v163, 0xffff0000, v87\n" \
  "v_cvt_pk_bf16_f32 v164, v148, v149\n" \
  "v_cvt_pk_bf16_f32 v165, v150, v151\n" \
  "v_cvt_pk_bf16_f32 v166, v152, v153\n" \
  "v_cvt_pk_bf16_f32 v167, v154, v155\n" \
  "v_lshlrev_b32 v148, 16, v164\n" \
  "v_and_b32 v149, 0xffff0000, v164\n" \
  "v_lshlrev_b32 v150, 16, v165\n" \
  "v_and_b32 v151, 0xffff0000, v165\n" \
  "v_lshlrev_b32 v152, 16, v166\n" \
  "v_and_b32 v153, 0xffff0000, v166\n" \
  "v_lshlrev_b32 v154, 16, v167\n" \
  "v_and_b32 v155, 0xffff0000, v167\n" \
  "v_mul_f32 v148, v148, v16\n" \
  "v_mul_f32 v149, v149, v17\n" \
  "v_mul_f32 v150, v150, v18\n" \
  "v_mul_f32 v151, v151, v19\n" \
  "v_mul_f32 v152, v152, v20\n" \
  "v_mul_f32 v153, v153, v21\n" \
  "v_mul_f32 v154, v154, v22\n" \
  "v_mul_f32 v155, v155, v23\n" \
  "v_cvt_pk_bf16_f32 v164, v148, v149\n" \
  "v_cvt_pk_bf16_f32 v165, v150, v151\n" \
  "v_cvt_pk_bf16_f32 v166, v152, v153\n" \
  "v_cvt_pk_bf16_f32 v167, v154, v155\n" \
  "v_lshlrev_b32 v148, 16, v164\n" \
  "v_and_b32 v149, 0xffff0000, v164\n" \
  "v_lshlrev_b32 v150, 16, v165\n" \
  "v_and_b32 v151, 0xffff0000, v165\n" \
  "v_lshlrev_b32 v152, 16, v166\n" \
  "v_and_b32 v153, 0xffff0000, v166\n" \
  "v_lshlrev_b32 v154, 16, v167\n" \
  "v_and_b32 v155, 0xffff0000, v167\n" \
  "v_add_f32 v148, v148, v156\n" \
  "v_add_f32 v149, v149, v157\n" \
  "v_add_f32 v150, v150, v158\n" \
  "v_add_f32 v151, v151, v159\n" \
  "v_add_f32 v152, v152, v160\n" \
  "v_add_f32 v153, v153, v161\n" \
  "v_add_f32 v154, v154, v162\n" \
  "v_add_f32 v155, v155, v163\n" \
  "v_cvt_pk_bf16_f32 v164, v148, v149\n" \
  "v_cvt_pk_bf16_f32 v165, v150, v151\n" \
  "v_cvt_pk_bf16_f32 v166, v152, v153\n" \
  "v_cvt_pk_bf16_f32 v167, v154, v155\n" \
  "buffer_store_dwordx4 v[164:167], v3, s[56:59], s50 offen sc1\n" \
  "v_mov_b32 v254, s80\n" \
  "s_add_u32 s50, s50, 40960\n" \
  "s_add_u32 s80, s80, 1\n" \
  "s_mov_b64 exec, 1\n" \
  "buffer_store_dword v254, off, s[36:39], s52 sc1\n" \
  "s_mov_b64 exec, -1\n" \
  "s_branch NX0%=\n" \
  "SL0%=:\n" \
  "s_waitcnt vmcnt(15)\n" \
  "v_lshlrev_b32 v148, 16, v28\n" \
  "v_and_b32 v149, 0xffff0000, v28\n" \
  "v_lshlrev_b32 v150, 16, v29\n" \
  "v_and_b32 v151, 0xffff0000, v29\n" \
  "v_lshlrev_b32 v152, 16, v30\n" \
  "v_and_b32 v153, 0xffff0000, v30\n" \
  "v_lshlrev_b32 v154, 16, v31\n" \
  "v_and_b32 v155, 0xffff0000, v31\n" \
  "s_waitcnt vmcnt(14)\n" \
  "v_dot2c_f32_bf16 v148, v32, v24\n" \
  "v_dot2c_f32_bf16 v149, v32, v25\n" \
  "v_dot2c_f32_bf16 v150, v33, v24\n" \
  "v_dot2c_f32_bf16 v151, v33, v25\n" \
  "v_dot2c_f32_bf16 v152, v34, v24\n" \
  "v_dot2c_f32_bf16 v153, v34, v25\n" \
  "v_dot2c_f32_bf16 v154, v35, v24\n" \
  "v_dot2c_f32_bf16 v155, v35, v25\n" \
  "s_waitcnt vmcnt(13)\n" \
  "v_dot2c_f32_bf16 v148, v36, v24\n" \
  "v_dot2c_f32_bf16 v149, v36, v25\n" \
  "v_dot2c_f32_bf16 v150, v37, v24\n" \
  "v_dot2c_f32_bf16 v151, v37, v25\n" \
  "v_dot2c_f32_bf16 v152, v38, v24\n" \
  "v_dot2c_f32_bf16 v153, v38, v25\n" \
  "v_dot2c_f32_bf16 v154, v39, v24\n" \
  "v_dot2c_f32_bf16 v155, v39, v25\n" \
  "s_waitcnt vmcnt(12)\n" \
  "v_dot2c_f32_bf16 v148, v40, v24\n" \
  "v_dot2c_f32_bf16 v149, v40, v25\n" \
  "v_dot2c_f32_bf16 v150, v41, v24\n" \
  "v_dot2c_f32_bf16 v151, v41, v25\n" \
  "v_dot2c_f32_bf16 v152, v42, v24\n" \
  "v_dot2c_f32_bf16 v153, v42, v25\n" \
  "v_dot2c_f32_bf16 v154, v43, v24\n" \
  "v_dot2c_f32_bf16 v155, v43, v25\n" \
  "s_waitcnt vmcnt(11)\n" \
  "v_dot2c_f32_bf16 v148, v44, v24\n" \
  "v_dot2c_f32_bf16 v149, v44, v25\n" \
  "v_dot2c_f32_bf16 v150, v45, v24\n" \
  "v_dot2c_f32_bf16 v151, v45, v25\n" \
  "v_dot2c_f32_bf16 v152, v46, v24\n" \
  "v_dot2c_f32_bf16 v153, v46, v25\n" \
  "v_dot2c_f32_bf16 v154, v47, v24\n" \
  "v_dot2c_f32_bf16 v155, v47, v25\n" \
  "s_waitcnt vmcnt(10)\n" \
  "v_dot2c_f32_bf16 v148, v48, v24\n" \
  "v_dot2c_f32_bf16 v149, v48, v25\n" \
  "v_dot2c_f32_bf16 v150, v49, v24\n" \
  "v_dot2c_f32_bf16 v151, v49, v25\n" \
  "v_dot2c_f32_bf16 v152, v50, v24\n" \
  "v_dot2c_f32_bf16 v153, v50, v25\n" \
  "v_dot2c_f32_bf16 v154, v51, v24\n" \
  "v_dot2c_f32_bf16 v155, v51, v25\n" \
  "s_waitcnt vmcnt(9)\n" \
  "v_dot2c_f32_bf16 v148, v52, v24\n" \
  "v_dot2c_f32_bf16 v149, v52, v25\n" \
  "v_dot2c_f32_bf16 v150, v53, v24\n" \
  "v_dot2c_f32_bf16 v151, v53, v25\n" \
  "v_dot2c_f32_bf16 v152, v54, v24\n" \
  "v_dot2c_f32_bf16 v153, v54, v25\n" \
  "v_dot2c_f32_bf16 v154, v55, v24\n" \
  "v_dot2c_f32_bf16 v155, v55, v25\n" \
  "s_waitcnt vmcnt(8)\n" \
  "v_dot2c_f32_bf16 v148, v56, v24\n" \
  "v_dot2c_f32_bf16 v149, v56, v25\n" \
  "v_dot2c_f32_bf16 v150, v57, v24\n" \
  "v_dot2c_f32_bf16 v151, v57, v25\n" \
  "v_dot2c_f32_bf16 v152, v58, v24\n" \
  "v_dot2c_f32_bf16 v153, v58, v25\n" \
  "v_dot2c_f32_bf16 v154, v59, v24\n" \
  "v_dot2c_f32_bf16 v155, v59, v25\n" \
  "s_waitcnt vmcnt(7)\n" \
  "v_dot2c_f32_bf16 v148, v60, v24\n" \
  "v_dot2c_f32_bf16 v149, v60, v25\n" \
  "v_dot2c_f32_bf16 v150, v61, v24\n" \
  "v_dot2c_f32_bf16 v151, v61, v25\n" \
  "v_dot2c_f32_bf16 v152, v62, v24\n" \
  "v_dot2c_f32_bf16 v153, v62, v25\n" \
  "v_dot2c_f32_bf16 v154, v63, v24\n" \
  "v_dot2c_f32_bf16 v155, v63, v25\n" \
  "s_waitcnt vmcnt(6)\n" \
  "v_dot2c_f32_bf16 v148, v64, v24\n" \
  "v_dot2c_f32_bf16 v149, v64, v25\n" \
  "v_dot2c_f32_bf16 v150, v65, v24\n" \
  "v_dot2c_f32_bf16 v151, v65, v25\n" \
  "v_dot2c_f32_bf16 v152, v66, v24\n" \
  "v_dot2c_f32_bf16 v153, v66, v25\n" \
  "v_dot2c_f32_bf16 v154, v67, v24\n" \
  "v_dot2c_f32_bf16 v155, v67, v25\n" \
  "s_waitcnt vmcnt(5)\n" \
  "v_dot2c_f32_bf16 v148, v68, v24\n" \
  "v_dot2c_f32_bf16 v149, v68, v25\n" \
  "v_dot2c_f32_bf16 v150, v69, v24\n" \
  "v_dot2c_f32_bf16 v151, v69, v25\n" \
  "v_dot2c_f32_bf16 v152, v70, v24\n" \
  "v_dot2c_f32_bf16 v153, v70, v25\n" \
  "v_dot2c_f32_bf16 v154, v71, v24\n" \
  "v_dot2c_f32_bf16 v155, v71, v25\n" \
  "s_waitcnt vmcnt(4)\n" \
  "v_dot2c_f32_bf16 v148, v72, v24\n" \
  "v_dot2c_f32_bf16 v149, v72, v25\n" \
  "v_dot2c_f32_bf16 v150, v73, v24\n" \
  "v_dot2c_f32_bf16 v151, v73, v25\n" \
  "v_dot2c_f32_bf16 v152, v74, v24\n" \
  "v_dot2c_f32_bf16 v153, v74, v25\n" \
  "v_dot2c_f32_bf16 v154, v75, v24\n" \
  "v_dot2c_f32_bf16 v155, v75, v25\n" \
  "s_waitcnt vmcnt(3)\n" \
  "v_dot2c_f32_bf16 v148, v76, v24\n" \
  "v_dot2c_f32_bf16 v149, v76, v25\n" \
  "v_dot2c_f32_bf16 v150, v77, v24\n" \
  "v_dot2c_f32_bf16 v151, v77, v25\n" \
  "v_dot2c_f32_bf16 v152, v78, v24\n" \
  "v_dot2c_f32_bf16 v153, v78, v25\n" \
  "v_dot2c_f32_bf16 v154, v79, v24\n" \
  "v_dot2c_f32_bf16 v155, v79, v25\n" \
  "s_waitcnt vmcnt(2)\n" \
  "v_dot2c_f32_bf16 v148, v80, v24\n" \
  "v_dot2c_f32_bf16 v149, v80, v25\n" \
  "v_dot2c_f32_bf16 v150, v81, v24\n" \
  "v_dot2c_f32_bf16 v151, v81, v25\n" \
  "v_dot2c_f32_bf16 v152, v82, v24\n" \
  "v_dot2c_f32_bf16 v153, v82, v25\n" \
  "v_dot2c_f32_bf16 v154, v83, v24\n" \
  "v_dot2c_f32_bf16 v155, v83, v25\n" \
  "s_waitcnt vmcnt(1)\n" \
  "v_lshlrev_b32 v156, 16, v84\n" \
  "v_and_b32 v157, 0xffff0000, v84\n" \
  "v_lshlrev_b32 v158, 16, v85\n" \
  "v_and_b32 v159, 0xffff0000, v85\n" \
  "v_lshlrev_b32 v160, 16, v86\n" \
  "v_and_b32 v161, 0xffff0000, v86\n" \
  "v_lshlrev_b32 v162, 16, v87\n" \
  "v_and_b32 v163, 0xffff0000, v87\n" \
  "v_cvt_pk_bf16_f32 v164, v148, v149\n" \
  "v_cvt_pk_bf16_f32 v165, v150, v151\n" \
  "v_cvt_pk_bf16_f32 v166, v152, v153\n" \
  "v_cvt_pk_bf16_f32 v167, v154, v155\n" \
  "v_lshlrev_b32 v148, 16, v164\n" \
  "v_and_b32 v149, 0xffff0000, v164\n" \
  "v_lshlrev_b32 v150, 16, v165\n" \
  "v_and_b32 v151, 0xffff0000, v165\n" \
  "v_lshlrev_b32 v152, 16, v166\n" \
  "v_and_b32 v153, 0xffff0000, v166\n" \
  "v_lshlrev_b32 v154, 16, v167\n" \
  "v_and_b32 v155, 0xffff0000, v167\n" \
  "v_mul_f32 v148, v148, v16\n" \
  "v_mul_f32 v149, v149, v17\n" \
  "v_mul_f32 v150, v150, v18\n" \
  "v_mul_f32 v151, v151, v19\n" \
  "v_mul_f32 v152, v152, v20\n" \
  "v_mul_f32 v153, v153, v21\n" \
  "v_mul_f32 v154, v154, v22\n" \
  "v_mul_f32 v155, v155, v23\n" \
  "v_cvt_pk_bf16_f32 v164, v148, v149\n" \
  "v_cvt_pk_bf16_f32 v165, v150, v151\n" \
  "v_cvt_pk_bf16_f32 v166, v152, v153\n" \
  "v_cvt_pk_bf16_f32 v167, v154, v155\n" \
  "v_lshlrev_b32 v148, 16, v164\n" \
  "v_and_b32 v149, 0xffff0000, v164\n" \
  "v_lshlrev_b32 v150, 16, v165\n" \
  "v_and_b32 v151, 0xffff0000, v165\n" \
  "v_lshlrev_b32 v152, 16, v166\n" \
  "v_and_b32 v153, 0xffff0000, v166\n" \
  "v_lshlrev_b32 v154, 16, v167\n" \
  "v_and_b32 v155, 0xffff0000, v167\n" \
  "v_add_f32 v148, v148, v156\n" \
  "v_add_f32 v149, v149, v157\n" \
  "v_add_f32 v150, v150, v158\n" \
  "v_add_f32 v151, v151, v159\n" \
  "v_add_f32 v152, v152, v160\n" \
  "v_add_f32 v153, v153, v161\n" \
  "v_add_f32 v154, v154, v162\n" \
  "v_add_f32 v155, v155, v163\n" \
  "v_cvt_pk_bf16_f32 v164, v148, v149\n" \
  "v_cvt_pk_bf16_f32 v165, v150, v151\n" \
  "v_cvt_pk_bf16_f32 v166, v152, v153\n" \
  "v_cvt_pk_bf16_f32 v167, v154, v155\n" \
  "buffer_store_dwordx4 v[164:167], v3, s[56:59], s50 offen sc1\n" \
  "v_mov_b32 v254, s80\n" \
  "s_add_u32 s50, s50, 40960\n" \
  "s_add_u32 s80, s80, 1\n" \
  "s_mov_b64 exec, 1\n" \
  "buffer_store_dword v254, off, s[36:39], s52 sc1\n" \
  "s_mov_b64 exec, -1\n" \
  "s_waitcnt vmcnt(2)\n" \
  "s_branch NW0%=\n" \
  "NA0%=:\n" \
  "s_waitcnt vmcnt(55)\n" \
  "v_lshlrev_b32 v148, 16, v28\n" \
  "v_and_b32 v149, 0xffff0000, v28\n" \
  "v_lshlrev_b32 v150, 16, v29\n" \
  "v_and_b32 v151, 0xffff0000, v29\n" \
  "v_lshlrev_b32 v152, 16, v30\n" \
  "v_and_b32 v153, 0xffff0000, v30\n" \
  "v_lshlrev_b32 v154, 16, v31\n" \
  "v_and_b32 v155, 0xffff0000, v31\n" \
  "s_waitcnt vmcnt(54)\n" \
  "v_dot2c_f32_bf16 v148, v32, v24\n" \
  "v_dot2c_f32_bf16 v149, v32, v25\n" \
  "v_dot2c_f32_bf16 v150, v33, v24\n" \
  "v_dot2c_f32_bf16 v151, v33, v25\n" \
  "v_dot2c_f32_bf16 v152, v34, v24\n" \
  "v_dot2c_f32_bf16 v153, v34, v25\n" \
  "v_dot2c_f32_bf16 v154, v35, v24\n" \
  "v_dot2c_f32_bf16 v155, v35, v25\n" \
  "s_waitcnt vmcnt(53)\n" \
  "v_dot2c_f32_bf16 v148, v36, v24\n" \
  "v_dot2c_f32_bf16 v149, v36, v25\n" \
  "v_dot2c_f32_bf16 v150, v37, v24\n" \
  "v_dot2c_f32_bf16 v151, v37, v25\n" \
  "v_dot2c_f32_bf16 v152, v38, v24\n" \
  "v_dot2c_f32_bf16 v153, v38, v25\n" \
  "v_dot2c_f32_bf16 v154, v39, v24\n" \
  "v_dot2c_f32_bf16 v155, v39, v25\n" \
  "s_waitcnt vmcnt(52)\n" \
  "v_dot2c_f32_bf16 v148, v40, v24\n" \
  "v_dot2c_f32_bf16 v149, v40, v25\n" \
  "v_dot2c_f32_bf16 v150, v41, v24\n" \
  "v_dot2c_f32_bf16 v151, v41, v25\n" \
  "v_dot2c_f32_bf16 v152, v42, v24\n" \
  "v_dot2c_f32_bf16 v153, v42, v25\n" \
  "v_dot2c_f32_bf16 v154, v43, v24\n" \
  "v_dot2c_f32_bf16 v155, v43, v25\n" \
  "s_waitcnt vmcnt(51)\n" \
  "v_dot2c_f32_bf16 v148, v44, v24\n" \
  "v_dot2c_f32_bf16 v149, v44, v25\n" \
  "v_dot2c_f32_bf16 v150, v45, v24\n" \
  "v_dot2c_f32_bf16 v151, v45, v25\n" \
  "v_dot2c_f32_bf16 v152, v46, v24\n" \
  "v_dot2c_f32_bf16 v153, v46, v25\n" \
  "v_dot2c_f32_bf16 v154, v47, v24\n" \
  "v_dot2c_f32_bf16 v155, v47, v25\n" \
  "s_waitcnt vmcnt(50)\n" \
  "v_dot2c_f32_bf16 v148, v48, v24\n" \
  "v_dot2c_f32_bf16 v149, v48, v25\n" \
  "v_dot2c_f32_bf16 v150, v49, v24\n" \
  "v_dot2c_f32_bf16 v151, v49, v25\n" \
  "v_dot2c_f32_bf16 v152, v50, v24\n" \
  "v_dot2c_f32_bf16 v153, v50, v25\n" \
  "v_dot2c_f32_bf16 v154, v51, v24\n" \
  "v_dot2c_f32_bf16 v155, v51, v25\n" \
  "s_waitcnt vmcnt(49)\n" \
  "v_dot2c_f32_bf16 v148, v52, v24\n" \
  "v_dot2c_f32_bf16 v149, v52, v25\n" \
  "v_dot2c_f32_bf16 v150, v53, v24\n" \
  "v_dot2c_f32_bf16 v151, v53, v25\n" \
  "v_dot2c_f32_bf16 v152, v54, v24\n" \
  "v_dot2c_f32_bf16 v153, v54, v25\n" \
  "v_dot2c_f32_bf16 v154, v55, v24\n" \
  "v_dot2c_f32_bf16 v155, v55, v25\n" \
  "s_waitcnt vmcnt(48)\n" \
  "v_dot2c_f32_bf16 v148, v56, v24\n" \
  "v_dot2c_f32_bf16 v149, v56, v25\n" \
  "v_dot2c_f32_bf16 v150, v57, v24\n" \
  "v_dot2c_f32_bf16 v151, v57, v25\n" \
  "v_dot2c_f32_bf16 v152, v58, v24\n" \
  "v_dot2c_f32_bf16 v153, v58, v25\n" \
  "v_dot2c_f32_bf16 v154, v59, v24\n" \
  "v_dot2c_f32_bf16 v155, v59, v25\n" \
  "s_waitcnt vmcnt(47)\n" \
  "v_dot2c_f32_bf16 v148, v60, v24\n" \
  "v_dot2c_f32_bf16 v149, v60, v25\n" \
  "v_dot2c_f32_bf16 v150, v61, v24\n" \
  "v_dot2c_f32_bf16 v151, v61, v25\n" \
  "v_dot2c_f32_bf16 v152, v62, v24\n" \
  "v_dot2c_f32_bf16 v153, v62, v25\n" \
  "v_dot2c_f32_bf16 v154, v63, v24\n" \
  "v_dot2c_f32_bf16 v155, v63, v25\n" \
  "s_waitcnt vmcnt(46)\n" \
  "v_dot2c_f32_bf16 v148, v64, v24\n" \
  "v_dot2c_f32_bf16 v149, v64, v25\n" \
  "v_dot2c_f32_bf16 v150, v65, v24\n" \
  "v_dot2c_f32_bf16 v151, v65, v25\n" \
  "v_dot2c_f32_bf16 v152, v66, v24\n" \
  "v_dot2c_f32_bf16 v153, v66, v25\n" \
  "v_dot2c_f32_bf16 v154, v67, v24\n" \
  "v_dot2c_f32_bf16 v155, v67, v25\n" \
  "s_waitcnt vmcnt(45)\n" \
  "v_dot2c_f32_bf16 v148, v68, v24\n" \
  "v_dot2c_f32_bf16 v149, v68, v25\n" \
  "v_dot2c_f32_bf16 v150, v69, v24\n" \
  "v_dot2c_f32_bf16 v151, v69, v25\n" \
  "v_dot2c_f32_bf16 v152, v70, v24\n" \
  "v_dot2c_f32_bf16 v153, v70, v25\n" \
  "v_dot2c_f32_bf16 v154, v71, v24\n" \
  "v_dot2c_f32_bf16 v155, v71, v25\n" \
  "s_waitcnt vmcnt(44)\n" \
  "v_dot2c_f32_bf16 v148, v72, v24\n" \
  "v_dot2c_f32_bf16 v149, v72, v25\n" \
  "v_dot2c_f32_bf16 v150, v73, v24\n" \
  "v_dot2c_f32_bf16 v151, v73, v25\n" \
  "v_dot2c_f32_bf16 v152, v74, v24\n" \
  "v_dot2c_f32_bf16 v153, v74, v25\n" \
  "v_dot2c_f32_bf16 v154, v75, v24\n" \
  "v_dot2c_f32_bf16 v155, v75, v25\n" \
  "s_waitcnt vmcnt(43)\n" \
  "v_dot2c_f32_bf16 v148, v76, v24\n" \
  "v_dot2c_f32_bf16 v149, v76, v25\n" \
  "v_dot2c_f32_bf16 v150, v77, v24\n" \
  "v_dot2c_f32_bf16 v151, v77, v25\n" \
  "v_dot2c_f32_bf16 v152, v78, v24\n" \
  "v_dot2c_f32_bf16 v153, v78, v25\n" \
  "v_dot2c_f32_bf16 v154, v79, v24\n" \
  "v_dot2c_f32_bf16 v155, v79, v25\n" \
  "s_waitcnt vmcnt(42)\n" \
  "v_dot2c_f32_bf16 v148, v80, v24\n" \
  "v_dot2c_f32_bf16 v149, v80, v25\n" \
  "v_dot2c_f32_bf16 v150, v81, v24\n" \
  "v_dot2c_f32_bf16 v151, v81, v25\n" \
  "v_dot2c_f32_bf16 v152, v82, v24\n" \
  "v_dot2c_f32_bf16 v153, v82, v25\n" \
  "v_dot2c_f32_bf16 v154, v83, v24\n" \
  "v_dot2c_f32_bf16 v155, v83, v25\n" \
  "s_waitcnt vmcnt(41)\n" \
  "v_lshlrev_b32 v156, 16, v84\n" \
  "v_and_b32 v157, 0xffff0000, v84\n" \
  "v_lshlrev_b32 v158, 16, v85\n" \
  "v_and_b32 v159, 0xffff0000, v85\n" \
  "v_lshlrev_b32 v160, 16, v86\n" \
  "v_and_b32 v161, 0xffff0000, v86\n" \
  "v_lshlrev_b32 v162, 16, v87\n" \
  "v_and_b32 v163, 0xffff0000, v87\n" \
  "v_cvt_pk_bf16_f32 v164, v148, v149\n" \
  "v_cvt_pk_bf16_f32 v165, v150, v151\n" \
  "v_cvt_pk_bf16_f32 v166, v152, v153\n" \
  "v_cvt_pk_bf16_f32 v167, v154, v155\n" \
  "v_lshlrev_b32 v148, 16, v164\n" \
  "v_and_b32 v149, 0xffff0000, v164\n" \
  "v_lshlrev_b32 v150, 16, v165\n" \
  "v_and_b32 v151, 0xffff0000, v165\n" \
  "v_lshlrev_b32 v152, 16, v166\n" \
  "v_and_b32 v153, 0xffff0000, v166\n" \
  "v_lshlrev_b32 v154, 16, v167\n" \
  "v_and_b32 v155, 0xffff0000, v167\n" \
  "v_mul_f32 v148, v148, v16\n" \
  "v_mul_f32 v149, v149, v17\n" \
  "v_mul_f32 v150, v150, v18\n" \
  "v_mul_f32 v151, v151, v19\n" \
  "v_mul_f32 v152, v152, v20\n" \
  "v_mul_f32 v153, v153, v21\n" \
  "v_mul_f32 v154, v154, v22\n" \
  "v_mul_f32 v155, v155, v23\n" \
  "v_cvt_pk_bf16_f32 v164, v148, v149\n" \
  "v_cvt_pk_bf16_f32 v165, v150, v151\n" \
  "v_cvt_pk_bf16_f32 v166, v152, v153\n" \
  "v_cvt_pk_bf16_f32 v167, v154, v155\n" \
  "v_lshlrev_b32 v148, 16, v164\n" \
  "v_and_b32 v149, 0xffff0000, v164\n" \
  "v_lshlrev_b32 v150, 16, v165\n" \
  "v_and_b32 v151, 0xffff0000, v165\n" \
  "v_lshlrev_b32 v152, 16, v166\n" \
  "v_and_b32 v153, 0xffff0000, v166\n" \
  "v_lshlrev_b32 v154, 16, v167\n" \
  "v_and_b32 v155, 0xffff0000, v167\n" \
  "v_add_f32 v148, v148, v156\n" \
  "v_add_f32 v149, v149, v157\n" \
  "v_add_f32 v150, v150, v158\n" \
  "v_add_f32 v151, v151, v159\n" \
  "v_add_f32 v152, v152, v160\n" \
  "v_add_f32 v153, v153, v161\n" \
  "v_add_f32 v154, v154, v162\n" \
  "v_add_f32 v155, v155, v163\n" \
  "v_cvt_pk_bf16_f32 v164, v148, v149\n" \
  "v_cvt_pk_bf16_f32 v165, v150, v151\n" \
  "v_cvt_pk_bf16_f32 v166, v152, v153\n" \
  "v_cvt_pk_bf16_f32 v167, v154, v155\n" \
  "buffer_store_dwordx4 v[164:167], v3, s[56:59], s50 offen sc1\n" \
  "v_mov_b32 v254, s80\n" \
  "s_add_u32 s50, s50, 40960\n" \
  "s_add_u32 s80, s80, 1\n" \
  "s_mov_b64 exec, 1\n" \
  "buffer_store_dword v254, off, s[36:39], s52 sc1\n" \
  "s_mov_b64 exec, -1\n" \
  "NW0%=:\n" \
  "s_cmp_ge_u32 s80, 64\n" \
  "s_cbranch_scc1 DONE%=\n" \
  "v_cmp_gt_i32 vcc, s53, v255\n" \
  "s_cbranch_vccnz NBw0%=\n" \
  "s_add_u32 s85, s48, 0\n" \
  "buffer_load_dwordx4 v[88:91], v1, s[44:47], s85 offen offset:0 sc1\n" \
  "buffer_load_dwordx4 v[92:95], v1, s[44:47], s85 offen offset:1024 sc1\n" \
  "buffer_load_dwordx4 v[96:99], v1, s[44:47], s85 offen offset:2048 sc1\n" \
  "buffer_load_dwordx4 v[100:103], v1, s[44:47], s85 offen offset:3072 sc1\n" \
  "s_add_u32 s85, s48, 4096\n" \
  "buffer_load_dwordx4 v[104:107], v1, s[44:47], s85 offen offset:0 sc1\n" \
  "buffer_load_dwordx4 v[108:111], v1, s[44:47], s85 offen offset:1024 sc1\n" \
  "buffer_load_dwordx4 v[112:115], v1, s[44:47], s85 offen offset:2048 sc1\n" \
  "buffer_load_dwordx4 v[116:119], v1, s[44:47], s85 offen offset:3072 sc1\n" \
  "s_add_u32 s85, s48, 8192\n" \
  "buffer_load_dwordx4 v[120:123], v1, s[44:47], s85 offen offset:0 sc1\n" \
  "buffer_load_dwordx4 v[124:127], v1, s[44:47], s85 offen offset:1024 sc1\n" \
  "buffer_load_dwordx4 v[128:131], v1, s[44:47], s85 offen offset:2048 sc1\n" \
  "buffer_load_dwordx4 v[132:135], v1, s[44:47], s85 offen offset:3072 sc1\n" \
  "s_add_u32 s85, s48, 12288\n" \
  "buffer_load_dwordx4 v[136:139], v1, s[44:47], s85 offen offset:0 sc1\n" \
  "buffer_load_dwordx4 v[140:143], v1, s[44:47], s85 offen offset:1024 sc1\n" \
  "buffer_load_dwordx4 v[144:147], v2, s[44:47], s49 offen sc1\n" \
  "s_add_u32 s48, s48, 0x100000\n" \
  "s_bitset0_b32 s48, 24\n" \
  "s_add_u32 s49, s49, 40960\n" \
  "s_add_u32 s53, s53, 1\n" \
  "buffer_load_dword v255, v9, s[36:39], 0 offen sc1\n" \
  "s_waitcnt vmcnt(0)\n" \
  "s_branch NX0%=\n" \
  "NBw0%=:\n" \
  "s_cmp_eq_u32 s55, 0\n" \
  "s_cbranch_scc1 NCw0%=\n" \
  "s_sub_u32 s55, s55, 1\n" \
  "s_sleep 4\n" \
  "buffer_load_dword v255, v9, s[36:39], 0 offen sc1\n" \
  "s_waitcnt vmcnt(0)\n" \
  "v_cmp_gt_i32 vcc, s53, v255\n" \
  "s_cbranch_vccnz NBw0%=\n" \
  "NCw0%=:\n" \
  "s_add_u32 s85, s48, 0\n" \
  "buffer_load_dwordx4 v[88:91], v1, s[44:47], s85 offen offset:0 sc1\n" \
  "buffer_load_dwordx4 v[92:95], v1, s[44:47], s85 offen offset:1024 sc1\n" \
  "buffer_load_dwordx4 v[96:99], v1, s[44:47], s85 offen offset:2048 sc1\n" \
  "buffer_load_dwordx4 v[100:103], v1, s[44:47], s85 offen offset:3072 sc1\n" \
  "s_add_u32 s85, s48, 4096\n" \
  "buffer_load_dwordx4 v[104:107], v1, s[44:47], s85 offen offset:0 sc1\n" \
  "buffer_load_dwordx4 v[108:111], v1, s[44:47], s85 offen offset:1024 sc1\n" \
  "buffer_load_dwordx4 v[112:115], v1, s[44:47], s85 offen offset:2048 sc1\n" \
  "buffer_load_dwordx4 v[116:119], v1, s[44:47], s85 offen offset:3072 sc1\n" \
  "s_add_u32 s85, s48, 8192\n" \
  "buffer_load_dwordx4 v[120:123], v1, s[44:47], s85 offen offset:0 sc1\n" \
  "buffer_load_dwordx4 v[124:127], v1, s[44:47], s85 offen offset:1024 sc1\n" \
  "buffer_load_dwordx4 v[128:131], v1, s[44:47], s85 offen offset:2048 sc1\n" \
  "buffer_load_dwordx4 v[132:135], v1, s[44:47], s85 offen offset:3072 sc1\n" \
  "s_add_u32 s85, s48, 12288\n" \
  "buffer_load_dwordx4 v[136:139], v1, s[44:47], s85 offen offset:0 sc1\n" \
  "buffer_load_dwordx4 v[140:143], v1, s[44:47], s85 offen offset:1024 sc1\n" \
  "buffer_load_dwordx4 v[144:147], v2, s[44:47], s49 offen sc1\n" \
  "s_add_u32 s48, s48, 0x100000\n" \
  "s_bitset0_b32 s48, 24\n" \
  "s_add_u32 s49, s49, 40960\n" \
  "s_add_u32 s53, s53, 1\n" \
  "buffer_load_dword v255, v9, s[36:39], 0 offen sc1\n" \
  "s_branch SL1%=\n" \
  "NX0%=:\n" \
  "s_waitcnt vmcnt(2)\n" \
  "v_cmp_gt_i32 vcc, s53, v255\n" \
  "s_cbranch_vccnz NA1%=\n" \
  "s_add_u32 s85, s48, 0\n" \
  "buffer_load_dwordx4 v[28:31], v1, s[44:47], s85 offen offset:0 sc1\n" \
  "buffer_load_dwordx4 v[32:35], v1, s[44:47], s85 offen offset:1024 sc1\n" \
  "buffer_load_dwordx4 v[36:39], v1, s[44:47], s85 offen offset:2048 sc1\n" \
  "buffer_load_dwordx4 v[40:43], v1, s[44:47], s85 offen offset:3072 sc1\n" \
  "s_add_u32 s85, s48, 4096\n" \
  "buffer_load_dwordx4 v[44:47], v1, s[44:47], s85 offen offset:0 sc1\n" \
  "buffer_load_dwordx4 v[48:51], v1, s[44:47], s85 offen offset:1024 sc1\n" \
  "buffer_load_dwordx4 v[52:55], v1, s[44:47], s85 offen offset:2048 sc1\n" \
  "buffer_load_dwordx4 v[56:59], v1, s[44:47], s85 offen offset:3072 sc1\n" \
  "s_add_u32 s85, s48, 8192\n" \
  "buffer_load_dwordx4 v[60:63], v1, s[44:47], s85 offen offset:0 sc1\n" \
  "buffer_load_dwordx4 v[64:67], v1, s[44:47], s85 offen offset:1024 sc1\n" \
  "buffer_load_dwordx4 v[68:71], v1, s[44:47], s85 offen offset:2048 sc1\n" \
  "buffer_load_dwordx4 v[72:75], v1, s[44:47], s85 offen offset:3072 sc1\n" \
  "s_add_u32 s85, s48, 12288\n" \
  "buffer_load_dwordx4 v[76:79], v1, s[44:47], s85 offen offset:0 sc1\n" \
  "buffer_load_dwordx4 v[80:83], v1, s[44:47], s85 offen offset:1024 sc1\n" \
  "buffer_load_dwordx4 v[84:87], v2, s[44:47], s49 offen sc1\n" \
  "s_add_u32 s48, s48, 0x100000\n" \
  "s_bitset0_b32 s48, 24\n" \
  "s_add_u32 s49, s49, 40960\n" \
  "s_add_u32 s53, s53, 1\n" \
  "buffer_load_dword v255, v9, s[36:39], 0 offen sc1\n" \
  "s_waitcnt vmcnt(55)\n" \
  "v_lshlrev_b32 v148, 16, v88\n" \
  "v_and_b32 v149, 0xffff0000, v88\n" \
  "v_lshlrev_b32 v150, 16, v89\n" \
  "v_and_b32 v151, 0xffff0000, v89\n" \
  "v_lshlrev_b32 v152, 16, v90\n" \
  "v_and_b32 v153, 0xffff0000, v90\n" \
  "v_lshlrev_b32 v154, 16, v91\n" \
  "v_and_b32 v155, 0xffff0000, v91\n" \
  "s_waitcnt vmcnt(54)\n" \
  "v_dot2c_f32_bf16 v148, v92, v24\n" \
  "v_dot2c_f32_bf16 v149, v92, v25\n" \
  "v_dot2c_f32_bf16 v150, v93, v24\n" \
  "v_dot2c_f32_bf16 v151, v93, v25\n" \
  "v_dot2c_f32_bf16 v152, v94, v24\n" \
  "v_dot2c_f32_bf16 v153, v94, v25\n" \
  "v_dot2c_f32_bf16 v154, v95, v24\n" \
  "v_dot2c_f32_bf16 v155, v95, v25\n" \
  "s_waitcnt vmcnt(53)\n" \
  "v_dot2c_f32_bf16 v148, v96, v24\n" \
  "v_dot2c_f32_bf16 v149, v96, v25\n" \
  "v_dot2c_f32_bf16 v150, v97, v24\n" \
  "v_dot2c_f32_bf16 v151, v97, v25\n" \
  "v_dot2c_f32_bf16 v152, v98, v24\n" \
  "v_dot2c_f32_bf16 v153, v98, v25\n" \
  "v_dot2c_f32_bf16 v154, v99, v24\n" \
  "v_dot2c_f32_bf16 v155, v99, v25\n" \
  "s_waitcnt vmcnt(52)\n" \
  "v_dot2c_f32_bf16 v148, v100, v24\n" \
  "v_dot2c_f32_bf16 v149, v100, v25\n" \
  "v_dot2c_f32_bf16 v150, v101, v24\n" \
  "v_dot2c_f32_bf16 v151, v101, v25\n" \
  "v_dot2c_f32_bf16 v152, v102, v24\n" \
  "v_dot2c_f32_bf16 v153, v102, v25\n" \
  "v_dot2c_f32_bf16 v154, v103, v24\n" \
  "v_dot2c_f32_bf16 v155, v103, v25\n" \
  "s_waitcnt vmcnt(51)\n" \
  "v_dot2c_f32_bf16 v148, v104, v24\n" \
  "v_dot2c_f32_bf16 v149, v104, v25\n" \
  "v_dot2c_f32_bf16 v150, v105, v24\n" \
  "v_dot2c_f32_bf16 v151, v105, v25\n" \
  "v_dot2c_f32_bf16 v152, v106, v24\n" \
  "v_dot2c_f32_bf16 v153, v106, v25\n" \
  "v_dot2c_f32_bf16 v154, v107, v24\n" \
  "v_dot2c_f32_bf16 v155, v107, v25\n" \
  "s_waitcnt vmcnt(50)\n" \
  "v_dot2c_f32_bf16 v148, v108, v24\n" \
  "v_dot2c_f32_bf16 v149, v108, v25\n" \
  "v_dot2c_f32_bf16 v150, v109, v24\n" \
  "v_dot2c_f32_bf16 v151, v109, v25\n" \
  "v_dot2c_f32_bf16 v152, v110, v24\n" \
  "v_dot2c_f32_bf16 v153, v110, v25\n" \
  "v_dot2c_f32_bf16 v154, v111, v24\n" \
  "v_dot2c_f32_bf16 v155, v111, v25\n" \
  "s_waitcnt vmcnt(49)\n" \
  "v_dot2c_f32_bf16 v148, v112, v24\n" \
  "v_dot2c_f32_bf16 v149, v112, v25\n" \
  "v_dot2c_f32_bf16 v150, v113, v24\n" \
  "v_dot2c_f32_bf16 v151, v113, v25\n" \
  "v_dot2c_f32_bf16 v152, v114, v24\n" \
  "v_dot2c_f32_bf16 v153, v114, v25\n" \
  "v_dot2c_f32_bf16 v154, v115, v24\n" \
  "v_dot2c_f32_bf16 v155, v115, v25\n" \
  "s_waitcnt vmcnt(48)\n" \
  "v_dot2c_f32_bf16 v148, v116, v24\n" \
  "v_dot2c_f32_bf16 v149, v116, v25\n" \
  "v_dot2c_f32_bf16 v150, v117, v24\n" \
  "v_dot2c_f32_bf16 v151, v117, v25\n" \
  "v_dot2c_f32_bf16 v152, v118, v24\n" \
  "v_dot2c_f32_bf16 v153, v118, v25\n" \
  "v_dot2c_f32_bf16 v154, v119, v24\n" \
  "v_dot2c_f32_bf16 v155, v119, v25\n" \
  "s_waitcnt vmcnt(47)\n" \
  "v_dot2c_f32_bf16 v148, v120, v24\n" \
  "v_dot2c_f32_bf16 v149, v120, v25\n" \
  "v_dot2c_f32_bf16 v150, v121, v24\n" \
  "v_dot2c_f32_bf16 v151, v121, v25\n" \
  "v_dot2c_f32_bf16 v152, v122, v24\n" \
  "v_dot2c_f32_bf16 v153, v122, v25\n" \
  "v_dot2c_f32_bf16 v154, v123, v24\n" \
  "v_dot2c_f32_bf16 v155, v123, v25\n" \
  "s_waitcnt vmcnt(46)\n" \
  "v_dot2c_f32_bf16 v148, v124, v24\n" \
  "v_dot2c_f32_bf16 v149, v124, v25\n" \
  "v_dot2c_f32_bf16 v150, v125, v24\n" \
  "v_dot2c_f32_bf16 v151, v125, v25\n" \
  "v_dot2c_f32_bf16 v152, v126, v24\n" \
  "v_dot2c_f32_bf16 v153, v126, v25\n" \
  "v_dot2c_f32_bf16 v154, v127, v24\n" \
  "v_dot2c_f32_bf16 v155, v127, v25\n" \
  "s_waitcnt vmcnt(45)\n" \
  "v_dot2c_f32_bf16 v148, v128, v24\n" \
  "v_dot2c_f32_bf16 v149, v128, v25\n" \
  "v_dot2c_f32_bf16 v150, v129, v24\n" \
  "v_dot2c_f32_bf16 v151, v129, v25\n" \
  "v_dot2c_f32_bf16 v152, v130, v24\n" \
  "v_dot2c_f32_bf16 v153, v130, v25\n" \
  "v_dot2c_f32_bf16 v154, v131, v24\n" \
  "v_dot2c_f32_bf16 v155, v131, v25\n" \
  "s_waitcnt vmcnt(44)\n" \
  "v_dot2c_f32_bf16 v148, v132, v24\n" \
  "v_dot2c_f32_bf16 v149, v132, v25\n" \
  "v_dot2c_f32_bf16 v150, v133, v24\n" \
  "v_dot2c_f32_bf16 v151, v133, v25\n" \
  "v_dot2c_f32_bf16 v152, v134, v24\n" \
  "v_dot2c_f32_bf16 v153, v134, v25\n" \
  "v_dot2c_f32_bf16 v154, v135, v24\n" \
  "v_dot2c_f32_bf16 v155, v135, v25\n" \
  "s_waitcnt vmcnt(43)\n" \
  "v_dot2c_f32_bf16 v148, v136, v24\n" \
  "v_dot2c_f32_bf16 v149, v136, v25\n" \
  "v_dot2c_f32_bf16 v150, v137, v24\n" \
  "v_dot2c_f32_bf16 v151, v137, v25\n" \
  "v_dot2c_f32_bf16 v152, v138, v24\n" \
  "v_dot2c_f32_bf16 v153, v138, v25\n" \
  "v_dot2c_f32_bf16 v154, v139, v24\n" \
  "v_dot2c_f32_bf16 v155, v139, v25\n" \
  "s_waitcnt vmcnt(42)\n" \
  "v_dot2c_f32_bf16 v148, v140, v24\n" \
  "v_dot2c_f32_bf16 v149, v140, v25\n" \
  "v_dot2c_f32_bf16 v150, v141, v24\n" \
  "v_dot2c_f32_bf16 v151, v141, v25\n" \
  "v_dot2c_f32_bf16 v152, v142, v24\n" \
  "v_dot2c_f32_bf16 v153, v142, v25\n" \
  "v_dot2c_f32_bf16 v154, v143, v24\n" \
  "v_dot2c_f32_bf16 v155, v143, v25\n" \
  "s_waitcnt vmcnt(41)\n" \
  "v_lshlrev_b32 v156, 16, v144\n" \
  "v_and_b32 v157, 0xffff0000, v144\n" \
  "v_lshlrev_b32 v158, 16, v145\n" \
  "v_and_b32 v159, 0xffff0000, v145\n" \
  "v_lshlrev_b32 v160, 16, v146\n" \
  "v_and_b32 v161, 0xffff0000, v146\n" \
  "v_lshlrev_b32 v162, 16, v147\n" \
  "v_and_b32 v163, 0xffff0000, v147\n" \
  "v_cvt_pk_bf16_f32 v164, v148, v149\n" \
  "v_cvt_pk_bf16_f32 v165, v150, v151\n" \
  "v_cvt_pk_bf16_f32 v166, v152, v153\n" \
  "v_cvt_pk_bf16_f32 v167, v154, v155\n" \
  "v_lshlrev_b32 v148, 16, v164\n" \
  "v_and_b32 v149, 0xffff0000, v164\n" \
  "v_lshlrev_b32 v150, 16, v165\n" \
  "v_and_b32 v151, 0xffff0000, v165\n" \
  "v_lshlrev_b32 v152, 16, v166\n" \
  "v_and_b32 v153, 0xffff0000, v166\n" \
  "v_lshlrev_b32 v154, 16, v167\n" \
  "v_and_b32 v155, 0xffff0000, v167\n" \
  "v_mul_f32 v148, v148, v16\n" \
  "v_mul_f32 v149, v149, v17\n" \
  "v_mul_f32 v150, v150, v18\n" \
  "v_mul_f32 v151, v151, v19\n" \
  "v_mul_f32 v152, v152, v20\n" \
  "v_mul_f32 v153, v153, v21\n" \
  "v_mul_f32 v154, v154, v22\n" \
  "v_mul_f32 v155, v155, v23\n" \
  "v_cvt_pk_bf16_f32 v164, v148, v149\n" \
  "v_cvt_pk_bf16_f32 v165, v150, v151\n" \
  "v_cvt_pk_bf16_f32 v166, v152, v153\n" \
  "v_cvt_pk_bf16_f32 v167, v154, v155\n" \
  "v_lshlrev_b32 v148, 16, v164\n" \
  "v_and_b32 v149, 0xffff0000, v164\n" \
  "v_lshlrev_b32 v150, 16, v165\n" \
  "v_and_b32 v151, 0xffff0000, v165\n" \
  "v_lshlrev_b32 v152, 16, v166\n" \
  "v_and_b32 v153, 0xffff0000, v166\n" \
  "v_lshlrev_b32 v154, 16, v167\n" \
  "v_and_b32 v155, 0xffff0000, v167\n" \
  "v_add_f32 v148, v148, v156\n" \
  "v_add_f32 v149, v149, v157\n" \
  "v_add_f32 v150, v150, v158\n" \
  "v_add_f32 v151, v151, v159\n" \
  "v_add_f32 v152, v152, v160\n" \
  "v_add_f32 v153, v153, v161\n" \
  "v_add_f32 v154, v154, v162\n" \
  "v_add_f32 v155, v155, v163\n" \
  "v_cvt_pk_bf16_f32 v164, v148, v149\n" \
  "v_cvt_pk_bf16_f32 v165, v150, v151\n" \
  "v_cvt_pk_bf16_f32 v166, v152, v153\n" \
  "v_cvt_pk_bf16_f32 v167, v154, v155\n" \
  "buffer_store_dwordx4 v[164:167], v3, s[56:59], s50 offen sc1\n" \
  "v_mov_b32 v254, s80\n" \
  "s_add_u32 s50, s50, 40960\n" \
  "s_add_u32 s80, s80, 1\n" \
  "s_mov_b64 exec, 1\n" \
  "buffer_store_dword v254, off, s[36:39], s52 sc1\n" \
  "s_mov_b64 exec, -1\n" \
  "s_branch NX1%=\n" \
  "SL1%=:\n" \
  "s_waitcnt vmcnt(15)\n" \
  "v_lshlrev_b32 v148, 16, v88\n" \
  "v_and_b32 v149, 0xffff0000, v88\n" \
  "v_lshlrev_b32 v150, 16, v89\n" \
  "v_and_b32 v151, 0xffff0000, v89\n" \
  "v_lshlrev_b32 v152, 16, v90\n" \
  "v_and_b32 v153, 0xffff0000, v90\n" \
  "v_lshlrev_b32 v154, 16, v91\n" \
  "v_and_b32 v155, 0xffff0000, v91\n" \
  "s_waitcnt vmcnt(14)\n" \
  "v_dot2c_f32_bf16 v148, v92, v24\n" \
  "v_dot2c_f32_bf16 v149, v92, v25\n" \
  "v_dot2c_f32_bf16 v150, v93, v24\n" \
  "v_dot2c_f32_bf16 v151, v93, v25\n" \
  "v_dot2c_f32_bf16 v152, v94, v24\n" \
  "v_dot2c_f32_bf16 v153, v94, v25\n" \
  "v_dot2c_f32_bf16 v154, v95, v24\n" \
  "v_dot2c_f32_bf16 v155, v95, v25\n" \
  "s_waitcnt vmcnt(13)\n" \
  "v_dot2c_f32_bf16 v148, v96, v24\n" \
  "v_dot2c_f32_bf16 v149, v96, v25\n" \
  "v_dot2c_f32_bf16 v150, v97, v24\n" \
  "v_dot2c_f32_bf16 v151, v97, v25\n" \
  "v_dot2c_f32_bf16 v152, v98, v24\n" \
  "v_dot2c_f32_bf16 v153, v98, v25\n" \
  "v_dot2c_f32_bf16 v154, v99, v24\n" \
  "v_dot2c_f32_bf16 v155, v99, v25\n" \
  "s_waitcnt vmcnt(12)\n" \
  "v_dot2c_f32_bf16 v148, v100, v24\n" \
  "v_dot2c_f32_bf16 v149, v100, v25\n" \
  "v_dot2c_f32_bf16 v150, v101, v24\n" \
  "v_dot2c_f32_bf16 v151, v101, v25\n" \
  "v_dot2c_f32_bf16 v152, v102, v24\n" \
  "v_dot2c_f32_bf16 v153, v102, v25\n" \
  "v_dot2c_f32_bf16 v154, v103, v24\n" \
  "v_dot2c_f32_bf16 v155, v103, v25\n" \
  "s_waitcnt vmcnt(11)\n" \
  "v_dot2c_f32_bf16 v148, v104, v24\n" \
  "v_dot2c_f32_bf16 v149, v104, v25\n" \
  "v_dot2c_f32_bf16 v150, v105, v24\n" \
  "v_dot2c_f32_bf16 v151, v105, v25\n" \
  "v_dot2c_f32_bf16 v152, v106, v24\n" \
  "v_dot2c_f32_bf16 v153, v106, v25\n" \
  "v_dot2c_f32_bf16 v154, v107, v24\n" \
  "v_dot2c_f32_bf16 v155, v107, v25\n" \
  "s_waitcnt vmcnt(10)\n" \
  "v_dot2c_f32_bf16 v148, v108, v24\n" \
  "v_dot2c_f32_bf16 v149, v108, v25\n" \
  "v_dot2c_f32_bf16 v150, v109, v24\n" \
  "v_dot2c_f32_bf16 v151, v109, v25\n" \
  "v_dot2c_f32_bf16 v152, v110, v24\n" \
  "v_dot2c_f32_bf16 v153, v110, v25\n" \
  "v_dot2c_f32_bf16 v154, v111, v24\n" \
  "v_dot2c_f32_bf16 v155, v111, v25\n" \
  "s_waitcnt vmcnt(9)\n" \
  "v_dot2c_f32_bf16 v148, v112, v24\n" \
  "v_dot2c_f32_bf16 v149, v112, v25\n" \
  "v_dot2c_f32_bf16 v150, v113, v24\n" \
  "v_dot2c_f32_bf16 v151, v113, v25\n" \
  "v_dot2c_f32_bf16 v152, v114, v24\n" \
  "v_dot2c_f32_bf16 v153, v114, v25\n" \
  "v_dot2c_f32_bf16 v154, v115, v24\n" \
  "v_dot2c_f32_bf16 v155, v115, v25\n" \
  "s_waitcnt vmcnt(8)\n" \
  "v_dot2c_f32_bf16 v148, v116, v24\n" \
  "v_dot2c_f32_bf16 v149, v116, v25\n" \
  "v_dot2c_f32_bf16 v150, v117, v24\n" \
  "v_dot2c_f32_bf16 v151, v117, v25\n" \
  "v_dot2c_f32_bf16 v152, v118, v24\n" \
  "v_dot2c_f32_bf16 v153, v118, v25\n" \
  "v_dot2c_f32_bf16 v154, v119, v24\n" \
  "v_dot2c_f32_bf16 v155, v119, v25\n" \
  "s_waitcnt vmcnt(7)\n" \
  "v_dot2c_f32_bf16 v148, v120, v24\n" \
  "v_dot2c_f32_bf16 v149, v120, v25\n" \
  "v_dot2c_f32_bf16 v150, v121, v24\n" \
  "v_dot2c_f32_bf16 v151, v121, v25\n" \
  "v_dot2c_f32_bf16 v152, v122, v24\n" \
  "v_dot2c_f32_bf16 v153, v122, v25\n" \
  "v_dot2c_f32_bf16 v154, v123, v24\n" \
  "v_dot2c_f32_bf16 v155, v123, v25\n" \
  "s_waitcnt vmcnt(6)\n" \
  "v_dot2c_f32_bf16 v148, v124, v24\n" \
  "v_dot2c_f32_bf16 v149, v124, v25\n" \
  "v_dot2c_f32_bf16 v150, v125, v24\n" \
  "v_dot2c_f32_bf16 v151, v125, v25\n" \
  "v_dot2c_f32_bf16 v152, v126, v24\n" \
  "v_dot2c_f32_bf16 v153, v126, v25\n" \
  "v_dot2c_f32_bf16 v154, v127, v24\n" \
  "v_dot2c_f32_bf16 v155, v127, v25\n" \
  "s_waitcnt vmcnt(5)\n" \
  "v_dot2c_f32_bf16 v148, v128, v24\n" \
  "v_dot2c_f32_bf16 v149, v128, v25\n" \
  "v_dot2c_f32_bf16 v150, v129, v24\n" \
  "v_dot2c_f32_bf16 v151, v129, v25\n" \
  "v_dot2c_f32_bf16 v152, v130, v24\n" \
  "v_dot2c_f32_bf16 v153, v130, v25\n" \
  "v_dot2c_f32_bf16 v154, v131, v24\n" \
  "v_dot2c_f32_bf16 v155, v131, v25\n" \
  "s_waitcnt vmcnt(4)\n" \
  "v_dot2c_f32_bf16 v148, v132, v24\n" \
  "v_dot2c_f32_bf16 v149, v132, v25\n" \
  "v_dot2c_f32_bf16 v150, v133, v24\n" \
  "v_dot2c_f32_bf16 v151, v133, v25\n" \
  "v_dot2c_f32_bf16 v152, v134, v24\n" \
  "v_dot2c_f32_bf16 v153, v134, v25\n" \
  "v_dot2c_f32_bf16 v154, v135, v24\n" \
  "v_dot2c_f32_bf16 v155, v135, v25\n" \
  "s_waitcnt vmcnt(3)\n" \
  "v_dot2c_f32_bf16 v148, v136, v24\n" \
  "v_dot2c_f32_bf16 v149, v136, v25\n" \
  "v_dot2c_f32_bf16 v150, v137, v24\n" \
  "v_dot2c_f32_bf16 v151, v137, v25\n" \
  "v_dot2c_f32_bf16 v152, v138, v24\n" \
  "v_dot2c_f32_bf16 v153, v138, v25\n" \
  "v_dot2c_f32_bf16 v154, v139, v24\n" \
  "v_dot2c_f32_bf16 v155, v139, v25\n" \
  "s_waitcnt vmcnt(2)\n" \
  "v_dot2c_f32_bf16 v148, v140, v24\n" \
  "v_dot2c_f32_bf16 v149, v140, v25\n" \
  "v_dot2c_f32_bf16 v150, v141, v24\n" \
  "v_dot2c_f32_bf16 v151, v141, v25\n" \
  "v_dot2c_f32_bf16 v152, v142, v24\n" \
  "v_dot2c_f32_bf16 v153, v142, v25\n" \
  "v_dot2c_f32_bf16 v154, v143, v24\n" \
  "v_dot2c_f32_bf16 v155, v143, v25\n" \
  "s_waitcnt vmcnt(1)\n" \
  "v_lshlrev_b32 v156, 16, v144\n" \
  "v_and_b32 v157, 0xffff0000, v144\n" \
  "v_lshlrev_b32 v158, 16, v145\n" \
  "v_and_b32 v159, 0xffff0000, v145\n" \
  "v_lshlrev_b32 v160, 16, v146\n" \
  "v_and_b32 v161, 0xffff0000, v146\n" \
  "v_lshlrev_b32 v162, 16, v147\n" \
  "v_and_b32 v163, 0xffff0000, v147\n" \
  "v_cvt_pk_bf16_f32 v164, v148, v149\n" \
  "v_cvt_pk_bf16_f32 v165, v150, v151\n" \
  "v_cvt_pk_bf16_f32 v166, v152, v153\n" \
  "v_cvt_pk_bf16_f32 v167, v154, v155\n" \
  "v_lshlrev_b32 v148, 16, v164\n" \
  "v_and_b32 v149, 0xffff0000, v164\n" \
  "v_lshlrev_b32 v150, 16, v165\n" \
  "v_and_b32 v151, 0xffff0000, v165\n" \
  "v_lshlrev_b32 v152, 16, v166\n" \
  "v_and_b32 v153, 0xffff0000, v166\n" \
  "v_lshlrev_b32 v154, 16, v167\n" \
  "v_and_b32 v155, 0xffff0000, v167\n" \
  "v_mul_f32 v148, v148, v16\n" \
  "v_mul_f32 v149, v149, v17\n" \
  "v_mul_f32 v150, v150, v18\n" \
  "v_mul_f32 v151, v151, v19\n" \
  "v_mul_f32 v152, v152, v20\n" \
  "v_mul_f32 v153, v153, v21\n" \
  "v_mul_f32 v154, v154, v22\n" \
  "v_mul_f32 v155, v155, v23\n" \
  "v_cvt_pk_bf16_f32 v164, v148, v149\n" \
  "v_cvt_pk_bf16_f32 v165, v150, v151\n" \
  "v_cvt_pk_bf16_f32 v166, v152, v153\n" \
  "v_cvt_pk_bf16_f32 v167, v154, v155\n" \
  "v_lshlrev_b32 v148, 16, v164\n" \
  "v_and_b32 v149, 0xffff0000, v164\n" \
  "v_lshlrev_b32 v150, 16, v165\n" \
  "v_and_b32 v151, 0xffff0000, v165\n" \
  "v_lshlrev_b32 v152, 16, v166\n" \
  "v_and_b32 v153, 0xffff0000, v166\n" \
  "v_lshlrev_b32 v154, 16, v167\n" \
  "v_and_b32 v155, 0xffff0000, v167\n" \
  "v_add_f32 v148, v148, v156\n" \
  "v_add_f32 v149, v149, v157\n" \
  "v_add_f32 v150, v150, v158\n" \
  "v_add_f32 v151, v151, v159\n" \
  "v_add_f32 v152, v152, v160\n" \
  "v_add_f32 v153, v153, v161\n" \
  "v_add_f32 v154, v154, v162\n" \
  "v_add_f32 v155, v155, v163\n" \
  "v_cvt_pk_bf16_f32 v164, v148, v149\n" \
  "v_cvt_pk_bf16_f32 v165, v150, v151\n" \
  "v_cvt_pk_bf16_f32 v166, v152, v153\n" \
  "v_cvt_pk_bf16_f32 v167, v154, v155\n" \
  "buffer_store_dwordx4 v[164:167], v3, s[56:59], s50 offen sc1\n" \
  "v_mov_b32 v254, s80\n" \
  "s_add_u32 s50, s50, 40960\n" \
  "s_add_u32 s80, s80, 1\n" \
  "s_mov_b64 exec, 1\n" \
  "buffer_store_dword v254, off, s[36:39], s52 sc1\n" \
  "s_mov_b64 exec, -1\n" \
  "s_waitcnt vmcnt(2)\n" \
  "s_branch NW1%=\n" \
  "NA1%=:\n" \
  "s_waitcnt vmcnt(55)\n" \
  "v_lshlrev_b32 v148, 16, v88\n" \
  "v_and_b32 v149, 0xffff0000, v88\n" \
  "v_lshlrev_b32 v150, 16, v89\n" \
  "v_and_b32 v151, 0xffff0000, v89\n" \
  "v_lshlrev_b32 v152, 16, v90\n" \
  "v_and_b32 v153, 0xffff0000, v90\n" \
  "v_lshlrev_b32 v154, 16, v91\n" \
  "v_and_b32 v155, 0xffff0000, v91\n" \
  "s_waitcnt vmcnt(54)\n" \
  "v_dot2c_f32_bf16 v148, v92, v24\n" \
  "v_dot2c_f32_bf16 v149, v92, v25\n" \
  "v_dot2c_f32_bf16 v150, v93, v24\n" \
  "v_dot2c_f32_bf16 v151, v93, v25\n" \
  "v_dot2c_f32_bf16 v152, v94, v24\n" \
  "v_dot2c_f32_bf16 v153, v94, v25\n" \
  "v_dot2c_f32_bf16 v154, v95, v24\n" \
  "v_dot2c_f32_bf16 v155, v95, v25\n" \
  "s_waitcnt vmcnt(53)\n" \
  "v_dot2c_f32_bf16 v148, v96, v24\n" \
  "v_dot2c_f32_bf16 v149, v96, v25\n" \
  "v_dot2c_f32_bf16 v150, v97, v24\n" \
  "v_dot2c_f32_bf16 v151, v97, v25\n" \
  "v_dot2c_f32_bf16 v152, v98, v24\n" \
  "v_dot2c_f32_bf16 v153, v98, v25\n" \
  "v_dot2c_f32_bf16 v154, v99, v24\n" \
  "v_dot2c_f32_bf16 v155, v99, v25\n" \
  "s_waitcnt vmcnt(52)\n" \
  "v_dot2c_f32_bf16 v148, v100, v24\n" \
  "v_dot2c_f32_bf16 v149, v100, v25\n" \
  "v_dot2c_f32_bf16 v150, v101, v24\n" \
  "v_dot2c_f32_bf16 v151, v101, v25\n" \
  "v_dot2c_f32_bf16 v152, v102, v24\n" \
  "v_dot2c_f32_bf16 v153, v102, v25\n" \
  "v_dot2c_f32_bf16 v154, v103, v24\n" \
  "v_dot2c_f32_bf16 v155, v103, v25\n" \
  "s_waitcnt vmcnt(51)\n" \
  "v_dot2c_f32_bf16 v148, v104, v24\n" \
  "v_dot2c_f32_bf16 v149, v104, v25\n" \
  "v_dot2c_f32_bf16 v150, v105, v24\n" \
  "v_dot2c_f32_bf16 v151, v105, v25\n" \
  "v_dot2c_f32_bf16 v152, v106, v24\n" \
  "v_dot2c_f32_bf16 v153, v106, v25\n" \
  "v_dot2c_f32_bf16 v154, v107, v24\n" \
  "v_dot2c_f32_bf16 v155, v107, v25\n" \
  "s_waitcnt vmcnt(50)\n" \
  "v_dot2c_f32_bf16 v148, v108, v24\n" \
  "v_dot2c_f32_bf16 v149, v108, v25\n" \
  "v_dot2c_f32_bf16 v150, v109, v24\n" \
  "v_dot2c_f32_bf16 v151, v109, v25\n" \
  "v_dot2c_f32_bf16 v152, v110, v24\n" \
  "v_dot2c_f32_bf16 v153, v110, v25\n" \
  "v_dot2c_f32_bf16 v154, v111, v24\n" \
  "v_dot2c_f32_bf16 v155, v111, v25\n" \
  "s_waitcnt vmcnt(49)\n" \
  "v_dot2c_f32_bf16 v148, v112, v24\n" \
  "v_dot2c_f32_bf16 v149, v112, v25\n" \
  "v_dot2c_f32_bf16 v150, v113, v24\n" \
  "v_dot2c_f32_bf16 v151, v113, v25\n" \
  "v_dot2c_f32_bf16 v152, v114, v24\n" \
  "v_dot2c_f32_bf16 v153, v114, v25\n" \
  "v_dot2c_f32_bf16 v154, v115, v24\n" \
  "v_dot2c_f32_bf16 v155, v115, v25\n" \
  "s_waitcnt vmcnt(48)\n" \
  "v_dot2c_f32_bf16 v148, v116, v24\n" \
  "v_dot2c_f32_bf16 v149, v116, v25\n" \
  "v_dot2c_f32_bf16 v150, v117, v24\n" \
  "v_dot2c_f32_bf16 v151, v117, v25\n" \
  "v_dot2c_f32_bf16 v152, v118, v24\n" \
  "v_dot2c_f32_bf16 v153, v118, v25\n" \
  "v_dot2c_f32_bf16 v154, v119, v24\n" \
  "v_dot2c_f32_bf16 v155, v119, v25\n" \
  "s_waitcnt vmcnt(47)\n" \
  "v_dot2c_f32_bf16 v148, v120, v24\n" \
  "v_dot2c_f32_bf16 v149, v120, v25\n" \
  "v_dot2c_f32_bf16 v150, v121, v24\n" \
  "v_dot2c_f32_bf16 v151, v121, v25\n" \
  "v_dot2c_f32_bf16 v152, v122, v24\n" \
  "v_dot2c_f32_bf16 v153, v122, v25\n" \
  "v_dot2c_f32_bf16 v154, v123, v24\n" \
  "v_dot2c_f32_bf16 v155, v123, v25\n" \
  "s_waitcnt vmcnt(46)\n" \
  "v_dot2c_f32_bf16 v148, v124, v24\n" \
  "v_dot2c_f32_bf16 v149, v124, v25\n" \
  "v_dot2c_f32_bf16 v150, v125, v24\n" \
  "v_dot2c_f32_bf16 v151, v125, v25\n" \
  "v_dot2c_f32_bf16 v152, v126, v24\n" \
  "v_dot2c_f32_bf16 v153, v126, v25\n" \
  "v_dot2c_f32_bf16 v154, v127, v24\n" \
  "v_dot2c_f32_bf16 v155, v127, v25\n" \
  "s_waitcnt vmcnt(45)\n" \
  "v_dot2c_f32_bf16 v148, v128, v24\n" \
  "v_dot2c_f32_bf16 v149, v128, v25\n" \
  "v_dot2c_f32_bf16 v150, v129, v24\n" \
  "v_dot2c_f32_bf16 v151, v129, v25\n" \
  "v_dot2c_f32_bf16 v152, v130, v24\n" \
  "v_dot2c_f32_bf16 v153, v130, v25\n" \
  "v_dot2c_f32_bf16 v154, v131, v24\n" \
  "v_dot2c_f32_bf16 v155, v131, v25\n" \
  "s_waitcnt vmcnt(44)\n" \
  "v_dot2c_f32_bf16 v148, v132, v24\n" \
  "v_dot2c_f32_bf16 v149, v132, v25\n" \
  "v_dot2c_f32_bf16 v150, v133, v24\n" \
  "v_dot2c_f32_bf16 v151, v133, v25\n" \
  "v_dot2c_f32_bf16 v152, v134, v24\n" \
  "v_dot2c_f32_bf16 v153, v134, v25\n" \
  "v_dot2c_f32_bf16 v154, v135, v24\n" \
  "v_dot2c_f32_bf16 v155, v135, v25\n" \
  "s_waitcnt vmcnt(43)\n" \
  "v_dot2c_f32_bf16 v148, v136, v24\n" \
  "v_dot2c_f32_bf16 v149, v136, v25\n" \
  "v_dot2c_f32_bf16 v150, v137, v24\n" \
  "v_dot2c_f32_bf16 v151, v137, v25\n" \
  "v_dot2c_f32_bf16 v152, v138, v24\n" \
  "v_dot2c_f32_bf16 v153, v138, v25\n" \
  "v_dot2c_f32_bf16 v154, v139, v24\n" \
  "v_dot2c_f32_bf16 v155, v139, v25\n" \
  "s_waitcnt vmcnt(42)\n" \
  "v_dot2c_f32_bf16 v148, v140, v24\n" \
  "v_dot2c_f32_bf16 v149, v140, v25\n" \
  "v_dot2c_f32_bf16 v150, v141, v24\n" \
  "v_dot2c_f32_bf16 v151, v141, v25\n" \
  "v_dot2c_f32_bf16 v152, v142, v24\n" \
  "v_dot2c_f32_bf16 v153, v142, v25\n" \
  "v_dot2c_f32_bf16 v154, v143, v24\n" \
  "v_dot2c_f32_bf16 v155, v143, v25\n" \
  "s_waitcnt vmcnt(41)\n" \
  "v_lshlrev_b32 v156, 16, v144\n" \
  "v_and_b32 v157, 0xffff0000, v144\n" \
  "v_lshlrev_b32 v158, 16, v145\n" \
  "v_and_b32 v159, 0xffff0000, v145\n" \
  "v_lshlrev_b32 v160, 16, v146\n" \
  "v_and_b32 v161, 0xffff0000, v146\n" \
  "v_lshlrev_b32 v162, 16, v147\n" \
  "v_and_b32 v163, 0xffff0000, v147\n" \
  "v_cvt_pk_bf16_f32 v164, v148, v149\n" \
  "v_cvt_pk_bf16_f32 v165, v150, v151\n" \
  "v_cvt_pk_bf16_f32 v166, v152, v153\n" \
  "v_cvt_pk_bf16_f32 v167, v154, v155\n" \
  "v_lshlrev_b32 v148, 16, v164\n" \
  "v_and_b32 v149, 0xffff0000, v164\n" \
  "v_lshlrev_b32 v150, 16, v165\n" \
  "v_and_b32 v151, 0xffff0000, v165\n" \
  "v_lshlrev_b32 v152, 16, v166\n" \
  "v_and_b32 v153, 0xffff0000, v166\n" \
  "v_lshlrev_b32 v154, 16, v167\n" \
  "v_and_b32 v155, 0xffff0000, v167\n" \
  "v_mul_f32 v148, v148, v16\n" \
  "v_mul_f32 v149, v149, v17\n" \
  "v_mul_f32 v150, v150, v18\n" \
  "v_mul_f32 v151, v151, v19\n" \
  "v_mul_f32 v152, v152, v20\n" \
  "v_mul_f32 v153, v153, v21\n" \
  "v_mul_f32 v154, v154, v22\n" \
  "v_mul_f32 v155, v155, v23\n" \
  "v_cvt_pk_bf16_f32 v164, v148, v149\n" \
  "v_cvt_pk_bf16_f32 v165, v150, v151\n" \
  "v_cvt_pk_bf16_f32 v166, v152, v153\n" \
  "v_cvt_pk_bf16_f32 v167, v154, v155\n" \
  "v_lshlrev_b32 v148, 16, v164\n" \
  "v_and_b32 v149, 0xffff0000, v164\n" \
  "v_lshlrev_b32 v150, 16, v165\n" \
  "v_and_b32 v151, 0xffff0000, v165\n" \
  "v_lshlrev_b32 v152, 16, v166\n" \
  "v_and_b32 v153, 0xffff0000, v166\n" \
  "v_lshlrev_b32 v154, 16, v167\n" \
  "v_and_b32 v155, 0xffff0000, v167\n" \
  "v_add_f32 v148, v148, v156\n" \
  "v_add_f32 v149, v149, v157\n" \
  "v_add_f32 v150, v150, v158\n" \
  "v_add_f32 v151, v151, v159\n" \
  "v_add_f32 v152, v152, v160\n" \
  "v_add_f32 v153, v153, v161\n" \
  "v_add_f32 v154, v154, v162\n" \
  "v_add_f32 v155, v155, v163\n" \
  "v_cvt_pk_bf16_f32 v164, v148, v149\n" \
  "v_cvt_pk_bf16_f32 v165, v150, v151\n" \
  "v_cvt_pk_bf16_f32 v166, v152, v153\n" \
  "v_cvt_pk_bf16_f32 v167, v154, v155\n" \
  "buffer_store_dwordx4 v[164:167], v3, s[56:59], s50 offen sc1\n" \
  "v_mov_b32 v254, s80\n" \
  "s_add_u32 s50, s50, 40960\n" \
  "s_add_u32 s80, s80, 1\n" \
  "s_mov_b64 exec, 1\n" \
  "buffer_store_dword v254, off, s[36:39], s52 sc1\n" \
  "s_mov_b64 exec, -1\n" \
  "NW1%=:\n" \
  "s_cmp_ge_u32 s80, 64\n" \
  "s_cbranch_scc1 DONE%=\n" \
  "v_cmp_gt_i32 vcc, s53, v255\n" \
  "s_cbranch_vccnz NBw1%=\n" \
  "s_add_u32 s85, s48, 0\n" \
  "buffer_load_dwordx4 v[28:31], v1, s[44:47], s85 offen offset:0 sc1\n" \
  "buffer_load_dwordx4 v[32:35], v1, s[44:47], s85 offen offset:1024 sc1\n" \
  "buffer_load_dwordx4 v[36:39], v1, s[44:47], s85 offen offset:2048 sc1\n" \
  "buffer_load_dwordx4 v[40:43], v1, s[44:47], s85 offen offset:3072 sc1\n" \
  "s_add_u32 s85, s48, 4096\n" \
  "buffer_load_dwordx4 v[44:47], v1, s[44:47], s85 offen offset:0 sc1\n" \
  "buffer_load_dwordx4 v[48:51], v1, s[44:47], s85 offen offset:1024 sc1\n" \
  "buffer_load_dwordx4 v[52:55], v1, s[44:47], s85 offen offset:2048 sc1\n" \
  "buffer_load_dwordx4 v[56:59], v1, s[44:47], s85 offen offset:3072 sc1\n" \
  "s_add_u32 s85, s48, 8192\n" \
  "buffer_load_dwordx4 v[60:63], v1, s[44:47], s85 offen offset:0 sc1\n" \
  "buffer_load_dwordx4 v[64:67], v1, s[44:47], s85 offen offset:1024 sc1\n" \
  "buffer_load_dwordx4 v[68:71], v1, s[44:47], s85 offen offset:2048 sc1\n" \
  "buffer_load_dwordx4 v[72:75], v1, s[44:47], s85 offen offset:3072 sc1\n" \
  "s_add_u32 s85, s48, 12288\n" \
  "buffer_load_dwordx4 v[76:79], v1, s[44:47], s85 offen offset:0 sc1\n" \
  "buffer_load_dwordx4 v[80:83], v1, s[44:47], s85 offen offset:1024 sc1\n" \
  "buffer_load_dwordx4 v[84:87], v2, s[44:47], s49 offen sc1\n" \
  "s_add_u32 s48, s48, 0x100000\n" \
  "s_bitset0_b32 s48, 24\n" \
  "s_add_u32 s49, s49, 40960\n" \
  "s_add_u32 s53, s53, 1\n" \
  "buffer_load_dword v255, v9, s[36:39], 0 offen sc1\n" \
  "s_waitcnt vmcnt(0)\n" \
  "s_branch NX1%=\n" \
  "NBw1%=:\n" \
  "s_cmp_eq_u32 s55, 0\n" \
  "s_cbranch_scc1 NCw1%=\n" \
  "s_sub_u32 s55, s55, 1\n" \
  "s_sleep 4\n" \
  "buffer_load_dword v255, v9, s[36:39], 0 offen sc1\n" \
  "s_waitcnt vmcnt(0)\n" \
  "v_cmp_gt_i32 vcc, s53, v255\n" \
  "s_cbranch_vccnz NBw1%=\n" \
  "NCw1%=:\n" \
  "s_add_u32 s85, s48, 0\n" \
  "buffer_load_dwordx4 v[28:31], v1, s[44:47], s85 offen offset:0 sc1\n" \
  "buffer_load_dwordx4 v[32:35], v1, s[44:47], s85 offen offset:1024 sc1\n" \
  "buffer_load_dwordx4 v[36:39], v1, s[44:47], s85 offen offset:2048 sc1\n" \
  "buffer_load_dwordx4 v[40:43], v1, s[44:47], s85 offen offset:3072 sc1\n" \
  "s_add_u32 s85, s48, 4096\n" \
  "buffer_load_dwordx4 v[44:47], v1, s[44:47], s85 offen offset:0 sc1\n" \
  "buffer_load_dwordx4 v[48:51], v1, s[44:47], s85 offen offset:1024 sc1\n" \
  "buffer_load_dwordx4 v[52:55], v1, s[44:47], s85 offen offset:2048 sc1\n" \
  "buffer_load_dwordx4 v[56:59], v1, s[44:47], s85 offen offset:3072 sc1\n" \
  "s_add_u32 s85, s48, 8192\n" \
  "buffer_load_dwordx4 v[60:63], v1, s[44:47], s85 offen offset:0 sc1\n" \
  "buffer_load_dwordx4 v[64:67], v1, s[44:47], s85 offen offset:1024 sc1\n" \
  "buffer_load_dwordx4 v[68:71], v1, s[44:47], s85 offen offset:2048 sc1\n" \
  "buffer_load_dwordx4 v[72:75], v1, s[44:47], s85 offen offset:3072 sc1\n" \
  "s_add_u32 s85, s48, 12288\n" \
  "buffer_load_dwordx4 v[76:79], v1, s[44:47], s85 offen offset:0 sc1\n" \
  "buffer_load_dwordx4 v[80:83], v1, s[44:47], s85 offen offset:1024 sc1\n" \
  "buffer_load_dwordx4 v[84:87], v2, s[44:47], s49 offen sc1\n" \
  "s_add_u32 s48, s48, 0x100000\n" \
  "s_bitset0_b32 s48, 24\n" \
  "s_add_u32 s49, s49, 40960\n" \
  "s_add_u32 s53, s53, 1\n" \
  "buffer_load_dword v255, v9, s[36:39], 0 offen sc1\n" \
  "s_branch SL0%=\n" \
  "NX1%=:\n" \
  "s_branch LOOP%=\n" \
  "DONE%=:\n" \
  "s_waitcnt vmcnt(0) lgkmcnt(0)\n" \
  "s_cmp_eq_u32 s73, -1\n" \
  "s_cbranch_scc1 END%=\n" \
  "s_memrealtime vcc\n" \
  "s_waitcnt lgkmcnt(0)\n" \
  "v_mov_b32 v32, vcc_lo\n" \
  "s_sub_u32 s22, vcc_lo, s22\n" \
  "s_memtime vcc\n" \
  "s_waitcnt lgkmcnt(0)\n" \
  "s_sub_u32 s20, vcc_lo, s20\n" \
  "v_mov_b32 v33, s20\n" \
  "v_mov_b32 v34, s22\n" \
  "v_mov_b32 v35, s24\n" \
  "s_mov_b64 exec, 1\n" \
  "buffer_store_dword v32, off, s[44:47], s73\n" \
  "buffer_store_dword v33, off, s[44:47], s73 offset:1024\n" \
  "buffer_store_dword v34, off, s[44:47], s73 offset:2048\n" \
  "buffer_store_dword v35, off, s[44:47], s73 offset:3072\n" \
  "s_waitcnt vmcnt(0)\n" \
  "END%=:\n" \
  "s_endpgm\n"
#define RING_SEGS 16
#define REC_C_ASM \
  "s_sub_u32 s9, s73, 2113536\n" \
  "s_lshl_b32 s9, s9, 2\n" \
  "s_add_u32 s9, s9, 11534336\n" \
  "s_mov_b64 s[2:3], 0\n" \
  "s_mov_b64 s[4:5], 0\n" \
  "s_mov_b64 s[6:7], 0\n" \
  "s_mov_b32 s80, 0\n" \
  "s_mov_b32 s55, 0x10000\n" \
  "s_mov_b32 s81, 0\n" \
  "s_mov_b32 s82, 65536\n" \
  "s_mov_b32 s83, 196608\n" \
  "s_mov_b32 s89, 1\n" \
  "s_mov_b32 s90, 0x2000\n" \
  "s_mov_b32 s91, 0x3e0293ee\n" \
  "v_mov_b64 v[28:29], -1\n" \
  "v_mov_b64 v[30:31], -1\n" \
  "s_add_u32 s87, s82, s70\n" \
  "s_add_u32 s8, s82, s66\n" \
  "s_mov_b32 s78, 0\n" \
  "v_mov_b64 v[32:33], -1\n" \
  "v_mov_b64 v[34:35], -1\n" \
  "LOOP%=:\n" \
  "s_nop 0\n" \
  "v_readfirstlane_b32 s85, v34\n" \
  "s_cmp_ge_i32 s85, s53\n" \
  "s_cbranch_scc1 NCu%=\n" \
  "NBu%=:\n" \
  "s_cmp_eq_u32 s55, 0\n" \
  "s_cbranch_scc1 NCu%=\n" \
  "s_sub_u32 s55, s55, 1\n" \
  "s_sleep 2\n" \
  "buffer_load_dword v34, v9, s[36:39], 0 offen sc1\n" \
  "s_waitcnt vmcnt(0)\n" \
  "v_cmp_gt_i32 vcc, s53, v34\n" \
  "s_cbranch_vccnz NBu%=\n" \
  "NCu%=:\n" \
  "s_add_u32 s53, s53, 4\n" \
  "s_add_u32 s98, s33, 4096\n" \
  "s_mov_b64 exec, 1\n" \
  "buffer_load_dword v255, off, s[40:43], s8 offset:256 sc1\n" \
  "s_mov_b64 exec, -1\n" \
  "buffer_load_dwordx4 v[36:39], v0, s[44:47], s33 offen offset:0 sc1\n" \
  "s_sleep 2\n" \
  "buffer_load_dwordx4 v[40:43], v0, s[44:47], s33 offen offset:1024 sc1\n" \
  "s_sleep 2\n" \
  "buffer_load_dwordx4 v[44:47], v0, s[44:47], s33 offen offset:2048 sc1\n" \
  "s_sleep 2\n" \
  "buffer_load_dwordx4 v[48:51], v0, s[44:47], s33 offen offset:3072 sc1\n" \
  "s_sleep 2\n" \
  "buffer_load_dwordx4 v[52:55], v0, s[44:47], s98 offen offset:0 sc1\n" \
  "s_sleep 2\n" \
  "buffer_load_dwordx4 v[56:59], v0, s[44:47], s98 offen offset:1024 sc1\n" \
  "s_sleep 2\n" \
  "buffer_load_dwordx4 v[60:63], v0, s[44:47], s98 offen offset:2048 sc1\n" \
  "s_sleep 2\n" \
  "buffer_load_dwordx4 v[64:67], v0, s[44:47], s98 offen offset:3072 sc1\n" \
  "s_sleep 2\n" \
  "s_xor_b32 s85, s81, 786432\n" \
  "s_cmp_eq_u32 s80, 0\n" \
  "s_cselect_b32 s85, 0x80000000, s85\n" \
  "s_cselect_b32 s86, 0x80000000, s48\n" \
  "buffer_store_dwordx4 v[20:23], v0, s[44:47], s86 offen sc1\n" \
  "buffer_store_dwordx4 v[24:27], v0, s[44:47], s86 offen offset:1024 sc1\n" \
  "s_add_u32 s48, s48, 8192\n" \
  "s_bitset0_b32 s48, 17\n" \
  "buffer_store_dwordx4 v[28:31], v2, s[40:43], s85 offen sc1\n" \
  "buffer_store_dwordx4 v[28:31], v3, s[40:43], s85 offen sc1\n" \
  "s_cmp_lg_u32 s64, 3\n" \
  "s_cbranch_scc1 N1%=\n" \
  "s_waitcnt vmcnt(12)\n" \
  "v_readfirstlane_b32 s86, v255\n" \
  "v_readfirstlane_b32 s88, v32\n" \
  "s_cmp_lg_u32 s88, -1\n" \
  "s_cbranch_scc1 PV%=\n" \
  "s_add_u32 s89, s89, 0x1000000\n" \
  "s_movk_i32 s85, 0x4000\n" \
  "PT1%=:\n" \
  "s_mov_b64 exec, 1\n" \
  "buffer_load_dwordx4 v[32:35], off, s[40:43], s87 sc1\n" \
  "s_mov_b64 exec, -1\n" \
  "s_waitcnt vmcnt(0)\n" \
  "v_readfirstlane_b32 s88, v32\n" \
  "s_sub_u32 s85, s85, 1\n" \
  "s_cbranch_scc1 PU1%=\n" \
  "s_cmp_eq_u32 s88, -1\n" \
  "s_cbranch_scc1 PT1%=\n" \
  "PU1%=:\n" \
  "PV%=:\n" \
  "v_readfirstlane_b32 s96, v33\n" \
  "v_readfirstlane_b32 s1, v35\n" \
  "s_and_b32 s96, s96, 0x3fff\n" \
  "s_and_b32 s1, s1, s78\n" \
  "s_and_b32 s85, s1, 0x3ff\n" \
  "s_add_u32 s88, s88, s85\n" \
  "s_lshr_b32 s85, s1, 10\n" \
  "s_cmp_eq_u32 s86, -1\n" \
  "s_cselect_b32 s85, 0, s85\n" \
  "s_add_u32 s88, s88, s85\n" \
  "s_movk_i32 s86, 0x400\n" \
  "NWc%=:\n" \
  "s_memrealtime vcc\n" \
  "s_waitcnt lgkmcnt(0)\n" \
  "s_sub_u32 s85, s88, vcc_lo\n" \
  "s_lshl_b32 s85, s85, 1\n" \
  "s_cmp_lt_i32 s85, 80\n" \
  "s_cbranch_scc1 NXc%=\n" \
  "s_sub_u32 s86, s86, 1\n" \
  "s_cbranch_scc0 NWc%=\n" \
  "NXc%=:\n" \
  "s_waitcnt lgkmcnt(0)\n" \
  "s_memtime s[10:11]\n" \
  "s_memrealtime s[12:13]\n" \
  "s_nop 1\n" \
  "s_memrealtime s[14:15]\n" \
  "s_nop 1\n" \
  "s_memrealtime s[16:17]\n" \
  "s_nop 1\n" \
  "s_memrealtime s[18:19]\n" \
  "s_nop 1\n" \
  "s_memrealtime s[20:21]\n" \
  "s_nop 1\n" \
  "s_memrealtime s[22:23]\n" \
  "s_nop 1\n" \
  "s_memrealtime s[24:25]\n" \
  "s_nop 1\n" \
  "s_memrealtime s[26:27]\n" \
  "s_nop 1\n" \
  "s_memrealtime s[28:29]\n" \
  "s_nop 1\n" \
  "s_memrealtime s[30:31]\n" \
  "s_nop 1\n" \
  "s_mov_b32 s85, 0\n" \
  "s_waitcnt lgkmcnt(0)\n" \
  "s_cmp_eq_u32 s14, s12\n" \
  "s_addc_u32 s85, s85, 0\n" \
  "s_cmp_eq_u32 s16, s12\n" \
  "s_addc_u32 s85, s85, 0\n" \
  "s_cmp_eq_u32 s18, s12\n" \
  "s_addc_u32 s85, s85, 0\n" \
  "s_cmp_eq_u32 s20, s12\n" \
  "s_addc_u32 s85, s85, 0\n" \
  "s_cmp_eq_u32 s22, s12\n" \
  "s_addc_u32 s85, s85, 0\n" \
  "s_cmp_eq_u32 s24, s12\n" \
  "s_addc_u32 s85, s85, 0\n" \
  "s_cmp_eq_u32 s26, s12\n" \
  "s_addc_u32 s85, s85, 0\n" \
  "s_cmp_eq_u32 s28, s12\n" \
  "s_addc_u32 s85, s85, 0\n" \
  "s_cmp_eq_u32 s30, s12\n" \
  "s_addc_u32 s85, s85, 0\n" \
  "s_mul_i32 s85, s85, 12\n" \
  "s_add_u32 s10, s10, s85\n" \
  "s_sub_u32 s85, s88, s12\n" \
  "s_lshl_b32 s85, s85, 1\n" \
  "s_ashr_i32 s85, s85, 1\n" \
  "s_sub_i32 s85, s85, 4\n" \
  "s_max_i32 s85, s85, -1000\n" \
  "s_min_i32 s85, s85, 1000\n" \
  "s_mul_i32 s85, s85, s96\n" \
  "s_ashr_i32 s85, s85, 8\n" \
  "s_add_u32 s97, s10, s85\n" \
  "s_sub_u32 s86, s97, 168\n" \
  "FWu%=:\n" \
  "s_memtime vcc\n" \
  "s_waitcnt lgkmcnt(0)\n" \
  "s_sub_u32 s85, s86, vcc_lo\n" \
  "s_cmp_gt_i32 s85, 360\n" \
  "s_cbranch_scc1 FWu%=\n" \
  "s_sub_i32 s85, s85, 108\n" \
  "s_max_i32 s85, s85, 0\n" \
  "s_and_b32 s85, s85, 0xfc\n" \
  "s_getpc_b64 vcc\n" \
  "FPu%=:\n" \
  "s_add_u32 vcc_lo, vcc_lo, FEu%=-FPu%=\n" \
  "s_addc_u32 vcc_hi, vcc_hi, 0\n" \
  "s_sub_u32 vcc_lo, vcc_lo, s85\n" \
  "s_subb_u32 vcc_hi, vcc_hi, 0\n" \
  "s_setpc_b64 vcc\n" \
  "s_nop 0\n" \
  "s_nop 0\n" \
  "s_nop 0\n" \
  "s_nop 0\n" \
  "s_nop 0\n" \
  "s_nop 0\n" \
  "s_nop 0\n" \
  "s_nop 0\n" \
  "s_nop 0\n" \
  "s_nop 0\n" \
  "s_nop 0\n" \
  "s_nop 0\n" \
  "s_nop 0\n" \
  "s_nop 0\n" \
  "s_nop 0\n" \
  "s_nop 0\n" \
  "s_nop 0\n" \
  "s_nop 0\n" \
  "s_nop 0\n" \
  "s_nop 0\n" \
  "s_nop 0\n" \
  "s_nop 0\n" \
  "s_nop 0\n" \
  "s_nop 0\n" \
  "s_nop 0\n" \
  "s_nop 0\n" \
  "s_nop 0\n" \
  "s_nop 0\n" \
  "s_nop 0\n" \
  "s_nop 0\n" \
  "s_nop 0\n" \
  "s_nop 0\n" \
  "s_nop 0\n" \
  "s_nop 0\n" \
  "s_nop 0\n" \
  "s_nop 0\n" \
  "s_nop 0\n" \
  "s_nop 0\n" \
  "s_nop 0\n" \
  "s_nop 0\n" \
  "s_nop 0\n" \
  "s_nop 0\n" \
  "s_nop 0\n" \
  "s_nop 0\n" \
  "s_nop 0\n" \
  "s_nop 0\n" \
  "s_nop 0\n" \
  "s_nop 0\n" \
  "s_nop 0\n" \
  "s_nop 0\n" \
  "s_nop 0\n" \
  "s_nop 0\n" \
  "s_nop 0\n" \
  "s_nop 0\n" \
  "s_nop 0\n" \
  "s_nop 0\n" \
  "s_nop 0\n" \
  "s_nop 0\n" \
  "s_nop 0\n" \
  "s_nop 0\n" \
  "s_nop 0\n" \
  "s_nop 0\n" \
  "s_nop 0\n" \
  "s_nop 0\n" \
  "FEu%=:\n" \
  "N1%=:\n" \
  "s_barrier\n" \
  "buffer_load_dwordx4 v[20:23], v2, s[40:43], s81 offen sc1\n" \
  "buffer_load_dwordx4 v[24:27], v3, s[40:43], s81 offen sc1\n" \
  "s_add_u32 s87, s82, s70\n" \
  "s_mov_b64 exec, 1\n" \
  "buffer_load_dwordx4 v[32:35], off, s[40:43], s87 sc1\n" \
  "s_mov_b64 exec, -1\n" \
  "s_nop 15\n" \
  "s_nop 15\n" \
  "buffer_load_dwordx4 v[68:71], v5, s[40:43], s82 offen sc1\n" \
  "buffer_load_dwordx4 v[72:75], v4, s[40:43], s82 offen offset:0 sc1\n" \
  "buffer_load_dwordx4 v[76:79], v4, s[40:43], s82 offen offset:1024 sc1\n" \
  "buffer_load_dwordx4 v[80:83], v4, s[40:43], s82 offen offset:2048 sc1\n" \
  "buffer_load_dwordx4 v[84:87], v4, s[40:43], s82 offen offset:3072 sc1\n" \
  "s_waitcnt vmcnt(4)\n" \
  "v_mfma_f32_16x16x32_bf16 v[88:91], v[20:23], v[68:71], 0\n" \
  "v_mfma_f32_16x16x32_bf16 v[92:95], v[24:27], v[68:71], 0\n" \
  "s_waitcnt vmcnt(3)\n" \
  "v_mfma_f32_16x16x32_bf16 v[92:95], v[40:43], v[72:75], v[92:95]\n" \
  "v_mfma_f32_16x16x32_bf16 v[88:91], v[36:39], v[72:75], v[88:91]\n" \
  "s_waitcnt vmcnt(2)\n" \
  "v_mfma_f32_16x16x32_bf16 v[92:95], v[48:51], v[76:79], v[92:95]\n" \
  "v_mfma_f32_16x16x32_bf16 v[88:91], v[44:47], v[76:79], v[88:91]\n" \
  "s_waitcnt vmcnt(1)\n" \
  "v_mfma_f32_16x16x32_bf16 v[92:95], v[56:59], v[80:83], v[92:95]\n" \
  "v_mfma_f32_16x16x32_bf16 v[88:91], v[52:55], v[80:83], v[88:91]\n" \
  "s_waitcnt vmcnt(0)\n" \
  "v_mfma_f32_16x16x32_bf16 v[92:95], v[64:67], v[84:87], v[92:95]\n" \
  "v_mfma_f32_16x16x32_bf16 v[88:91], v[60:63], v[84:87], v[88:91]\n" \
  "s_nop 7\n" \
  "v_add_f32_dpp v88, v92, v88 row_ror:8 row_mask:0xf bank_mask:0xf\n" \
  "v_add_f32_dpp v89, v93, v89 row_ror:8 row_mask:0xf bank_mask:0xf\n" \
  "v_add_f32_dpp v90, v94, v90 row_ror:8 row_mask:0xf bank_mask:0xf\n" \
  "v_add_f32_dpp v91, v95, v91 row_ror:8 row_mask:0xf bank_mask:0xf\n" \
  "ds_write_b128 v1, v[88:91]\n" \
  "v_add_f32 v255, v88, v89\n" \
  "v_add_f32 v254, v90, v91\n" \
  "v_cmp_u_f32 vcc, v255, v254\n" \
  "s_cbranch_vccnz RA%=\n" \
  "RAB%=:\n" \
  "s_cmp_lg_u32 s64, 0\n" \
  "s_waitcnt lgkmcnt(0)\n" \
  "s_barrier\n" \
  "s_cbranch_scc1 NQ%=\n" \
  "ds_read_b64 v[36:37], v7 offset:0\n" \
  "ds_read_b64 v[38:39], v7 offset:1024\n" \
  "ds_read_b64 v[40:41], v7 offset:2048\n" \
  "ds_read_b64 v[42:43], v7 offset:3072\n" \
  "s_waitcnt lgkmcnt(2)\n" \
  "v_add_f32 v36, v36, v38\n" \
  "v_add_f32 v37, v37, v39\n" \
  "s_waitcnt lgkmcnt(0)\n" \
  "v_add_f32 v40, v40, v42\n" \
  "v_add_f32 v41, v41, v43\n" \
  "v_add_f32 v36, v36, v40\n" \
  "v_add_f32 v37, v37, v41\n" \
  "v_cvt_pk_bf16_f32 v255, v36, v37\n" \
  "buffer_store_dword v255, v6, s[40:43], s82 offen sc1\n" \
  "buffer_store_dword v28, v6, s[40:43], s83 offen sc1\n" \
  "s_branch NP%=\n" \
  "NQ%=:\n" \
  "s_sleep 3\n" \
  "NP%=:\n" \
  "s_sleep 0\n" \
  "s_waitcnt lgkmcnt(0)\n" \
  "s_add_u32 s33, s33, 32768\n" \
  "s_mov_b32 s78, 0xfffff\n" \
  "s_add_u32 s8, s82, s66\n" \
  "s_bitset0_b32 s33, 19\n" \
  "s_add_u32 s80, s80, 1\n" \
  "s_xor_b32 s81, s81, 786432\n" \
  "s_add_u32 s82, s82, 65536\n" \
  "s_and_b32 s82, s82, 262143\n" \
  "s_xor_b32 s83, s82, 131072\n" \
  "s_cmp_lg_u32 s80, 63\n" \
  "s_cbranch_scc1 LOOP%=\n" \
  "buffer_store_dwordx4 v[20:23], v0, s[44:47], s48 offen sc1\n" \
  "buffer_store_dwordx4 v[24:27], v0, s[44:47], s48 offen offset:1024 sc1\n" \
  "s_waitcnt vmcnt(0)\n" \
  "s_mov_b64 exec, 1\n" \
  "buffer_atomic_add v10, off, s[36:39], s52\n" \
  "s_mov_b64 exec, -1\n" \
  "s_branch DONE%=\n" \
  "RA%=:\n" \
  "v_max_u32 v255, v20, v21\n" \
  "v_max3_u32 v255, v22, v23, v255\n" \
  "v_max3_u32 v255, v24, v25, v255\n" \
  "v_max3_u32 v255, v26, v27, v255\n" \
  "v_cmp_eq_u32 vcc, -1, v255\n" \
  "s_cbranch_vccz RA0%=\n" \
  "s_add_u32 s89, s89, 0x10000\n" \
  "s_bitset1_b64 s[4:5], s80\n" \
  "RA0%=:\n" \
  "v_max_u32 v255, v20, v21\n" \
  "v_max3_u32 v255, v22, v23, v255\n" \
  "v_max3_u32 v255, v24, v25, v255\n" \
  "v_max3_u32 v255, v26, v27, v255\n" \
  "v_max3_u32 v255, v72, v73, v255\n" \
  "v_max3_u32 v255, v74, v75, v255\n" \
  "v_max3_u32 v255, v76, v77, v255\n" \
  "v_max3_u32 v255, v78, v79, v255\n" \
  "v_max3_u32 v255, v80, v81, v255\n" \
  "v_max3_u32 v255, v82, v83, v255\n" \
  "v_max3_u32 v255, v84, v85, v255\n" \
  "v_max3_u32 v255, v86, v87, v255\n" \
  "v_max3_u32 v255, v68, v69, v255\n" \
  "v_max3_u32 v255, v70, v71, v255\n" \
  "v_cmp_eq_u32 vcc, -1, v255\n" \
  "s_cbranch_vccz RAB%=\n" \
  "s_add_u32 s89, s89, 0x100\n" \
  "s_bitset1_b64 s[2:3], s80\n" \
  "RA1%=:\n" \
  "s_cmp_eq_u32 s90, 0\n" \
  "s_cbranch_scc1 RA2%=\n" \
  "s_sub_u32 s90, s90, 1\n" \
  "buffer_load_dwordx4 v[20:23], v2, s[40:43], s81 offen sc1\n" \
  "buffer_load_dwordx4 v[24:27], v3, s[40:43], s81 offen sc1\n" \
  "buffer_load_dwordx4 v[68:71], v5, s[40:43], s82 offen sc1\n" \
  "buffer_load_dwordx4 v[72:75], v4, s[40:43], s82 offen offset:0 sc1\n" \
  "buffer_load_dwordx4 v[76:79], v4, s[40:43], s82 offen offset:1024 sc1\n" \
  "buffer_load_dwordx4 v[80:83], v4, s[40:43], s82 offen offset:2048 sc1\n" \
  "buffer_load_dwordx4 v[84:87], v4, s[40:43], s82 offen offset:3072 sc1\n" \
  "s_waitcnt vmcnt(6)\n" \
  "v_max_u32 v255, v20, v21\n" \
  "v_max3_u32 v255, v22, v23, v255\n" \
  "s_waitcnt vmcnt(5)\n" \
  "v_max3_u32 v255, v24, v25, v255\n" \
  "v_max3_u32 v255, v26, v27, v255\n" \
  "s_waitcnt vmcnt(3)\n" \
  "v_max3_u32 v255, v72, v73, v255\n" \
  "v_max3_u32 v255, v74, v75, v255\n" \
  "s_waitcnt vmcnt(2)\n" \
  "v_max3_u32 v255, v76, v77, v255\n" \
  "v_max3_u32 v255, v78, v79, v255\n" \
  "s_waitcnt vmcnt(1)\n" \
  "v_max3_u32 v255, v80, v81, v255\n" \
  "v_max3_u32 v255, v82, v83, v255\n" \
  "s_waitcnt vmcnt(0)\n" \
  "v_max3_u32 v255, v84, v85, v255\n" \
  "v_max3_u32 v255, v86, v87, v255\n" \
  "v_max3_u32 v255, v68, v69, v255\n" \
  "v_max3_u32 v255, v70, v71, v255\n" \
  "v_cmp_eq_u32 vcc, -1, v255\n" \
  "s_cbranch_vccnz RA1%=\n" \
  "RA2%=:\n" \
  "v_mfma_f32_16x16x32_bf16 v[88:91], v[20:23], v[68:71], 0\n" \
  "v_mfma_f32_16x16x32_bf16 v[92:95], v[24:27], v[68:71], 0\n" \
  "v_mfma_f32_16x16x32_bf16 v[92:95], v[40:43], v[72:75], v[92:95]\n" \
  "v_mfma_f32_16x16x32_bf16 v[88:91], v[36:39], v[72:75], v[88:91]\n" \
  "v_mfma_f32_16x16x32_bf16 v[92:95], v[48:51], v[76:79], v[92:95]\n" \
  "v_mfma_f32_16x16x32_bf16 v[88:91], v[44:47], v[76:79], v[88:91]\n" \
  "v_mfma_f32_16x16x32_bf16 v[92:95], v[56:59], v[80:83], v[92:95]\n" \
  "v_mfma_f32_16x16x32_bf16 v[88:91], v[52:55], v[80:83], v[88:91]\n" \
  "v_mfma_f32_16x16x32_bf16 v[92:95], v[64:67], v[84:87], v[92:95]\n" \
  "v_mfma_f32_16x16x32_bf16 v[88:91], v[60:63], v[84:87], v[88:91]\n" \
  "s_nop 7\n" \
  "v_add_f32_dpp v88, v92, v88 row_ror:8 row_mask:0xf bank_mask:0xf\n" \
  "v_add_f32_dpp v89, v93, v89 row_ror:8 row_mask:0xf bank_mask:0xf\n" \
  "v_add_f32_dpp v90, v94, v90 row_ror:8 row_mask:0xf bank_mask:0xf\n" \
  "v_add_f32_dpp v91, v95, v91 row_ror:8 row_mask:0xf bank_mask:0xf\n" \
  "ds_write_b128 v1, v[88:91]\n" \
  "s_branch RAB%=\n" \
  "DONE%=:\n" \
  "s_cmp_eq_u32 s73, -1\n" \
  "s_cbranch_scc1 END%=\n" \
  "s_mov_b64 exec, 1\n" \
  "v_mov_b32 v32, s89\n" \
  "buffer_store_dword v32, off, s[44:47], s73\n" \
  "v_mov_b32 v33, s2\n" \
  "buffer_store_dword v33, off, s[44:47], s73 offset:40\n" \
  "v_mov_b32 v33, s3\n" \
  "buffer_store_dword v33, off, s[44:47], s73 offset:44\n" \
  "v_mov_b32 v33, s4\n" \
  "buffer_store_dword v33, off, s[44:47], s73 offset:48\n" \
  "v_mov_b32 v33, s5\n" \
  "buffer_store_dword v33, off, s[44:47], s73 offset:52\n" \
  "v_mov_b32 v33, s6\n" \
  "buffer_store_dword v33, off, s[44:47], s73 offset:56\n" \
  "v_mov_b32 v33, s7\n" \
  "buffer_store_dword v33, off, s[44:47], s73 offset:60\n" \
  "s_mov_b64 exec, -1\n" \
  "END%=:\n" \
  "s_waitcnt vmcnt(0) lgkmcnt(0)\n" \
  "s_endpgm\n"
#define REC_S_ASM \
  "s_mov_b32 m0, s72\n" \
  "s_nop 0\n" \
  "global_load_lds_dwordx4 v[24:25], off\n" \
  "s_add_u32 m0, m0, 1024\n" \
  "s_nop 0\n" \
  "global_load_lds_dwordx4 v[26:27], off\n" \
  "s_add_u32 m0, m0, 1024\n" \
  "s_mov_b32 exec_hi, 0\n" \
  "global_load_lds_dwordx4 v[28:29], off\n" \
  "s_mov_b32 exec_hi, -1\n" \
  "s_lshl_b32 s86, s64, 12\n" \
  "s_add_u32 s86, s86, 32768\n" \
  "s_add_u32 m0, s86, 0\n" \
  "s_mov_b32 s85, 0\n" \
  "buffer_load_dwordx4 v16, s[56:59], s85 offen nt lds\n" \
  "s_add_u32 m0, s86, 1024\n" \
  "s_mov_b32 s85, 16384\n" \
  "buffer_load_dwordx4 v16, s[56:59], s85 offen nt lds\n" \
  "s_add_u32 m0, s86, 2048\n" \
  "s_mov_b32 s85, 32768\n" \
  "buffer_load_dwordx4 v17, s[56:59], s85 offen nt lds\n" \
  "s_add_u32 m0, s86, 3072\n" \
  "s_mov_b32 s85, 49152\n" \
  "buffer_load_dwordx4 v17, s[56:59], s85 offen nt lds\n" \
  "s_add_u32 m0, s86, 16384\n" \
  "s_mov_b32 s85, 0\n" \
  "buffer_load_dwordx4 v16, s[60:63], s85 offen nt lds\n" \
  "s_add_u32 m0, s86, 17408\n" \
  "s_mov_b32 s85, 16384\n" \
  "buffer_load_dwordx4 v16, s[60:63], s85 offen nt lds\n" \
  "s_add_u32 m0, s86, 18432\n" \
  "s_mov_b32 s85, 32768\n" \
  "buffer_load_dwordx4 v17, s[60:63], s85 offen nt lds\n" \
  "s_add_u32 m0, s86, 19456\n" \
  "s_mov_b32 s85, 49152\n" \
  "buffer_load_dwordx4 v17, s[60:63], s85 offen nt lds\n" \
  "s_add_u32 m0, s86, 32768\n" \
  "s_mov_b32 s85, 524288\n" \
  "buffer_load_dwordx4 v16, s[56:59], s85 offen nt lds\n" \
  "s_add_u32 m0, s86, 33792\n" \
  "s_mov_b32 s85, 540672\n" \
  "buffer_load_dwordx4 v16, s[56:59], s85 offen nt lds\n" \
  "s_add_u32 m0, s86, 34816\n" \
  "s_mov_b32 s85, 557056\n" \
  "buffer_load_dwordx4 v17, s[56:59], s85 offen nt lds\n" \
  "s_add_u32 m0, s86, 35840\n" \
  "s_mov_b32 s85, 573440\n" \
  "buffer_load_dwordx4 v17, s[56:59], s85 offen nt lds\n" \
  "s_add_u32 m0, s86, 49152\n" \
  "s_mov_b32 s85, 524288\n" \
  "buffer_load_dwordx4 v16, s[60:63], s85 offen nt lds\n" \
  "s_add_u32 m0, s86, 50176\n" \
  "s_mov_b32 s85, 540672\n" \
  "buffer_load_dwordx4 v16, s[60:63], s85 offen nt lds\n" \
  "s_add_u32 m0, s86, 51200\n" \
  "s_mov_b32 s85, 557056\n" \
  "buffer_load_dwordx4 v17, s[60:63], s85 offen nt lds\n" \
  "s_add_u32 m0, s86, 52224\n" \
  "s_mov_b32 s85, 573440\n" \
  "buffer_load_dwordx4 v17, s[60:63], s85 offen nt lds\n" \
  "s_add_u32 m0, s86, 65536\n" \
  "s_mov_b32 s85, 1048576\n" \
  "buffer_load_dwordx4 v16, s[56:59], s85 offen nt lds\n" \
  "s_add_u32 m0, s86, 66560\n" \
  "s_mov_b32 s85, 1064960\n" \
  "buffer_load_dwordx4 v16, s[56:59], s85 offen nt lds\n" \
  "s_add_u32 m0, s86, 67584\n" \
  "s_mov_b32 s85, 1081344\n" \
  "buffer_load_dwordx4 v17, s[56:59], s85 offen nt lds\n" \
  "s_add_u32 m0, s86, 68608\n" \
  "s_mov_b32 s85, 1097728\n" \
  "buffer_load_dwordx4 v17, s[56:59], s85 offen nt lds\n" \
  "s_add_u32 m0, s86, 81920\n" \
  "s_mov_b32 s85, 1048576\n" \
  "buffer_load_dwordx4 v16, s[60:63], s85 offen nt lds\n" \
  "s_add_u32 m0, s86, 82944\n" \
  "s_mov_b32 s85, 1064960\n" \
  "buffer_load_dwordx4 v16, s[60:63], s85 offen nt lds\n" \
  "s_add_u32 m0, s86, 83968\n" \
  "s_mov_b32 s85, 1081344\n" \
  "buffer_load_dwordx4 v17, s[60:63], s85 offen nt lds\n" \
  "s_add_u32 m0, s86, 84992\n" \
  "s_mov_b32 s85, 1097728\n" \
  "buffer_load_dwordx4 v17, s[60:63], s85 offen nt lds\n" \
  "s_waitcnt vmcnt(16) lgkmcnt(0)\n" \
  "s_barrier\n" \
  "s_add_u32 m0, s86, 98304\n" \
  "s_mov_b32 s85, 1572864\n" \
  "buffer_load_dwordx4 v16, s[56:59], s85 offen nt lds\n" \
  "s_add_u32 m0, s86, 99328\n" \
  "s_mov_b32 s85, 1589248\n" \
  "buffer_load_dwordx4 v16, s[56:59], s85 offen nt lds\n" \
  "s_add_u32 m0, s86, 100352\n" \
  "s_mov_b32 s85, 1605632\n" \
  "buffer_load_dwordx4 v17, s[56:59], s85 offen nt lds\n" \
  "s_add_u32 m0, s86, 101376\n" \
  "s_mov_b32 s85, 1622016\n" \
  "buffer_load_dwordx4 v17, s[56:59], s85 offen nt lds\n" \
  "s_add_u32 m0, s86, 114688\n" \
  "s_mov_b32 s85, 1572864\n" \
  "buffer_load_dwordx4 v16, s[60:63], s85 offen nt lds\n" \
  "s_add_u32 m0, s86, 115712\n" \
  "s_mov_b32 s85, 1589248\n" \
  "buffer_load_dwordx4 v16, s[60:63], s85 offen nt lds\n" \
  "s_add_u32 m0, s86, 116736\n" \
  "s_mov_b32 s85, 1605632\n" \
  "buffer_load_dwordx4 v17, s[60:63], s85 offen nt lds\n" \
  "s_add_u32 m0, s86, 117760\n" \
  "s_mov_b32 s85, 1622016\n" \
  "buffer_load_dwordx4 v17, s[60:63], s85 offen nt lds\n" \
  "ds_read_b64_tr_b16 a[0:1], v18 offset:0\n" \
  "ds_read_b64_tr_b16 a[2:3], v18 offset:1024\n" \
  "ds_read_b64_tr_b16 a[4:5], v18 offset:8192\n" \
  "ds_read_b64_tr_b16 a[6:7], v18 offset:9216\n" \
  "ds_read_b64_tr_b16 a[80:81], v19 offset:0\n" \
  "ds_read_b64_tr_b16 a[82:83], v19 offset:1024\n" \
  "ds_read_b64_tr_b16 a[84:85], v19 offset:8192\n" \
  "ds_read_b64_tr_b16 a[86:87], v19 offset:9216\n" \
  "ds_read_b64_tr_b16 a[160:161], v18 offset:16384\n" \
  "ds_read_b64_tr_b16 a[162:163], v18 offset:17408\n" \
  "ds_read_b64_tr_b16 a[164:165], v18 offset:24576\n" \
  "ds_read_b64_tr_b16 a[166:167], v18 offset:25600\n" \
  "ds_read_b64_tr_b16 a[240:241], v19 offset:16384\n" \
  "ds_read_b64_tr_b16 a[242:243], v19 offset:17408\n" \
  "ds_read_b64_tr_b16 a[244:245], v19 offset:24576\n" \
  "ds_read_b64_tr_b16 a[246:247], v19 offset:25600\n" \
  "s_waitcnt vmcnt(16) lgkmcnt(0)\n" \
  "s_barrier\n" \
  "s_add_u32 m0, s86, 0\n" \
  "s_mov_b32 s85, 2097152\n" \
  "buffer_load_dwordx4 v16, s[56:59], s85 offen nt lds\n" \
  "s_add_u32 m0, s86, 1024\n" \
  "s_mov_b32 s85, 2113536\n" \
  "buffer_load_dwordx4 v16, s[56:59], s85 offen nt lds\n" \
  "s_add_u32 m0, s86, 2048\n" \
  "s_mov_b32 s85, 2129920\n" \
  "buffer_load_dwordx4 v17, s[56:59], s85 offen nt lds\n" \
  "s_add_u32 m0, s86, 3072\n" \
  "s_mov_b32 s85, 2146304\n" \
  "buffer_load_dwordx4 v17, s[56:59], s85 offen nt lds\n" \
  "s_add_u32 m0, s86, 16384\n" \
  "s_mov_b32 s85, 2097152\n" \
  "buffer_load_dwordx4 v16, s[60:63], s85 offen nt lds\n" \
  "s_add_u32 m0, s86, 17408\n" \
  "s_mov_b32 s85, 2113536\n" \
  "buffer_load_dwordx4 v16, s[60:63], s85 offen nt lds\n" \
  "s_add_u32 m0, s86, 18432\n" \
  "s_mov_b32 s85, 2129920\n" \
  "buffer_load_dwordx4 v17, s[60:63], s85 offen nt lds\n" \
  "s_add_u32 m0, s86, 19456\n" \
  "s_mov_b32 s85, 2146304\n" \
  "buffer_load_dwordx4 v17, s[60:63], s85 offen nt lds\n" \
  "ds_read_b64_tr_b16 a[8:9], v18 offset:32768\n" \
  "ds_read_b64_tr_b16 a[10:11], v18 offset:33792\n" \
  "ds_read_b64_tr_b16 a[12:13], v18 offset:40960\n" \
  "ds_read_b64_tr_b16 a[14:15], v18 offset:41984\n" \
  "ds_read_b64_tr_b16 a[88:89], v19 offset:32768\n" \
  "ds_read_b64_tr_b16 a[90:91], v19 offset:33792\n" \
  "ds_read_b64_tr_b16 a[92:93], v19 offset:40960\n" \
  "ds_read_b64_tr_b16 a[94:95], v19 offset:41984\n" \
  "ds_read_b64_tr_b16 a[168:169], v18 offset:49152\n" \
  "ds_read_b64_tr_b16 a[170:171], v18 offset:50176\n" \
  "ds_read_b64_tr_b16 a[172:173], v18 offset:57344\n" \
  "ds_read_b64_tr_b16 a[174:175], v18 offset:58368\n" \
  "ds_read_b64_tr_b16 a[248:249], v19 offset:49152\n" \
  "ds_read_b64_tr_b16 a[250:251], v19 offset:50176\n" \
  "ds_read_b64_tr_b16 a[252:253], v19 offset:57344\n" \
  "ds_read_b64_tr_b16 a[254:255], v19 offset:58368\n" \
  "s_waitcnt vmcnt(16) lgkmcnt(0)\n" \
  "s_barrier\n" \
  "s_add_u32 m0, s86, 32768\n" \
  "s_mov_b32 s85, 2621440\n" \
  "buffer_load_dwordx4 v16, s[56:59], s85 offen nt lds\n" \
  "s_add_u32 m0, s86, 33792\n" \
  "s_mov_b32 s85, 2637824\n" \
  "buffer_load_dwordx4 v16, s[56:59], s85 offen nt lds\n" \
  "s_add_u32 m0, s86, 34816\n" \
  "s_mov_b32 s85, 2654208\n" \
  "buffer_load_dwordx4 v17, s[56:59], s85 offen nt lds\n" \
  "s_add_u32 m0, s86, 35840\n" \
  "s_mov_b32 s85, 2670592\n" \
  "buffer_load_dwordx4 v17, s[56:59], s85 offen nt lds\n" \
  "s_add_u32 m0, s86, 49152\n" \
  "s_mov_b32 s85, 2621440\n" \
  "buffer_load_dwordx4 v16, s[60:63], s85 offen nt lds\n" \
  "s_add_u32 m0, s86, 50176\n" \
  "s_mov_b32 s85, 2637824\n" \
  "buffer_load_dwordx4 v16, s[60:63], s85 offen nt lds\n" \
  "s_add_u32 m0, s86, 51200\n" \
  "s_mov_b32 s85, 2654208\n" \
  "buffer_load_dwordx4 v17, s[60:63], s85 offen nt lds\n" \
  "s_add_u32 m0, s86, 52224\n" \
  "s_mov_b32 s85, 2670592\n" \
  "buffer_load_dwordx4 v17, s[60:63], s85 offen nt lds\n" \
  "v_xor_b32 v18, 0x10000, v18\n" \
  "v_xor_b32 v19, 0x10000, v19\n" \
  "ds_read_b64_tr_b16 a[16:17], v18 offset:0\n" \
  "ds_read_b64_tr_b16 a[18:19], v18 offset:1024\n" \
  "ds_read_b64_tr_b16 a[20:21], v18 offset:8192\n" \
  "ds_read_b64_tr_b16 a[22:23], v18 offset:9216\n" \
  "ds_read_b64_tr_b16 a[96:97], v19 offset:0\n" \
  "ds_read_b64_tr_b16 a[98:99], v19 offset:1024\n" \
  "ds_read_b64_tr_b16 a[100:101], v19 offset:8192\n" \
  "ds_read_b64_tr_b16 a[102:103], v19 offset:9216\n" \
  "ds_read_b64_tr_b16 a[176:177], v18 offset:16384\n" \
  "ds_read_b64_tr_b16 a[178:179], v18 offset:17408\n" \
  "ds_read_b64_tr_b16 a[180:181], v18 offset:24576\n" \
  "ds_read_b64_tr_b16 a[182:183], v18 offset:25600\n" \
  "ds_read_b64_tr_b16 v[192:193], v19 offset:16384\n" \
  "ds_read_b64_tr_b16 v[194:195], v19 offset:17408\n" \
  "ds_read_b64_tr_b16 v[196:197], v19 offset:24576\n" \
  "ds_read_b64_tr_b16 v[198:199], v19 offset:25600\n" \
  "s_waitcnt vmcnt(16) lgkmcnt(0)\n" \
  "s_barrier\n" \
  "s_add_u32 m0, s86, 65536\n" \
  "s_mov_b32 s85, 3145728\n" \
  "buffer_load_dwordx4 v16, s[56:59], s85 offen nt lds\n" \
  "s_add_u32 m0, s86, 66560\n" \
  "s_mov_b32 s85, 3162112\n" \
  "buffer_load_dwordx4 v16, s[56:59], s85 offen nt lds\n" \
  "s_add_u32 m0, s86, 67584\n" \
  "s_mov_b32 s85, 3178496\n" \
  "buffer_load_dwordx4 v17, s[56:59], s85 offen nt lds\n" \
  "s_add_u32 m0, s86, 68608\n" \
  "s_mov_b32 s85, 3194880\n" \
  "buffer_load_dwordx4 v17, s[56:59], s85 offen nt lds\n" \
  "s_add_u32 m0, s86, 81920\n" \
  "s_mov_b32 s85, 3145728\n" \
  "buffer_load_dwordx4 v16, s[60:63], s85 offen nt lds\n" \
  "s_add_u32 m0, s86, 82944\n" \
  "s_mov_b32 s85, 3162112\n" \
  "buffer_load_dwordx4 v16, s[60:63], s85 offen nt lds\n" \
  "s_add_u32 m0, s86, 83968\n" \
  "s_mov_b32 s85, 3178496\n" \
  "buffer_load_dwordx4 v17, s[60:63], s85 offen nt lds\n" \
  "s_add_u32 m0, s86, 84992\n" \
  "s_mov_b32 s85, 3194880\n" \
  "buffer_load_dwordx4 v17, s[60:63], s85 offen nt lds\n" \
  "ds_read_b64_tr_b16 a[24:25], v18 offset:32768\n" \
  "ds_read_b64_tr_b16 a[26:27], v18 offset:33792\n" \
  "ds_read_b64_tr_b16 a[28:29], v18 offset:40960\n" \
  "ds_read_b64_tr_b16 a[30:31], v18 offset:41984\n" \
  "ds_read_b64_tr_b16 a[104:105], v19 offset:32768\n" \
  "ds_read_b64_tr_b16 a[106:107], v19 offset:33792\n" \
  "ds_read_b64_tr_b16 a[108:109], v19 offset:40960\n" \
  "ds_read_b64_tr_b16 a[110:111], v19 offset:41984\n" \
  "ds_read_b64_tr_b16 a[184:185], v18 offset:49152\n" \
  "ds_read_b64_tr_b16 a[186:187], v18 offset:50176\n" \
  "ds_read_b64_tr_b16 a[188:189], v18 offset:57344\n" \
  "ds_read_b64_tr_b16 a[190:191], v18 offset:58368\n" \
  "ds_read_b64_tr_b16 v[200:201], v19 offset:49152\n" \
  "ds_read_b64_tr_b16 v[202:203], v19 offset:50176\n" \
  "ds_read_b64_tr_b16 v[204:205], v19 offset:57344\n" \
  "ds_read_b64_tr_b16 v[206:207], v19 offset:58368\n" \
  "s_waitcnt vmcnt(16) lgkmcnt(0)\n" \
  "s_barrier\n" \
  "s_add_u32 m0, s86, 98304\n" \
  "s_mov_b32 s85, 3670016\n" \
  "buffer_load_dwordx4 v16, s[56:59], s85 offen nt lds\n" \
  "s_add_u32 m0, s86, 99328\n" \
  "s_mov_b32 s85, 3686400\n" \
  "buffer_load_dwordx4 v16, s[56:59], s85 offen nt lds\n" \
  "s_add_u32 m0, s86, 100352\n" \
  "s_mov_b32 s85, 3702784\n" \
  "buffer_load_dwordx4 v17, s[56:59], s85 offen nt lds\n" \
  "s_add_u32 m0, s86, 101376\n" \
  "s_mov_b32 s85, 3719168\n" \
  "buffer_load_dwordx4 v17, s[56:59], s85 offen nt lds\n" \
  "s_add_u32 m0, s86, 114688\n" \
  "s_mov_b32 s85, 3670016\n" \
  "buffer_load_dwordx4 v16, s[60:63], s85 offen nt lds\n" \
  "s_add_u32 m0, s86, 115712\n" \
  "s_mov_b32 s85, 3686400\n" \
  "buffer_load_dwordx4 v16, s[60:63], s85 offen nt lds\n" \
  "s_add_u32 m0, s86, 116736\n" \
  "s_mov_b32 s85, 3702784\n" \
  "buffer_load_dwordx4 v17, s[60:63], s85 offen nt lds\n" \
  "s_add_u32 m0, s86, 117760\n" \
  "s_mov_b32 s85, 3719168\n" \
  "buffer_load_dwordx4 v17, s[60:63], s85 offen nt lds\n" \
  "v_xor_b32 v18, 0x10000, v18\n" \
  "v_xor_b32 v19, 0x10000, v19\n" \
  "ds_read_b64_tr_b16 a[32:33], v18 offset:0\n" \
  "ds_read_b64_tr_b16 a[34:35], v18 offset:1024\n" \
  "ds_read_b64_tr_b16 a[36:37], v18 offset:8192\n" \
  "ds_read_b64_tr_b16 a[38:39], v18 offset:9216\n" \
  "ds_read_b64_tr_b16 a[112:113], v19 offset:0\n" \
  "ds_read_b64_tr_b16 a[114:115], v19 offset:1024\n" \
  "ds_read_b64_tr_b16 a[116:117], v19 offset:8192\n" \
  "ds_read_b64_tr_b16 a[118:119], v19 offset:9216\n" \
  "ds_read_b64_tr_b16 a[192:193], v18 offset:16384\n" \
  "ds_read_b64_tr_b16 a[194:195], v18 offset:17408\n" \
  "ds_read_b64_tr_b16 a[196:197], v18 offset:24576\n" \
  "ds_read_b64_tr_b16 a[198:199], v18 offset:25600\n" \
  "ds_read_b64_tr_b16 v[208:209], v19 offset:16384\n" \
  "ds_read_b64_tr_b16 v[210:211], v19 offset:17408\n" \
  "ds_read_b64_tr_b16 v[212:213], v19 offset:24576\n" \
  "ds_read_b64_tr_b16 v[214:215], v19 offset:25600\n" \
  "s_waitcnt vmcnt(16) lgkmcnt(0)\n" \
  "s_barrier\n" \
  "s_add_u32 m0, s86, 0\n" \
  "s_mov_b32 s85, 4194304\n" \
  "buffer_load_dwordx4 v16, s[56:59], s85 offen nt lds\n" \
  "s_add_u32 m0, s86, 1024\n" \
  "s_mov_b32 s85, 4210688\n" \
  "buffer_load_dwordx4 v16, s[56:59], s85 offen nt lds\n" \
  "s_add_u32 m0, s86, 2048\n" \
  "s_mov_b32 s85, 4227072\n" \
  "buffer_load_dwordx4 v17, s[56:59], s85 offen nt lds\n" \
  "s_add_u32 m0, s86, 3072\n" \
  "s_mov_b32 s85, 4243456\n" \
  "buffer_load_dwordx4 v17, s[56:59], s85 offen nt lds\n" \
  "s_add_u32 m0, s86, 16384\n" \
  "s_mov_b32 s85, 4194304\n" \
  "buffer_load_dwordx4 v16, s[60:63], s85 offen nt lds\n" \
  "s_add_u32 m0, s86, 17408\n" \
  "s_mov_b32 s85, 4210688\n" \
  "buffer_load_dwordx4 v16, s[60:63], s85 offen nt lds\n" \
  "s_add_u32 m0, s86, 18432\n" \
  "s_mov_b32 s85, 4227072\n" \
  "buffer_load_dwordx4 v17, s[60:63], s85 offen nt lds\n" \
  "s_add_u32 m0, s86, 19456\n" \
  "s_mov_b32 s85, 4243456\n" \
  "buffer_load_dwordx4 v17, s[60:63], s85 offen nt lds\n" \
  "ds_read_b64_tr_b16 a[40:41], v18 offset:32768\n" \
  "ds_read_b64_tr_b16 a[42:43], v18 offset:33792\n" \
  "ds_read_b64_tr_b16 a[44:45], v18 offset:40960\n" \
  "ds_read_b64_tr_b16 a[46:47], v18 offset:41984\n" \
  "ds_read_b64_tr_b16 a[120:121], v19 offset:32768\n" \
  "ds_read_b64_tr_b16 a[122:123], v19 offset:33792\n" \
  "ds_read_b64_tr_b16 a[124:125], v19 offset:40960\n" \
  "ds_read_b64_tr_b16 a[126:127], v19 offset:41984\n" \
  "ds_read_b64_tr_b16 a[200:201], v18 offset:49152\n" \
  "ds_read_b64_tr_b16 a[202:203], v18 offset:50176\n" \
  "ds_read_b64_tr_b16 a[204:205], v18 offset:57344\n" \
  "ds_read_b64_tr_b16 a[206:207], v18 offset:58368\n" \
  "ds_read_b64_tr_b16 v[216:217], v19 offset:49152\n" \
  "ds_read_b64_tr_b16 v[218:219], v19 offset:50176\n" \
  "ds_read_b64_tr_b16 v[220:221], v19 offset:57344\n" \
  "ds_read_b64_tr_b16 v[222:223], v19 offset:58368\n" \
  "s_waitcnt vmcnt(16) lgkmcnt(0)\n" \
  "s_barrier\n" \
  "s_add_u32 m0, s86, 32768\n" \
  "s_mov_b32 s85, 4718592\n" \
  "buffer_load_dwordx4 v16, s[56:59], s85 offen nt lds\n" \
  "s_add_u32 m0, s86, 33792\n" \
  "s_mov_b32 s85, 4734976\n" \
  "buffer_load_dwordx4 v16, s[56:59], s85 offen nt lds\n" \
  "s_add_u32 m0, s86, 34816\n" \
  "s_mov_b32 s85, 4751360\n" \
  "buffer_load_dwordx4 v17, s[56:59], s85 offen nt lds\n" \
  "s_add_u32 m0, s86, 35840\n" \
  "s_mov_b32 s85, 4767744\n" \
  "buffer_load_dwordx4 v17, s[56:59], s85 offen nt lds\n" \
  "s_add_u32 m0, s86, 49152\n" \
  "s_mov_b32 s85, 4718592\n" \
  "buffer_load_dwordx4 v16, s[60:63], s85 offen nt lds\n" \
  "s_add_u32 m0, s86, 50176\n" \
  "s_mov_b32 s85, 4734976\n" \
  "buffer_load_dwordx4 v16, s[60:63], s85 offen nt lds\n" \
  "s_add_u32 m0, s86, 51200\n" \
  "s_mov_b32 s85, 4751360\n" \
  "buffer_load_dwordx4 v17, s[60:63], s85 offen nt lds\n" \
  "s_add_u32 m0, s86, 52224\n" \
  "s_mov_b32 s85, 4767744\n" \
  "buffer_load_dwordx4 v17, s[60:63], s85 offen nt lds\n" \
  "v_xor_b32 v18, 0x10000, v18\n" \
  "v_xor_b32 v19, 0x10000, v19\n" \
  "ds_read_b64_tr_b16 a[48:49], v18 offset:0\n" \
  "ds_read_b64_tr_b16 a[50:51], v18 offset:1024\n" \
  "ds_read_b64_tr_b16 a[52:53], v18 offset:8192\n" \
  "ds_read_b64_tr_b16 a[54:55], v18 offset:9216\n" \
  "ds_read_b64_tr_b16 a[128:129], v19 offset:0\n" \
  "ds_read_b64_tr_b16 a[130:131], v19 offset:1024\n" \
  "ds_read_b64_tr_b16 a[132:133], v19 offset:8192\n" \
  "ds_read_b64_tr_b16 a[134:135], v19 offset:9216\n" \
  "ds_read_b64_tr_b16 a[208:209], v18 offset:16384\n" \
  "ds_read_b64_tr_b16 a[210:211], v18 offset:17408\n" \
  "ds_read_b64_tr_b16 a[212:213], v18 offset:24576\n" \
  "ds_read_b64_tr_b16 a[214:215], v18 offset:25600\n" \
  "ds_read_b64_tr_b16 v[224:225], v19 offset:16384\n" \
  "ds_read_b64_tr_b16 v[226:227], v19 offset:17408\n" \
  "ds_read_b64_tr_b16 v[228:229], v19 offset:24576\n" \
  "ds_read_b64_tr_b16 v[230:231], v19 offset:25600\n" \
  "s_waitcnt vmcnt(16) lgkmcnt(0)\n" \
  "s_barrier\n" \
  "ds_read_b64_tr_b16 a[56:57], v18 offset:32768\n" \
  "ds_read_b64_tr_b16 a[58:59], v18 offset:33792\n" \
  "ds_read_b64_tr_b16 a[60:61], v18 offset:40960\n" \
  "ds_read_b64_tr_b16 a[62:63], v18 offset:41984\n" \
  "ds_read_b64_tr_b16 a[136:137], v19 offset:32768\n" \
  "ds_read_b64_tr_b16 a[138:139], v19 offset:33792\n" \
  "ds_read_b64_tr_b16 a[140:141], v19 offset:40960\n" \
  "ds_read_b64_tr_b16 a[142:143], v19 offset:41984\n" \
  "ds_read_b64_tr_b16 a[216:217], v18 offset:49152\n" \
  "ds_read_b64_tr_b16 a[218:219], v18 offset:50176\n" \
  "ds_read_b64_tr_b16 a[220:221], v18 offset:57344\n" \
  "ds_read_b64_tr_b16 a[222:223], v18 offset:58368\n" \
  "ds_read_b64_tr_b16 v[232:233], v19 offset:49152\n" \
  "ds_read_b64_tr_b16 v[234:235], v19 offset:50176\n" \
  "ds_read_b64_tr_b16 v[236:237], v19 offset:57344\n" \
  "ds_read_b64_tr_b16 v[238:239], v19 offset:58368\n" \
  "s_waitcnt vmcnt(8) lgkmcnt(0)\n" \
  "s_barrier\n" \
  "v_xor_b32 v18, 0x10000, v18\n" \
  "v_xor_b32 v19, 0x10000, v19\n" \
  "ds_read_b64_tr_b16 a[64:65], v18 offset:0\n" \
  "ds_read_b64_tr_b16 a[66:67], v18 offset:1024\n" \
  "ds_read_b64_tr_b16 a[68:69], v18 offset:8192\n" \
  "ds_read_b64_tr_b16 a[70:71], v18 offset:9216\n" \
  "ds_read_b64_tr_b16 a[144:145], v19 offset:0\n" \
  "ds_read_b64_tr_b16 a[146:147], v19 offset:1024\n" \
  "ds_read_b64_tr_b16 a[148:149], v19 offset:8192\n" \
  "ds_read_b64_tr_b16 a[150:151], v19 offset:9216\n" \
  "ds_read_b64_tr_b16 a[224:225], v18 offset:16384\n" \
  "ds_read_b64_tr_b16 a[226:227], v18 offset:17408\n" \
  "ds_read_b64_tr_b16 a[228:229], v18 offset:24576\n" \
  "ds_read_b64_tr_b16 a[230:231], v18 offset:25600\n" \
  "ds_read_b64_tr_b16 v[240:241], v19 offset:16384\n" \
  "ds_read_b64_tr_b16 v[242:243], v19 offset:17408\n" \
  "ds_read_b64_tr_b16 v[244:245], v19 offset:24576\n" \
  "ds_read_b64_tr_b16 v[246:247], v19 offset:25600\n" \
  "s_waitcnt vmcnt(0) lgkmcnt(0)\n" \
  "s_barrier\n" \
  "ds_read_b64_tr_b16 a[72:73], v18 offset:32768\n" \
  "ds_read_b64_tr_b16 a[74:75], v18 offset:33792\n" \
  "ds_read_b64_tr_b16 a[76:77], v18 offset:40960\n" \
  "ds_read_b64_tr_b16 a[78:79], v18 offset:41984\n" \
  "ds_read_b64_tr_b16 a[152:153], v19 offset:32768\n" \
  "ds_read_b64_tr_b16 a[154:155], v19 offset:33792\n" \
  "ds_read_b64_tr_b16 a[156:157], v19 offset:40960\n" \
  "ds_read_b64_tr_b16 a[158:159], v19 offset:41984\n" \
  "ds_read_b64_tr_b16 a[232:233], v18 offset:49152\n" \
  "ds_read_b64_tr_b16 a[234:235], v18 offset:50176\n" \
  "ds_read_b64_tr_b16 a[236:237], v18 offset:57344\n" \
  "ds_read_b64_tr_b16 a[238:239], v18 offset:58368\n" \
  "ds_read_b64_tr_b16 v[248:249], v19 offset:49152\n" \
  "ds_read_b64_tr_b16 v[250:251], v19 offset:50176\n" \
  "ds_read_b64_tr_b16 v[252:253], v19 offset:57344\n" \
  "ds_read_b64_tr_b16 v[254:255], v19 offset:58368\n" \
  "s_waitcnt lgkmcnt(0)\n" \
  "s_barrier\n" \
  "s_mov_b32 s55, 0x40000\n" \
  "s_mov_b32 s88, 119808\n" \
  "s_mov_b64 exec, 1\n" \
  "buffer_load_dwordx2 v[188:189], off, s[36:39], s88 sc1\n" \
  "IW%=:\n" \
  "buffer_load_dwordx2 v[190:191], off, s[36:39], s65 offset:1032 sc1\n" \
  "s_sub_u32 s55, s55, 1\n" \
  "s_cbranch_scc1 IX%=\n" \
  "s_waitcnt vmcnt(0)\n" \
  "v_readfirstlane_b32 s85, v188\n" \
  "v_readfirstlane_b32 s86, v189\n" \
  "v_readfirstlane_b32 s10, v190\n" \
  "v_readfirstlane_b32 s11, v191\n" \
  "s_mul_i32 s12, s85, 0x9e3779b1\n" \
  "s_add_u32 s12, s12, 0x7f4a7c15\n" \
  "s_mul_i32 s13, s86, 0x85ebca6b\n" \
  "s_add_u32 s13, s13, 0xc2b2ae35\n" \
  "s_xor_b64 s[10:11], s[10:11], s[12:13]\n" \
  "s_lshr_b32 s12, s11, 1\n" \
  "s_or_b32 s12, s12, s10\n" \
  "s_cbranch_scc0 IX%=\n" \
  "s_sleep 3\n" \
  "s_branch IW%=\n" \
  "IX%=:\n" \
  "s_waitcnt vmcnt(0)\n" \
  "s_mov_b64 exec, -1\n" \
  "s_mov_b32 s55, 0x10000\n" \
  "s_and_b32 s85, s11, 1\n" \
  "s_lshl_b32 s85, s85, 13\n" \
  "s_add_u32 s40, s40, s85\n" \
  "s_cmp_lg_u32 s64, 0\n" \
  "s_cbranch_scc1 NF%=\n" \
  "s_mov_b64 exec, 1\n" \
  "buffer_store_dword v10, off, s[36:39], s76 sc1\n" \
  "s_mov_b64 exec, -1\n" \
  "NF%=:\n" \
  "s_sub_u32 s9, s73, 2113536\n" \
  "s_lshl_b32 s9, s9, 2\n" \
  "s_add_u32 s9, s9, 11534336\n" \
  "s_mov_b64 s[2:3], 0\n" \
  "s_mov_b64 s[4:5], 0\n" \
  "s_mov_b64 s[6:7], 0\n" \
  "s_mov_b32 s80, 0\n" \
  "s_mov_b32 s55, 0x10000\n" \
  "s_mov_b32 s81, 0\n" \
  "s_mov_b32 s82, 65536\n" \
  "s_mov_b32 s83, 196608\n" \
  "s_mov_b32 s89, 1\n" \
  "s_mov_b32 s90, 0x2000\n" \
  "s_mov_b32 s91, 0x3e0293ee\n" \
  "s_mov_b32 s94, 0xcccccccc\n" \
  "s_mov_b32 s95, 0xcccccccc\n" \
  "buffer_load_dword v191, v9, s[36:39], 0 offen sc1\n" \
  "s_waitcnt vmcnt(0)\n" \
  "v_cmp_gt_i32 vcc, s53, v191\n" \
  "s_cbranch_vccz NCg%=\n" \
  "NBg%=:\n" \
  "s_cmp_eq_u32 s55, 0\n" \
  "s_cbranch_scc1 NCg%=\n" \
  "s_sub_u32 s55, s55, 1\n" \
  "s_sleep 2\n" \
  "buffer_load_dword v191, v9, s[36:39], 0 offen sc1\n" \
  "s_waitcnt vmcnt(0)\n" \
  "v_cmp_gt_i32 vcc, s53, v191\n" \
  "s_cbranch_vccnz NBg%=\n" \
  "NCg%=:\n" \
  "s_add_u32 s85, s48, 4096\n" \
  "buffer_load_dwordx4 v[32:35], v0, s[44:47], s48 offen offset:0 sc1\n" \
  "buffer_load_dwordx4 v[36:39], v0, s[44:47], s48 offen offset:1024 sc1\n" \
  "buffer_load_dwordx4 v[40:43], v0, s[44:47], s48 offen offset:2048 sc1\n" \
  "buffer_load_dwordx4 v[44:47], v0, s[44:47], s48 offen offset:3072 sc1\n" \
  "buffer_load_dwordx4 v[48:51], v0, s[44:47], s85 offen offset:0 sc1\n" \
  "buffer_load_dwordx4 v[52:55], v0, s[44:47], s85 offen offset:1024 sc1\n" \
  "buffer_load_dwordx4 v[56:59], v0, s[44:47], s85 offen offset:2048 sc1\n" \
  "buffer_load_dwordx4 v[60:63], v0, s[44:47], s85 offen offset:3072 sc1\n" \
  "buffer_load_dwordx4 v[64:67], v0, s[44:47], s49 offen offset:0 sc1\n" \
  "buffer_load_dwordx4 v[68:71], v0, s[44:47], s49 offen offset:1024 sc1\n" \
  "buffer_load_dwordx4 v[72:75], v0, s[44:47], s49 offen offset:2048 sc1\n" \
  "buffer_load_dwordx4 v[76:79], v0, s[44:47], s49 offen offset:3072 sc1\n" \
  "s_add_u32 s48, s48, 8192\n" \
  "s_add_u32 s49, s49, 8192\n" \
  "s_add_u32 s53, s53, 4\n" \
  "s_bitset0_b32 s48, 17\n" \
  "s_bitset0_b32 s49, 17\n" \
  "v_mov_b64 v[16:17], 0\n" \
  "v_mov_b64 v[18:19], 0\n" \
  "v_mov_b64 v[20:21], 0\n" \
  "v_mov_b64 v[22:23], 0\n" \
  "v_mov_b64 v[24:25], 0\n" \
  "v_mov_b64 v[26:27], 0\n" \
  "v_mov_b64 v[28:29], 0\n" \
  "v_mov_b64 v[30:31], 0\n" \
  "s_waitcnt vmcnt(0) lgkmcnt(0)\n" \
  "s_barrier\n" \
  "LOOP%=:\n" \
  "s_waitcnt vmcnt(2)\n" \
  "s_barrier\n" \
  "ds_read_b128 v[80:83], v0 offset:0\n" \
  "ds_read_b128 v[84:87], v0 offset:3072\n" \
  "ds_read_b128 v[88:91], v0 offset:6144\n" \
  "ds_read_b128 v[92:95], v0 offset:9216\n" \
  "s_waitcnt vmcnt(0)\n" \
  "s_barrier\n" \
  "ds_read_b128 v[96:99], v0 offset:1024\n" \
  "ds_read_b128 v[100:103], v0 offset:4096\n" \
  "ds_read_b128 v[104:107], v0 offset:7168\n" \
  "ds_read_b128 v[108:111], v0 offset:10240\n" \
  "ds_read_b128 v[112:115], v13 offset:2048\n" \
  "ds_read_b128 v[116:119], v13 offset:8192\n" \
  "s_waitcnt lgkmcnt(9)\n" \
  "v_smfmac_f32_16x16x64_bf16 v[16:19], v[80:83], a[0:7], v5\n" \
  "v_smfmac_f32_16x16x64_bf16 v[20:23], v[80:83], a[80:87], v5\n" \
  "s_waitcnt lgkmcnt(8)\n" \
  "v_smfmac_f32_16x16x64_bf16 v[16:19], v[84:87], a[8:15], v5\n" \
  "v_smfmac_f32_16x16x64_bf16 v[20:23], v[84:87], a[88:95], v5\n" \
  "s_waitcnt lgkmcnt(7)\n" \
  "v_smfmac_f32_16x16x64_bf16 v[16:19], v[88:91], a[16:23], v5\n" \
  "v_smfmac_f32_16x16x64_bf16 v[20:23], v[88:91], a[96:103], v5\n" \
  "s_waitcnt lgkmcnt(6)\n" \
  "v_smfmac_f32_16x16x64_bf16 v[16:19], v[92:95], a[24:31], v5\n" \
  "v_smfmac_f32_16x16x64_bf16 v[20:23], v[92:95], a[104:111], v5\n" \
  "s_waitcnt lgkmcnt(5)\n" \
  "v_smfmac_f32_16x16x64_bf16 v[16:19], v[96:99], a[32:39], v5\n" \
  "v_smfmac_f32_16x16x64_bf16 v[20:23], v[96:99], a[112:119], v5\n" \
  "s_waitcnt lgkmcnt(4)\n" \
  "v_smfmac_f32_16x16x64_bf16 v[16:19], v[100:103], a[40:47], v5\n" \
  "v_smfmac_f32_16x16x64_bf16 v[20:23], v[100:103], a[120:127], v5\n" \
  "s_waitcnt lgkmcnt(3)\n" \
  "v_smfmac_f32_16x16x64_bf16 v[16:19], v[104:107], a[48:55], v5\n" \
  "v_smfmac_f32_16x16x64_bf16 v[20:23], v[104:107], a[128:135], v5\n" \
  "s_waitcnt lgkmcnt(2)\n" \
  "v_smfmac_f32_16x16x64_bf16 v[16:19], v[108:111], a[56:63], v5\n" \
  "v_smfmac_f32_16x16x64_bf16 v[20:23], v[108:111], a[136:143], v5\n" \
  "s_waitcnt lgkmcnt(1)\n" \
  "v_smfmac_f32_16x16x64_bf16 v[16:19], v[112:115], a[64:71], v5\n" \
  "v_smfmac_f32_16x16x64_bf16 v[20:23], v[112:115], a[144:151], v5\n" \
  "s_waitcnt lgkmcnt(0)\n" \
  "v_smfmac_f32_16x16x64_bf16 v[16:19], v[116:119], a[72:79], v5\n" \
  "v_smfmac_f32_16x16x64_bf16 v[20:23], v[116:119], a[152:159], v5\n" \
  "v_smfmac_f32_16x16x64_bf16 v[24:27], v[80:83], a[160:167], v5\n" \
  "s_nop 1\n" \
  "v_smfmac_f32_16x16x64_bf16 v[24:27], v[84:87], a[168:175], v5\n" \
  "s_nop 1\n" \
  "v_smfmac_f32_16x16x64_bf16 v[24:27], v[88:91], a[176:183], v5\n" \
  "v_add_f32 v16, v16, v18\n" \
  "v_add_f32 v17, v17, v19\n" \
  "v_add_f32 v20, v20, v22\n" \
  "v_add_f32 v21, v21, v23\n" \
  "v_cvt_pk_bf16_f32 v14, v16, v17\n" \
  "v_cvt_pk_bf16_f32 v120, v20, v21\n" \
  "v_cmp_u_f32 vcc, v16, v17\n" \
  "s_cbranch_vccnz RB%=\n" \
  "RBB%=:\n" \
  "v_smfmac_f32_16x16x64_bf16 v[24:27], v[92:95], a[184:191], v5\n" \
  "ds_write2_b32 v2, v14, v14 offset0:0 offset1:1\n" \
  "v_smfmac_f32_16x16x64_bf16 v[24:27], v[96:99], a[192:199], v5\n" \
  "ds_write2_b32 v2, v120, v120 offset0:128 offset1:129\n" \
  "v_smfmac_f32_16x16x64_bf16 v[24:27], v[100:103], a[200:207], v5\n" \
  "v_smfmac_f32_16x16x64_bf16 v[24:27], v[104:107], a[208:215], v5\n" \
  "v_smfmac_f32_16x16x64_bf16 v[24:27], v[108:111], a[216:223], v5\n" \
  "v_smfmac_f32_16x16x64_bf16 v[24:27], v[112:115], a[224:231], v5\n" \
  "v_smfmac_f32_16x16x64_bf16 v[24:27], v[116:119], a[232:239], v5\n" \
  "s_cmp_lg_u32 s64, 0\n" \
  "s_waitcnt lgkmcnt(0)\n" \
  "s_barrier\n" \
  "s_cbranch_scc1 SIDE%=\n" \
  "v_smfmac_f32_16x16x64_bf16 v[28:31], v[80:83], a[240:247], v5\n" \
  "ds_read_b64_tr_b16 v[120:121], v3 offset:0\n" \
  "v_smfmac_f32_16x16x64_bf16 v[28:31], v[84:87], a[248:255], v5\n" \
  "ds_read_b64_tr_b16 v[122:123], v4 offset:0\n" \
  "v_cvt_pk_bf16_f32 v14, v24, v25\n" \
  "v_cvt_pk_bf16_f32 v15, v26, v27\n" \
  "v_smfmac_f32_16x16x64_bf16 v[28:31], v[88:91], v[192:199], v5\n" \
  "ds_write_b64 v2, v[14:15] offset:4096\n" \
  "v_smfmac_f32_16x16x64_bf16 v[28:31], v[92:95], v[200:207], v5\n" \
  "ds_read_b64_tr_b16 v[124:125], v3 offset:256\n" \
  "v_smfmac_f32_16x16x64_bf16 v[28:31], v[96:99], v[208:215], v5\n" \
  "ds_read_b64_tr_b16 v[126:127], v4 offset:256\n" \
  "v_smfmac_f32_16x16x64_bf16 v[28:31], v[100:103], v[216:223], v5\n" \
  "ds_read_b64_tr_b16 v[128:129], v3 offset:512\n" \
  "v_smfmac_f32_16x16x64_bf16 v[28:31], v[104:107], v[224:231], v5\n" \
  "ds_read_b64_tr_b16 v[130:131], v4 offset:512\n" \
  "v_smfmac_f32_16x16x64_bf16 v[28:31], v[108:111], v[232:239], v5\n" \
  "s_waitcnt lgkmcnt(4)\n" \
  "s_barrier\n" \
  "ds_read_b64_tr_b16 v[132:133], v3 offset:768\n" \
  "v_smfmac_f32_16x16x64_bf16 v[28:31], v[112:115], v[240:247], v5\n" \
  "ds_read_b64_tr_b16 v[134:135], v4 offset:768\n" \
  "v_smfmac_f32_16x16x64_bf16 v[28:31], v[116:119], v[248:255], v5\n" \
  "ds_read_b64_tr_b16 v[136:137], v3 offset:4096\n" \
  "ds_read_b64_tr_b16 v[138:139], v4 offset:4096\n" \
  "v_mfma_f32_16x16x32_bf16 v[140:143], v[32:35], v[120:123], 0\n" \
  "ds_read_b64_tr_b16 v[144:145], v3 offset:4352\n" \
  "ds_read_b64_tr_b16 v[146:147], v4 offset:4352\n" \
  "v_mfma_f32_16x16x32_bf16 v[148:151], v[48:51], v[120:123], 0\n" \
  "s_add_u32 s85, s82, s34\n" \
  "s_add_u32 s86, s82, s35\n" \
  "v_cvt_pk_bf16_f32 v152, v28, v29\n" \
  "s_waitcnt lgkmcnt(8)\n" \
  "v_mfma_f32_16x16x32_bf16 v[140:143], v[36:39], v[124:127], v[140:143]\n" \
  "v_cvt_pk_bf16_f32 v153, v30, v31\n" \
  "ds_write_b64 v2, v[152:153] offset:4608\n" \
  "v_mfma_f32_16x16x32_bf16 v[148:151], v[52:55], v[124:127], v[148:151]\n" \
  "s_waitcnt lgkmcnt(7)\n" \
  "v_mfma_f32_16x16x32_bf16 v[140:143], v[40:43], v[128:131], v[140:143]\n" \
  "v_mfma_f32_16x16x32_bf16 v[148:151], v[56:59], v[128:131], v[148:151]\n" \
  "s_waitcnt lgkmcnt(5)\n" \
  "v_mfma_f32_16x16x32_bf16 v[140:143], v[44:47], v[132:135], v[140:143]\n" \
  "v_mfma_f32_16x16x32_bf16 v[148:151], v[60:63], v[132:135], v[148:151]\n" \
  "s_cmp_eq_u32 s80, 63\n" \
  "s_waitcnt lgkmcnt(0)\n" \
  "s_barrier\n" \
  "ds_read_b64_tr_b16 v[156:157], v3 offset:4608\n" \
  "ds_read_b64_tr_b16 v[158:159], v4 offset:4608\n" \
  "v_mfma_f32_16x16x32_bf16 v[160:163], v[136:139], v[120:123], 0\n" \
  "ds_read_b64_tr_b16 v[120:121], v3 offset:4864\n" \
  "ds_read_b64_tr_b16 v[122:123], v4 offset:4864\n" \
  "v_mfma_f32_16x16x32_bf16 v[160:163], v[144:147], v[124:127], v[160:163]\n" \
  "s_cbranch_scc1 DONE%=\n" \
  "v_cndmask_b32 v140, v140, v148, s[94:95]\n" \
  "v_cndmask_b32 v141, v141, v149, s[94:95]\n" \
  "v_cndmask_b32 v142, v142, v150, s[94:95]\n" \
  "v_cndmask_b32 v143, v143, v151, s[94:95]\n" \
  "v_max_f32 v191, v140, v141\n" \
  "v_max3_f32 v191, v142, v143, v191\n" \
  "s_waitcnt lgkmcnt(2)\n" \
  "v_mfma_f32_16x16x32_bf16 v[160:163], v[156:159], v[128:131], v[160:163]\n" \
  "s_waitcnt lgkmcnt(0)\n" \
  "v_mfma_f32_16x16x32_bf16 v[160:163], v[120:123], v[132:135], v[160:163]\n" \
  "v_max_f32_dpp v190, v191, v191 quad_perm:[2,3,0,1] row_mask:0xf bank_mask:0xf\n" \
  "v_mul_f32 v189, s91, v190\n" \
  "v_fma_f32 v140, v140, s91, -v189\n" \
  "v_fma_f32 v141, v141, s91, -v189\n" \
  "v_fma_f32 v142, v142, s91, -v189\n" \
  "v_fma_f32 v143, v143, s91, -v189\n" \
  "v_exp_f32 v140, v140\n" \
  "v_exp_f32 v141, v141\n" \
  "v_add_f32 v160, v160, v162\n" \
  "v_add_f32 v161, v161, v163\n" \
  "v_max3_f32 v190, v160, v161, v190\n" \
  "v_mov_b32 v191, v190\n" \
  "v_exp_f32 v142, v142\n" \
  "v_add_f32 v188, v140, v141\n" \
  "v_permlane32_swap_b32 v190, v191\n" \
  "v_max_f32 v190, v190, v191\n" \
  "v_mov_b32 v191, v190\n" \
  "v_exp_f32 v143, v143\n" \
  "v_add_f32 v188, v188, v142\n" \
  "v_permlane16_swap_b32 v190, v191\n" \
  "v_max_f32 v190, v190, v191\n" \
  "v_add_f32 v188, v188, v143\n" \
  "v_mul_f32 v187, s91, v190\n" \
  "v_fma_f32 v160, v160, s91, -v187\n" \
  "v_fma_f32 v161, v161, s91, -v187\n" \
  "v_sub_f32 v190, v189, v187\n" \
  "v_exp_f32 v160, v160\n" \
  "v_exp_f32 v161, v161\n" \
  "v_exp_f32 v190, v190\n" \
  "v_add_f32_dpp v188, v188, v188 quad_perm:[2,3,0,1] row_mask:0xf bank_mask:0xf\n" \
  "v_add_f32 v189, v160, v161\n" \
  "v_fmac_f32 v189, v190, v188\n" \
  "v_mov_b32 v191, v189\n" \
  "v_mul_f32 v140, v190, v140\n" \
  "v_mul_f32 v141, v190, v141\n" \
  "v_permlane32_swap_b32 v189, v191\n" \
  "v_add_f32 v189, v189, v191\n" \
  "v_mov_b32 v191, v189\n" \
  "v_mul_f32 v142, v190, v142\n" \
  "v_mul_f32 v143, v190, v143\n" \
  "v_permlane16_swap_b32 v189, v191\n" \
  "v_add_f32 v189, v189, v191\n" \
  "v_rcp_f32 v189, v189\n" \
  "s_nop 0\n" \
  "v_mul_f32 v140, v189, v140\n" \
  "v_mul_f32 v141, v189, v141\n" \
  "v_mul_f32 v142, v189, v142\n" \
  "v_mul_f32 v143, v189, v143\n" \
  "v_cvt_pk_bf16_f32 v120, v140, v141\n" \
  "v_mul_f32 v160, v189, v160\n" \
  "v_cvt_pk_bf16_f32 v121, v142, v143\n" \
  "v_mul_f32 v161, v189, v161\n" \
  "buffer_store_dwordx2 v[120:121], v6, s[40:43], s85 offen sc1\n" \
  "v_cvt_pk_bf16_f32 v191, v160, v161\n" \
  "buffer_store_dword v191, v7, s[40:43], s86 offen sc1\n" \
  "s_add_u32 s87, s82, s70\n" \
  "s_mov_b64 exec, 1\n" \
  "buffer_load_dword v191, off, s[40:43], s87 offset:8 sc1\n" \
  "s_mov_b64 exec, -1\n" \
  "v_mov_b64 v[120:121], -1\n" \
  "s_add_u32 s85, s83, s34\n" \
  "s_add_u32 s86, s83, s35\n" \
  "buffer_store_dwordx2 v[120:121], v6, s[40:43], s85 offen sc1\n" \
  "buffer_store_dword v120, v7, s[40:43], s86 offen sc1\n" \
  "s_waitcnt vmcnt(2)\n" \
  "s_branch COL%=\n" \
  "SIDE%=:\n" \
  "v_smfmac_f32_16x16x64_bf16 v[28:31], v[80:83], a[240:247], v5\n" \
  "s_nop 0\n" \
  "v_smfmac_f32_16x16x64_bf16 v[28:31], v[84:87], a[248:255], v5\n" \
  "s_nop 0\n" \
  "v_cvt_pk_bf16_f32 v14, v24, v25\n" \
  "v_cvt_pk_bf16_f32 v15, v26, v27\n" \
  "v_smfmac_f32_16x16x64_bf16 v[28:31], v[88:91], v[192:199], v5\n" \
  "ds_write_b64 v2, v[14:15] offset:4096\n" \
  "v_smfmac_f32_16x16x64_bf16 v[28:31], v[92:95], v[200:207], v5\n" \
  "v_smfmac_f32_16x16x64_bf16 v[28:31], v[96:99], v[208:215], v5\n" \
  "v_smfmac_f32_16x16x64_bf16 v[28:31], v[100:103], v[216:223], v5\n" \
  "v_smfmac_f32_16x16x64_bf16 v[28:31], v[104:107], v[224:231], v5\n" \
  "v_smfmac_f32_16x16x64_bf16 v[28:31], v[108:111], v[232:239], v5\n" \
  "s_waitcnt lgkmcnt(0)\n" \
  "s_barrier\n" \
  "v_smfmac_f32_16x16x64_bf16 v[28:31], v[112:115], v[240:247], v5\n" \
  "v_smfmac_f32_16x16x64_bf16 v[28:31], v[116:119], v[248:255], v5\n" \
  "s_nop 7\n" \
  "v_cvt_pk_bf16_f32 v152, v28, v29\n" \
  "v_cvt_pk_bf16_f32 v153, v30, v31\n" \
  "ds_write_b64 v2, v[152:153] offset:4608\n" \
  "s_waitcnt lgkmcnt(0)\n" \
  "s_barrier\n" \
  "s_cmp_eq_u32 s64, 3\n" \
  "s_cbranch_scc1 W3%=\n" \
  "v_mov_b32 v190, 0xd02877db\n" \
  "s_add_u32 s87, s82, s70\n" \
  "s_mov_b64 exec, 1\n" \
  "buffer_load_dword v191, off, s[40:43], s87 offset:8 sc1\n" \
  "s_mov_b64 exec, -1\n" \
  "v_mfma_f32_16x16x32_bf16 v[80:83], v[32:35], v[64:67], 0\n" \
  "v_mfma_f32_16x16x32_bf16 v[84:87], v[48:51], v[64:67], 0\n" \
  "v_mfma_f32_16x16x32_bf16 v[80:83], v[36:39], v[68:71], v[80:83]\n" \
  "v_mfma_f32_16x16x32_bf16 v[84:87], v[52:55], v[68:71], v[84:87]\n" \
  "v_mfma_f32_16x16x32_bf16 v[80:83], v[40:43], v[72:75], v[80:83]\n" \
  "v_mfma_f32_16x16x32_bf16 v[84:87], v[56:59], v[72:75], v[84:87]\n" \
  "v_mfma_f32_16x16x32_bf16 v[80:83], v[44:47], v[76:79], v[80:83]\n" \
  "v_mfma_f32_16x16x32_bf16 v[84:87], v[60:63], v[76:79], v[84:87]\n" \
  "ds_read_b64_tr_b16 v[88:89], v3 offset:4096\n" \
  "ds_read_b64_tr_b16 v[90:91], v4 offset:4096\n" \
  "ds_read_b64_tr_b16 v[92:93], v3 offset:4352\n" \
  "ds_read_b64_tr_b16 v[94:95], v4 offset:4352\n" \
  "ds_read_b64_tr_b16 v[96:97], v3 offset:4608\n" \
  "ds_read_b64_tr_b16 v[98:99], v4 offset:4608\n" \
  "ds_read_b64_tr_b16 v[100:101], v3 offset:4864\n" \
  "ds_read_b64_tr_b16 v[102:103], v4 offset:4864\n" \
  "s_waitcnt lgkmcnt(6)\n" \
  "v_mfma_f32_16x16x32_bf16 v[104:107], v[88:91], v[64:67], 0\n" \
  "s_waitcnt lgkmcnt(4)\n" \
  "v_mfma_f32_16x16x32_bf16 v[104:107], v[92:95], v[68:71], v[104:107]\n" \
  "s_waitcnt lgkmcnt(2)\n" \
  "v_mfma_f32_16x16x32_bf16 v[104:107], v[96:99], v[72:75], v[104:107]\n" \
  "s_waitcnt lgkmcnt(0)\n" \
  "v_mfma_f32_16x16x32_bf16 v[104:107], v[100:103], v[76:79], v[104:107]\n" \
  "v_cmp_le_i32 vcc, 0, v1\n" \
  "s_nop 1\n" \
  "v_cndmask_b32 v80, v190, v80, vcc\n" \
  "v_cmp_le_i32 vcc, 1, v1\n" \
  "s_nop 1\n" \
  "v_cndmask_b32 v81, v190, v81, vcc\n" \
  "v_cmp_le_i32 vcc, 2, v1\n" \
  "s_nop 1\n" \
  "v_cndmask_b32 v82, v190, v82, vcc\n" \
  "v_cmp_le_i32 vcc, 3, v1\n" \
  "s_nop 1\n" \
  "v_cndmask_b32 v83, v190, v83, vcc\n" \
  "v_cmp_le_i32 vcc, 16, v1\n" \
  "s_nop 1\n" \
  "v_cndmask_b32 v84, v190, v84, vcc\n" \
  "v_cmp_le_i32 vcc, 17, v1\n" \
  "s_nop 1\n" \
  "v_cndmask_b32 v85, v190, v85, vcc\n" \
  "v_cmp_le_i32 vcc, 18, v1\n" \
  "s_nop 1\n" \
  "v_cndmask_b32 v86, v190, v86, vcc\n" \
  "v_cmp_le_i32 vcc, 19, v1\n" \
  "s_nop 1\n" \
  "v_cndmask_b32 v87, v190, v87, vcc\n" \
  "v_add_f32 v104, v104, v106\n" \
  "v_add_f32 v105, v105, v107\n" \
  "v_max_f32 v190, v80, v81\n" \
  "v_max3_f32 v190, v82, v83, v190\n" \
  "v_max3_f32 v190, v84, v85, v190\n" \
  "v_max3_f32 v190, v86, v87, v190\n" \
  "v_max3_f32 v190, v104, v105, v190\n" \
  "v_mov_b32 v189, v190\n" \
  "s_nop 1\n" \
  "v_permlane32_swap_b32 v190, v189\n" \
  "v_max_f32 v190, v190, v189\n" \
  "v_mov_b32 v189, v190\n" \
  "s_nop 1\n" \
  "v_permlane16_swap_b32 v190, v189\n" \
  "v_max_f32 v190, v190, v189\n" \
  "v_mul_f32 v190, s91, v190\n" \
  "v_fma_f32 v80, v80, s91, -v190\n" \
  "v_fma_f32 v81, v81, s91, -v190\n" \
  "v_fma_f32 v82, v82, s91, -v190\n" \
  "v_fma_f32 v83, v83, s91, -v190\n" \
  "v_fma_f32 v84, v84, s91, -v190\n" \
  "v_fma_f32 v85, v85, s91, -v190\n" \
  "v_fma_f32 v86, v86, s91, -v190\n" \
  "v_fma_f32 v87, v87, s91, -v190\n" \
  "v_fma_f32 v104, v104, s91, -v190\n" \
  "v_fma_f32 v105, v105, s91, -v190\n" \
  "v_exp_f32 v80, v80\n" \
  "v_exp_f32 v81, v81\n" \
  "v_exp_f32 v82, v82\n" \
  "v_exp_f32 v83, v83\n" \
  "v_exp_f32 v84, v84\n" \
  "v_exp_f32 v85, v85\n" \
  "v_exp_f32 v86, v86\n" \
  "v_exp_f32 v87, v87\n" \
  "v_exp_f32 v104, v104\n" \
  "v_exp_f32 v105, v105\n" \
  "v_add_f32 v190, v80, v81\n" \
  "v_add_f32 v190, v190, v82\n" \
  "v_add_f32 v190, v190, v83\n" \
  "v_add_f32 v190, v190, v84\n" \
  "v_add_f32 v190, v190, v85\n" \
  "v_add_f32 v190, v190, v86\n" \
  "v_add_f32 v190, v190, v87\n" \
  "v_add_f32 v190, v190, v104\n" \
  "v_add_f32 v190, v190, v105\n" \
  "v_mov_b32 v189, v190\n" \
  "s_nop 1\n" \
  "v_permlane32_swap_b32 v190, v189\n" \
  "v_add_f32 v190, v190, v189\n" \
  "v_mov_b32 v189, v190\n" \
  "s_nop 1\n" \
  "v_permlane16_swap_b32 v190, v189\n" \
  "v_add_f32 v190, v190, v189\n" \
  "v_rcp_f32 v190, v190\n" \
  "s_nop 0\n" \
  "v_mul_f32 v80, v190, v80\n" \
  "v_mul_f32 v81, v190, v81\n" \
  "v_mul_f32 v82, v190, v82\n" \
  "v_mul_f32 v83, v190, v83\n" \
  "v_mul_f32 v84, v190, v84\n" \
  "v_mul_f32 v85, v190, v85\n" \
  "v_mul_f32 v86, v190, v86\n" \
  "v_mul_f32 v87, v190, v87\n" \
  "v_mul_f32 v104, v190, v104\n" \
  "v_mul_f32 v105, v190, v105\n" \
  "v_cvt_pk_bf16_f32 v88, v80, v81\n" \
  "v_cvt_pk_bf16_f32 v89, v82, v83\n" \
  "v_cvt_pk_bf16_f32 v90, v84, v85\n" \
  "v_cvt_pk_bf16_f32 v91, v86, v87\n" \
  "s_waitcnt vmcnt(0)\n" \
  "v_cvt_pk_bf16_f32 v189, v104, v105\n" \
  "v_lshrrev_b32 v190, 1, v0\n" \
  "buffer_store_dword v189, v8, s[44:47], s51 offen sc1\n" \
  "s_cmp_eq_u32 s64, 1\n" \
  "s_cbranch_scc1 Q0%=\n" \
  "buffer_store_dwordx4 v[88:91], v0, s[44:47], s50 offen sc1\n" \
  "s_branch Q1%=\n" \
  "Q0%=:\n" \
  "buffer_store_dwordx2 v[88:89], v190, s[44:47], s50 offen sc1\n" \
  "Q1%=:\n" \
  "s_cmp_eq_u32 s80, 63\n" \
  "s_cbranch_scc1 LAST%=\n" \
  "COL%=:\n" \
  "v_readfirstlane_b32 s85, v191\n" \
  "s_cmp_ge_i32 s85, s53\n" \
  "s_cbranch_scc1 NCk%=\n" \
  "NBk%=:\n" \
  "s_cmp_eq_u32 s55, 0\n" \
  "s_cbranch_scc1 NCk%=\n" \
  "s_sub_u32 s55, s55, 1\n" \
  "s_sleep 2\n" \
  "buffer_load_dword v191, v9, s[36:39], 0 offen sc1\n" \
  "s_waitcnt vmcnt(0)\n" \
  "v_cmp_gt_i32 vcc, s53, v191\n" \
  "s_cbranch_vccnz NBk%=\n" \
  "NCk%=:\n" \
  "s_add_u32 s85, s48, 4096\n" \
  "buffer_load_dwordx4 v[32:35], v0, s[44:47], s48 offen offset:0 sc1\n" \
  "buffer_load_dwordx4 v[36:39], v0, s[44:47], s48 offen offset:1024 sc1\n" \
  "buffer_load_dwordx4 v[40:43], v0, s[44:47], s48 offen offset:2048 sc1\n" \
  "buffer_load_dwordx4 v[44:47], v0, s[44:47], s48 offen offset:3072 sc1\n" \
  "buffer_load_dwordx4 v[48:51], v0, s[44:47], s85 offen offset:0 sc1\n" \
  "buffer_load_dwordx4 v[52:55], v0, s[44:47], s85 offen offset:1024 sc1\n" \
  "buffer_load_dwordx4 v[56:59], v0, s[44:47], s85 offen offset:2048 sc1\n" \
  "buffer_load_dwordx4 v[60:63], v0, s[44:47], s85 offen offset:3072 sc1\n" \
  "s_cmp_eq_u32 s64, 0\n" \
  "s_cbranch_scc1 NK%=\n" \
  "buffer_load_dwordx4 v[64:67], v0, s[44:47], s49 offen offset:0 sc1\n" \
  "buffer_load_dwordx4 v[68:71], v0, s[44:47], s49 offen offset:1024 sc1\n" \
  "buffer_load_dwordx4 v[72:75], v0, s[44:47], s49 offen offset:2048 sc1\n" \
  "buffer_load_dwordx4 v[76:79], v0, s[44:47], s49 offen offset:3072 sc1\n" \
  "s_branch NK%=\n" \
  "W3%=:\n" \
  "s_cmp_eq_u32 s80, 63\n" \
  "s_cbranch_scc1 LAST%=\n" \
  "s_sleep 3\n" \
  "NK%=:\n" \
  "s_nop 7\n" \
  "s_add_u32 s48, s48, 8192\n" \
  "s_add_u32 s49, s49, 8192\n" \
  "s_add_u32 s53, s53, 4\n" \
  "s_bitset0_b32 s48, 17\n" \
  "s_bitset0_b32 s49, 17\n" \
  "s_add_u32 s50, s50, 2048\n" \
  "s_add_u32 s51, s51, 2048\n" \
  "v_mov_b64 v[16:17], 0\n" \
  "v_mov_b64 v[18:19], 0\n" \
  "v_mov_b64 v[20:21], 0\n" \
  "v_mov_b64 v[22:23], 0\n" \
  "v_mov_b64 v[24:25], 0\n" \
  "v_mov_b64 v[26:27], 0\n" \
  "v_mov_b64 v[28:29], 0\n" \
  "v_mov_b64 v[30:31], 0\n" \
  "s_add_u32 s98, s82, s71\n" \
  "s_cmp_lg_u32 s64, 3\n" \
  "s_cbranch_scc1 NT%=\n" \
  "s_add_u32 s87, s82, s70\n" \
  "s_add_u32 s8, s82, s66\n" \
  "s_mov_b64 exec, 1\n" \
  "buffer_load_dwordx4 v[80:83], off, s[40:43], s87 sc1\n" \
  "buffer_load_dword v191, off, s[40:43], s8 offset:256 sc1\n" \
  "s_mov_b64 exec, -1\n" \
  "s_waitcnt vmcnt(0)\n" \
  "v_readfirstlane_b32 s88, v80\n" \
  "s_cmp_eq_u32 s88, -1\n" \
  "s_cbranch_scc1 RT%=\n" \
  "RTB%=:\n" \
  "v_readfirstlane_b32 s96, v81\n" \
  "v_readfirstlane_b32 s1, v83\n" \
  "v_readfirstlane_b32 s86, v191\n" \
  "s_bfe_u32 s85, s96, 0x90010\n" \
  "s_and_b32 s96, s96, 0x3fff\n" \
  "s_add_u32 s88, s88, s85\n" \
  "s_bfe_u32 s85, s1, 0xa000a\n" \
  "s_cmp_eq_u32 s86, -1\n" \
  "s_cselect_b32 s85, 0, s85\n" \
  "s_add_u32 s88, s88, s85\n" \
  "s_movk_i32 s86, 0x400\n" \
  "NWb%=:\n" \
  "s_memrealtime vcc\n" \
  "s_waitcnt lgkmcnt(0)\n" \
  "s_sub_u32 s85, s88, vcc_lo\n" \
  "s_lshl_b32 s85, s85, 1\n" \
  "s_cmp_lt_i32 s85, 68\n" \
  "s_cbranch_scc1 NXb%=\n" \
  "s_sub_u32 s86, s86, 1\n" \
  "s_cbranch_scc0 NWb%=\n" \
  "NXb%=:\n" \
  "s_waitcnt lgkmcnt(0)\n" \
  "s_memtime s[10:11]\n" \
  "s_memrealtime s[12:13]\n" \
  "s_nop 1\n" \
  "s_memrealtime s[14:15]\n" \
  "s_nop 1\n" \
  "s_memrealtime s[16:17]\n" \
  "s_nop 1\n" \
  "s_memrealtime s[18:19]\n" \
  "s_nop 1\n" \
  "s_memrealtime s[20:21]\n" \
  "s_nop 1\n" \
  "s_memrealtime s[22:23]\n" \
  "s_nop 1\n" \
  "s_memrealtime s[24:25]\n" \
  "s_nop 1\n" \
  "s_memrealtime s[26:27]\n" \
  "s_nop 1\n" \
  "s_memrealtime s[28:29]\n" \
  "s_nop 1\n" \
  "s_memrealtime s[30:31]\n" \
  "s_nop 1\n" \
  "s_mov_b32 s85, 0\n" \
  "s_waitcnt lgkmcnt(0)\n" \
  "s_cmp_eq_u32 s14, s12\n" \
  "s_addc_u32 s85, s85, 0\n" \
  "s_cmp_eq_u32 s16, s12\n" \
  "s_addc_u32 s85, s85, 0\n" \
  "s_cmp_eq_u32 s18, s12\n" \
  "s_addc_u32 s85, s85, 0\n" \
  "s_cmp_eq_u32 s20, s12\n" \
  "s_addc_u32 s85, s85, 0\n" \
  "s_cmp_eq_u32 s22, s12\n" \
  "s_addc_u32 s85, s85, 0\n" \
  "s_cmp_eq_u32 s24, s12\n" \
  "s_addc_u32 s85, s85, 0\n" \
  "s_cmp_eq_u32 s26, s12\n" \
  "s_addc_u32 s85, s85, 0\n" \
  "s_cmp_eq_u32 s28, s12\n" \
  "s_addc_u32 s85, s85, 0\n" \
  "s_cmp_eq_u32 s30, s12\n" \
  "s_addc_u32 s85, s85, 0\n" \
  "s_mul_i32 s85, s85, 12\n" \
  "s_add_u32 s10, s10, s85\n" \
  "s_sub_u32 s85, s88, s12\n" \
  "s_lshl_b32 s85, s85, 1\n" \
  "s_ashr_i32 s85, s85, 1\n" \
  "s_sub_i32 s85, s85, 4\n" \
  "s_max_i32 s85, s85, -1000\n" \
  "s_min_i32 s85, s85, 1000\n" \
  "s_mul_i32 s85, s85, s96\n" \
  "s_ashr_i32 s85, s85, 8\n" \
  "s_add_u32 s97, s10, s85\n" \
  "FWb%=:\n" \
  "s_memtime vcc\n" \
  "s_waitcnt lgkmcnt(0)\n" \
  "s_sub_u32 s85, s97, vcc_lo\n" \
  "s_cmp_gt_i32 s85, 360\n" \
  "s_cbranch_scc1 FWb%=\n" \
  "s_sub_i32 s85, s85, 108\n" \
  "s_max_i32 s85, s85, 0\n" \
  "s_and_b32 s85, s85, 0xfc\n" \
  "s_getpc_b64 vcc\n" \
  "FPb%=:\n" \
  "s_add_u32 vcc_lo, vcc_lo, FEb%=-FPb%=\n" \
  "s_addc_u32 vcc_hi, vcc_hi, 0\n" \
  "s_sub_u32 vcc_lo, vcc_lo, s85\n" \
  "s_subb_u32 vcc_hi, vcc_hi, 0\n" \
  "s_setpc_b64 vcc\n" \
  "s_nop 0\n" \
  "s_nop 0\n" \
  "s_nop 0\n" \
  "s_nop 0\n" \
  "s_nop 0\n" \
  "s_nop 0\n" \
  "s_nop 0\n" \
  "s_nop 0\n" \
  "s_nop 0\n" \
  "s_nop 0\n" \
  "s_nop 0\n" \
  "s_nop 0\n" \
  "s_nop 0\n" \
  "s_nop 0\n" \
  "s_nop 0\n" \
  "s_nop 0\n" \
  "s_nop 0\n" \
  "s_nop 0\n" \
  "s_nop 0\n" \
  "s_nop 0\n" \
  "s_nop 0\n" \
  "s_nop 0\n" \
  "s_nop 0\n" \
  "s_nop 0\n" \
  "s_nop 0\n" \
  "s_nop 0\n" \
  "s_nop 0\n" \
  "s_nop 0\n" \
  "s_nop 0\n" \
  "s_nop 0\n" \
  "s_nop 0\n" \
  "s_nop 0\n" \
  "s_nop 0\n" \
  "s_nop 0\n" \
  "s_nop 0\n" \
  "s_nop 0\n" \
  "s_nop 0\n" \
  "s_nop 0\n" \
  "s_nop 0\n" \
  "s_nop 0\n" \
  "s_nop 0\n" \
  "s_nop 0\n" \
  "s_nop 0\n" \
  "s_nop 0\n" \
  "s_nop 0\n" \
  "s_nop 0\n" \
  "s_nop 0\n" \
  "s_nop 0\n" \
  "s_nop 0\n" \
  "s_nop 0\n" \
  "s_nop 0\n" \
  "s_nop 0\n" \
  "s_nop 0\n" \
  "s_nop 0\n" \
  "s_nop 0\n" \
  "s_nop 0\n" \
  "s_nop 0\n" \
  "s_nop 0\n" \
  "s_nop 0\n" \
  "s_nop 0\n" \
  "s_nop 0\n" \
  "s_nop 0\n" \
  "s_nop 0\n" \
  "s_nop 0\n" \
  "FEb%=:\n" \
  "NT%=:\n" \
  "s_mov_b32 m0, s72\n" \
  "s_barrier\n" \
  "buffer_load_dwordx4 v0, s[40:43], s98 offen sc1 lds\n" \
  "buffer_load_dwordx4 v0, s[40:43], s98 offen offset:1024 sc1 lds\n" \
  "buffer_load_dwordx4 v12, s[40:43], s98 offen offset:2048 sc1 lds\n" \
  "s_add_u32 s80, s80, 1\n" \
  "s_xor_b32 s81, s81, 786432\n" \
  "s_add_u32 s82, s82, 65536\n" \
  "s_and_b32 s82, s82, 262143\n" \
  "s_xor_b32 s83, s82, 131072\n" \
  "s_branch LOOP%=\n" \
  "RB%=:\n" \
  "v_max_u32 v191, v80, v81\n" \
  "v_max3_u32 v191, v82, v83, v191\n" \
  "v_max3_u32 v191, v84, v85, v191\n" \
  "v_max3_u32 v191, v86, v87, v191\n" \
  "v_max3_u32 v191, v88, v89, v191\n" \
  "v_max3_u32 v191, v90, v91, v191\n" \
  "v_max3_u32 v191, v92, v93, v191\n" \
  "v_max3_u32 v191, v94, v95, v191\n" \
  "v_max3_u32 v191, v96, v97, v191\n" \
  "v_max3_u32 v191, v98, v99, v191\n" \
  "v_max3_u32 v191, v100, v101, v191\n" \
  "v_max3_u32 v191, v102, v103, v191\n" \
  "v_max3_u32 v191, v104, v105, v191\n" \
  "v_max3_u32 v191, v106, v107, v191\n" \
  "v_max3_u32 v191, v108, v109, v191\n" \
  "v_max3_u32 v191, v110, v111, v191\n" \
  "v_max3_u32 v191, v112, v113, v191\n" \
  "v_max3_u32 v191, v114, v115, v191\n" \
  "v_max3_u32 v191, v116, v117, v191\n" \
  "v_max3_u32 v191, v118, v119, v191\n" \
  "v_cmp_eq_u32 vcc, -1, v191\n" \
  "s_cbranch_vccz RBB%=\n" \
  "s_cmp_eq_u32 s90, 0\n" \
  "s_cbranch_scc1 RBB%=\n" \
  "s_cmp_eq_u32 s80, 0\n" \
  "s_cbranch_scc1 RBB%=\n" \
  "s_add_u32 s89, s89, 2\n" \
  "s_bitset1_b64 s[2:3], s80\n" \
  "s_cmp_eq_u32 s67, 0\n" \
  "s_cbranch_scc1 RB0%=\n" \
  "s_add_u32 s85, s82, s66\n" \
  "s_mov_b64 exec, 1\n" \
  "buffer_store_dword v10, off, s[40:43], s85 offset:256 sc1\n" \
  "s_mov_b64 exec, -1\n" \
  "RB0%=:\n" \
  "s_nop 7\n" \
  "RB1%=:\n" \
  "s_waitcnt lgkmcnt(0)\n" \
  "s_barrier\n" \
  "s_mov_b32 m0, s72\n" \
  "s_nop 0\n" \
  "buffer_load_dwordx4 v0, s[40:43], s98 offen sc1 lds\n" \
  "buffer_load_dwordx4 v0, s[40:43], s98 offen offset:1024 sc1 lds\n" \
  "buffer_load_dwordx4 v12, s[40:43], s98 offen offset:2048 sc1 lds\n" \
  "s_sub_u32 s90, s90, 1\n" \
  "s_waitcnt vmcnt(0)\n" \
  "s_barrier\n" \
  "ds_read_b128 v[80:83], v0 offset:0\n" \
  "ds_read_b128 v[84:87], v0 offset:3072\n" \
  "ds_read_b128 v[88:91], v0 offset:6144\n" \
  "ds_read_b128 v[92:95], v0 offset:9216\n" \
  "ds_read_b128 v[96:99], v0 offset:1024\n" \
  "ds_read_b128 v[100:103], v0 offset:4096\n" \
  "ds_read_b128 v[104:107], v0 offset:7168\n" \
  "ds_read_b128 v[108:111], v0 offset:10240\n" \
  "ds_read_b128 v[112:115], v13 offset:2048\n" \
  "ds_read_b128 v[116:119], v13 offset:8192\n" \
  "s_waitcnt lgkmcnt(9)\n" \
  "v_max_u32 v191, v80, v81\n" \
  "v_max3_u32 v191, v82, v83, v191\n" \
  "s_waitcnt lgkmcnt(8)\n" \
  "v_max3_u32 v191, v84, v85, v191\n" \
  "v_max3_u32 v191, v86, v87, v191\n" \
  "s_waitcnt lgkmcnt(7)\n" \
  "v_max3_u32 v191, v88, v89, v191\n" \
  "v_max3_u32 v191, v90, v91, v191\n" \
  "s_waitcnt lgkmcnt(6)\n" \
  "v_max3_u32 v191, v92, v93, v191\n" \
  "v_max3_u32 v191, v94, v95, v191\n" \
  "s_waitcnt lgkmcnt(5)\n" \
  "v_max3_u32 v191, v96, v97, v191\n" \
  "v_max3_u32 v191, v98, v99, v191\n" \
  "s_waitcnt lgkmcnt(4)\n" \
  "v_max3_u32 v191, v100, v101, v191\n" \
  "v_max3_u32 v191, v102, v103, v191\n" \
  "s_waitcnt lgkmcnt(3)\n" \
  "v_max3_u32 v191, v104, v105, v191\n" \
  "v_max3_u32 v191, v106, v107, v191\n" \
  "s_waitcnt lgkmcnt(2)\n" \
  "v_max3_u32 v191, v108, v109, v191\n" \
  "v_max3_u32 v191, v110, v111, v191\n" \
  "s_waitcnt lgkmcnt(1)\n" \
  "v_max3_u32 v191, v112, v113, v191\n" \
  "v_max3_u32 v191, v114, v115, v191\n" \
  "s_waitcnt lgkmcnt(0)\n" \
  "v_max3_u32 v191, v116, v117, v191\n" \
  "v_max3_u32 v191, v118, v119, v191\n" \
  "v_cmp_eq_u32 vcc, -1, v191\n" \
  "s_cbranch_vccz RB2%=\n" \
  "s_cmp_lg_u32 s90, 0\n" \
  "s_cbranch_scc1 RB1%=\n" \
  "RB2%=:\n" \
  "v_mov_b64 v[16:17], 0\n" \
  "v_mov_b64 v[18:19], 0\n" \
  "v_mov_b64 v[20:21], 0\n" \
  "v_mov_b64 v[22:23], 0\n" \
  "v_mov_b64 v[24:25], 0\n" \
  "v_mov_b64 v[26:27], 0\n" \
  "v_mov_b64 v[28:29], 0\n" \
  "v_mov_b64 v[30:31], 0\n" \
  "s_branch LOOP%=\n" \
  "LAST%=:\n" \
  "s_waitcnt vmcnt(0)\n" \
  "s_mov_b64 exec, 1\n" \
  "buffer_atomic_add v10, off, s[36:39], s52\n" \
  "s_mov_b64 exec, -1\n" \
  "s_branch DONE%=\n" \
  "RT%=:\n" \
  "s_add_u32 s89, s89, 0x1000000\n" \
  "s_movk_i32 s85, 0x4000\n" \
  "RT1%=:\n" \
  "s_mov_b64 exec, 1\n" \
  "buffer_load_dwordx4 v[80:83], off, s[40:43], s87 sc1\n" \
  "buffer_load_dword v191, off, s[40:43], s8 offset:256 sc1\n" \
  "s_mov_b64 exec, -1\n" \
  "s_waitcnt vmcnt(0)\n" \
  "s_sub_u32 s85, s85, 1\n" \
  "s_cbranch_scc1 RTB%=\n" \
  "v_readfirstlane_b32 s88, v80\n" \
  "s_cmp_eq_u32 s88, -1\n" \
  "s_cbranch_scc1 RT1%=\n" \
  "s_branch RTB%=\n" \
  "DONE%=:\n" \
  "s_cmp_eq_u32 s73, -1\n" \
  "s_cbranch_scc1 END%=\n" \
  "s_mov_b64 exec, 1\n" \
  "v_mov_b32 v32, s89\n" \
  "buffer_store_dword v32, off, s[44:47], s73\n" \
  "v_mov_b32 v33, s2\n" \
  "buffer_store_dword v33, off, s[44:47], s73 offset:40\n" \
  "v_mov_b32 v33, s3\n" \
  "buffer_store_dword v33, off, s[44:47], s73 offset:44\n" \
  "v_mov_b32 v33, s4\n" \
  "buffer_store_dword v33, off, s[44:47], s73 offset:48\n" \
  "v_mov_b32 v33, s5\n" \
  "buffer_store_dword v33, off, s[44:47], s73 offset:52\n" \
  "v_mov_b32 v33, s6\n" \
  "buffer_store_dword v33, off, s[44:47], s73 offset:56\n" \
  "v_mov_b32 v33, s7\n" \
  "buffer_store_dword v33, off, s[44:47], s73 offset:60\n" \
  "s_mov_b64 exec, -1\n" \
  "END%=:\n" \
  "s_waitcnt vmcnt(0) lgkmcnt(0)\n" \
  "s_endpgm\n"
#define REC_V_ASM \
  "s_mov_b32 m0, s72\n" \
  "s_nop 0\n" \
  "global_load_lds_dwordx4 v[24:25], off\n" \
  "s_add_u32 m0, m0, 1024\n" \
  "s_nop 0\n" \
  "global_load_lds_dwordx4 v[26:27], off\n" \
  "s_add_u32 m0, m0, 1024\n" \
  "s_mov_b32 exec_hi, 0\n" \
  "global_load_lds_dwordx4 v[28:29], off\n" \
  "s_mov_b32 exec_hi, -1\n" \
  "s_lshl_b32 s86, s64, 12\n" \
  "s_add_u32 s86, s86, 32768\n" \
  "s_add_u32 m0, s86, 0\n" \
  "s_mov_b32 s85, 0\n" \
  "buffer_load_dwordx4 v20, s[60:63], s85 offen nt lds\n" \
  "s_add_u32 m0, s86, 1024\n" \
  "s_mov_b32 s85, 5120\n" \
  "buffer_load_dwordx4 v20, s[60:63], s85 offen nt lds\n" \
  "s_add_u32 m0, s86, 2048\n" \
  "s_mov_b32 s85, 10240\n" \
  "buffer_load_dwordx4 v21, s[60:63], s85 offen nt lds\n" \
  "s_add_u32 m0, s86, 3072\n" \
  "s_mov_b32 s85, 15360\n" \
  "buffer_load_dwordx4 v21, s[60:63], s85 offen nt lds\n" \
  "s_add_u32 m0, s86, 16384\n" \
  "s_mov_b32 s85, 81920\n" \
  "buffer_load_dwordx4 v20, s[60:63], s85 offen nt lds\n" \
  "s_add_u32 m0, s86, 17408\n" \
  "s_mov_b32 s85, 87040\n" \
  "buffer_load_dwordx4 v20, s[60:63], s85 offen nt lds\n" \
  "s_add_u32 m0, s86, 18432\n" \
  "s_mov_b32 s85, 92160\n" \
  "buffer_load_dwordx4 v21, s[60:63], s85 offen nt lds\n" \
  "s_add_u32 m0, s86, 19456\n" \
  "s_mov_b32 s85, 97280\n" \
  "buffer_load_dwordx4 v21, s[60:63], s85 offen nt lds\n" \
  "s_add_u32 m0, s86, 32768\n" \
  "s_mov_b32 s85, 256\n" \
  "buffer_load_dwordx4 v20, s[60:63], s85 offen nt lds\n" \
  "s_add_u32 m0, s86, 33792\n" \
  "s_mov_b32 s85, 5376\n" \
  "buffer_load_dwordx4 v20, s[60:63], s85 offen nt lds\n" \
  "s_add_u32 m0, s86, 34816\n" \
  "s_mov_b32 s85, 10496\n" \
  "buffer_load_dwordx4 v21, s[60:63], s85 offen nt lds\n" \
  "s_add_u32 m0, s86, 35840\n" \
  "s_mov_b32 s85, 15616\n" \
  "buffer_load_dwordx4 v21, s[60:63], s85 offen nt lds\n" \
  "s_add_u32 m0, s86, 49152\n" \
  "s_mov_b32 s85, 82176\n" \
  "buffer_load_dwordx4 v20, s[60:63], s85 offen nt lds\n" \
  "s_add_u32 m0, s86, 50176\n" \
  "s_mov_b32 s85, 87296\n" \
  "buffer_load_dwordx4 v20, s[60:63], s85 offen nt lds\n" \
  "s_add_u32 m0, s86, 51200\n" \
  "s_mov_b32 s85, 92416\n" \
  "buffer_load_dwordx4 v21, s[60:63], s85 offen nt lds\n" \
  "s_add_u32 m0, s86, 52224\n" \
  "s_mov_b32 s85, 97536\n" \
  "buffer_load_dwordx4 v21, s[60:63], s85 offen nt lds\n" \
  "s_add_u32 m0, s86, 65536\n" \
  "s_mov_b32 s85, 512\n" \
  "buffer_load_dwordx4 v20, s[60:63], s85 offen nt lds\n" \
  "s_add_u32 m0, s86, 66560\n" \
  "s_mov_b32 s85, 5632\n" \
  "buffer_load_dwordx4 v20, s[60:63], s85 offen nt lds\n" \
  "s_add_u32 m0, s86, 67584\n" \
  "s_mov_b32 s85, 10752\n" \
  "buffer_load_dwordx4 v21, s[60:63], s85 offen nt lds\n" \
  "s_add_u32 m0, s86, 68608\n" \
  "s_mov_b32 s85, 15872\n" \
  "buffer_load_dwordx4 v21, s[60:63], s85 offen nt lds\n" \
  "s_add_u32 m0, s86, 81920\n" \
  "s_mov_b32 s85, 82432\n" \
  "buffer_load_dwordx4 v20, s[60:63], s85 offen nt lds\n" \
  "s_add_u32 m0, s86, 82944\n" \
  "s_mov_b32 s85, 87552\n" \
  "buffer_load_dwordx4 v20, s[60:63], s85 offen nt lds\n" \
  "s_add_u32 m0, s86, 83968\n" \
  "s_mov_b32 s85, 92672\n" \
  "buffer_load_dwordx4 v21, s[60:63], s85 offen nt lds\n" \
  "s_add_u32 m0, s86, 84992\n" \
  "s_mov_b32 s85, 97792\n" \
  "buffer_load_dwordx4 v21, s[60:63], s85 offen nt lds\n" \
  "s_waitcnt vmcnt(16) lgkmcnt(0)\n" \
  "s_barrier\n" \
  "s_add_u32 m0, s86, 98304\n" \
  "s_mov_b32 s85, 768\n" \
  "buffer_load_dwordx4 v20, s[60:63], s85 offen nt lds\n" \
  "s_add_u32 m0, s86, 99328\n" \
  "s_mov_b32 s85, 5888\n" \
  "buffer_load_dwordx4 v20, s[60:63], s85 offen nt lds\n" \
  "s_add_u32 m0, s86, 100352\n" \
  "s_mov_b32 s85, 11008\n" \
  "buffer_load_dwordx4 v21, s[60:63], s85 offen nt lds\n" \
  "s_add_u32 m0, s86, 101376\n" \
  "s_mov_b32 s85, 16128\n" \
  "buffer_load_dwordx4 v21, s[60:63], s85 offen nt lds\n" \
  "s_add_u32 m0, s86, 114688\n" \
  "s_mov_b32 s85, 82688\n" \
  "buffer_load_dwordx4 v20, s[60:63], s85 offen nt lds\n" \
  "s_add_u32 m0, s86, 115712\n" \
  "s_mov_b32 s85, 87808\n" \
  "buffer_load_dwordx4 v20, s[60:63], s85 offen nt lds\n" \
  "s_add_u32 m0, s86, 116736\n" \
  "s_mov_b32 s85, 92928\n" \
  "buffer_load_dwordx4 v21, s[60:63], s85 offen nt lds\n" \
  "s_add_u32 m0, s86, 117760\n" \
  "s_mov_b32 s85, 98048\n" \
  "buffer_load_dwordx4 v21, s[60:63], s85 offen nt lds\n" \
  "ds_read_b64_tr_b16 a[0:1], v18 offset:0\n" \
  "ds_read_b64_tr_b16 a[2:3], v18 offset:1024\n" \
  "ds_read_b64_tr_b16 a[4:5], v18 offset:8192\n" \
  "ds_read_b64_tr_b16 a[6:7], v18 offset:9216\n" \
  "ds_read_b64_tr_b16 a[16:17], v19 offset:0\n" \
  "ds_read_b64_tr_b16 a[18:19], v19 offset:1024\n" \
  "ds_read_b64_tr_b16 a[20:21], v19 offset:8192\n" \
  "ds_read_b64_tr_b16 a[22:23], v19 offset:9216\n" \
  "ds_read_b64_tr_b16 a[8:9], v18 offset:16384\n" \
  "ds_read_b64_tr_b16 a[10:11], v18 offset:17408\n" \
  "ds_read_b64_tr_b16 a[12:13], v18 offset:24576\n" \
  "ds_read_b64_tr_b16 a[14:15], v18 offset:25600\n" \
  "ds_read_b64_tr_b16 a[24:25], v19 offset:16384\n" \
  "ds_read_b64_tr_b16 a[26:27], v19 offset:17408\n" \
  "ds_read_b64_tr_b16 a[28:29], v19 offset:24576\n" \
  "ds_read_b64_tr_b16 a[30:31], v19 offset:25600\n" \
  "s_waitcnt vmcnt(16) lgkmcnt(0)\n" \
  "s_barrier\n" \
  "s_add_u32 m0, s86, 0\n" \
  "s_mov_b32 s85, 1024\n" \
  "buffer_load_dwordx4 v20, s[60:63], s85 offen nt lds\n" \
  "s_add_u32 m0, s86, 1024\n" \
  "s_mov_b32 s85, 6144\n" \
  "buffer_load_dwordx4 v20, s[60:63], s85 offen nt lds\n" \
  "s_add_u32 m0, s86, 2048\n" \
  "s_mov_b32 s85, 11264\n" \
  "buffer_load_dwordx4 v21, s[60:63], s85 offen nt lds\n" \
  "s_add_u32 m0, s86, 3072\n" \
  "s_mov_b32 s85, 16384\n" \
  "buffer_load_dwordx4 v21, s[60:63], s85 offen nt lds\n" \
  "s_add_u32 m0, s86, 16384\n" \
  "s_mov_b32 s85, 82944\n" \
  "buffer_load_dwordx4 v20, s[60:63], s85 offen nt lds\n" \
  "s_add_u32 m0, s86, 17408\n" \
  "s_mov_b32 s85, 88064\n" \
  "buffer_load_dwordx4 v20, s[60:63], s85 offen nt lds\n" \
  "s_add_u32 m0, s86, 18432\n" \
  "s_mov_b32 s85, 93184\n" \
  "buffer_load_dwordx4 v21, s[60:63], s85 offen nt lds\n" \
  "s_add_u32 m0, s86, 19456\n" \
  "s_mov_b32 s85, 98304\n" \
  "buffer_load_dwordx4 v21, s[60:63], s85 offen nt lds\n" \
  "ds_read_b64_tr_b16 a[32:33], v18 offset:32768\n" \
  "ds_read_b64_tr_b16 a[34:35], v18 offset:33792\n" \
  "ds_read_b64_tr_b16 a[36:37], v18 offset:40960\n" \
  "ds_read_b64_tr_b16 a[38:39], v18 offset:41984\n" \
  "ds_read_b64_tr_b16 a[48:49], v19 offset:32768\n" \
  "ds_read_b64_tr_b16 a[50:51], v19 offset:33792\n" \
  "ds_read_b64_tr_b16 a[52:53], v19 offset:40960\n" \
  "ds_read_b64_tr_b16 a[54:55], v19 offset:41984\n" \
  "ds_read_b64_tr_b16 a[40:41], v18 offset:49152\n" \
  "ds_read_b64_tr_b16 a[42:43], v18 offset:50176\n" \
  "ds_read_b64_tr_b16 a[44:45], v18 offset:57344\n" \
  "ds_read_b64_tr_b16 a[46:47], v18 offset:58368\n" \
  "ds_read_b64_tr_b16 a[56:57], v19 offset:49152\n" \
  "ds_read_b64_tr_b16 a[58:59], v19 offset:50176\n" \
  "ds_read_b64_tr_b16 a[60:61], v19 offset:57344\n" \
  "ds_read_b64_tr_b16 a[62:63], v19 offset:58368\n" \
  "s_waitcnt vmcnt(16) lgkmcnt(0)\n" \
  "s_barrier\n" \
  "s_add_u32 m0, s86, 32768\n" \
  "s_mov_b32 s85, 0\n" \
  "buffer_load_dwordx4 v16, s[56:59], s85 offen nt lds\n" \
  "s_add_u32 m0, s86, 33792\n" \
  "s_mov_b32 s85, 16384\n" \
  "buffer_load_dwordx4 v16, s[56:59], s85 offen nt lds\n" \
  "s_add_u32 m0, s86, 34816\n" \
  "s_mov_b32 s85, 32768\n" \
  "buffer_load_dwordx4 v17, s[56:59], s85 offen nt lds\n" \
  "s_add_u32 m0, s86, 35840\n" \
  "s_mov_b32 s85, 49152\n" \
  "buffer_load_dwordx4 v17, s[56:59], s85 offen nt lds\n" \
  "s_add_u32 m0, s86, 49152\n" \
  "s_mov_b32 s85, 524288\n" \
  "buffer_load_dwordx4 v16, s[56:59], s85 offen nt lds\n" \
  "s_add_u32 m0, s86, 50176\n" \
  "s_mov_b32 s85, 540672\n" \
  "buffer_load_dwordx4 v16, s[56:59], s85 offen nt lds\n" \
  "s_add_u32 m0, s86, 51200\n" \
  "s_mov_b32 s85, 557056\n" \
  "buffer_load_dwordx4 v17, s[56:59], s85 offen nt lds\n" \
  "s_add_u32 m0, s86, 52224\n" \
  "s_mov_b32 s85, 573440\n" \
  "buffer_load_dwordx4 v17, s[56:59], s85 offen nt lds\n" \
  "v_xor_b32 v18, 0x10000, v18\n" \
  "v_xor_b32 v19, 0x10000, v19\n" \
  "ds_read_b64_tr_b16 a[64:65], v18 offset:0\n" \
  "ds_read_b64_tr_b16 a[66:67], v18 offset:1024\n" \
  "ds_read_b64_tr_b16 a[68:69], v18 offset:8192\n" \
  "ds_read_b64_tr_b16 a[70:71], v18 offset:9216\n" \
  "ds_read_b64_tr_b16 a[80:81], v19 offset:0\n" \
  "ds_read_b64_tr_b16 a[82:83], v19 offset:1024\n" \
  "ds_read_b64_tr_b16 a[84:85], v19 offset:8192\n" \
  "ds_read_b64_tr_b16 a[86:87], v19 offset:9216\n" \
  "ds_read_b64_tr_b16 a[72:73], v18 offset:16384\n" \
  "ds_read_b64_tr_b16 a[74:75], v18 offset:17408\n" \
  "ds_read_b64_tr_b16 a[76:77], v18 offset:24576\n" \
  "ds_read_b64_tr_b16 a[78:79], v18 offset:25600\n" \
  "ds_read_b64_tr_b16 a[88:89], v19 offset:16384\n" \
  "ds_read_b64_tr_b16 a[90:91], v19 offset:17408\n" \
  "ds_read_b64_tr_b16 a[92:93], v19 offset:24576\n" \
  "ds_read_b64_tr_b16 a[94:95], v19 offset:25600\n" \
  "s_waitcnt vmcnt(16) lgkmcnt(0)\n" \
  "s_barrier\n" \
  "s_add_u32 m0, s86, 65536\n" \
  "s_mov_b32 s85, 1048576\n" \
  "buffer_load_dwordx4 v16, s[56:59], s85 offen nt lds\n" \
  "s_add_u32 m0, s86, 66560\n" \
  "s_mov_b32 s85, 1064960\n" \
  "buffer_load_dwordx4 v16, s[56:59], s85 offen nt lds\n" \
  "s_add_u32 m0, s86, 67584\n" \
  "s_mov_b32 s85, 1081344\n" \
  "buffer_load_dwordx4 v17, s[56:59], s85 offen nt lds\n" \
  "s_add_u32 m0, s86, 68608\n" \
  "s_mov_b32 s85, 1097728\n" \
  "buffer_load_dwordx4 v17, s[56:59], s85 offen nt lds\n" \
  "s_add_u32 m0, s86, 81920\n" \
  "s_mov_b32 s85, 1572864\n" \
  "buffer_load_dwordx4 v16, s[56:59], s85 offen nt lds\n" \
  "s_add_u32 m0, s86, 82944\n" \
  "s_mov_b32 s85, 1589248\n" \
  "buffer_load_dwordx4 v16, s[56:59], s85 offen nt lds\n" \
  "s_add_u32 m0, s86, 83968\n" \
  "s_mov_b32 s85, 1605632\n" \
  "buffer_load_dwordx4 v17, s[56:59], s85 offen nt lds\n" \
  "s_add_u32 m0, s86, 84992\n" \
  "s_mov_b32 s85, 1622016\n" \
  "buffer_load_dwordx4 v17, s[56:59], s85 offen nt lds\n" \
  "ds_read_b64_tr_b16 a[96:97], v18 offset:32768\n" \
  "ds_read_b64_tr_b16 a[98:99], v18 offset:33792\n" \
  "ds_read_b64_tr_b16 a[100:101], v18 offset:40960\n" \
  "ds_read_b64_tr_b16 a[102:103], v18 offset:41984\n" \
  "ds_read_b64_tr_b16 a[112:113], v19 offset:32768\n" \
  "ds_read_b64_tr_b16 a[114:115], v19 offset:33792\n" \
  "ds_read_b64_tr_b16 a[116:117], v19 offset:40960\n" \
  "ds_read_b64_tr_b16 a[118:119], v19 offset:41984\n" \
  "ds_read_b64_tr_b16 a[104:105], v18 offset:49152\n" \
  "ds_read_b64_tr_b16 a[106:107], v18 offset:50176\n" \
  "ds_read_b64_tr_b16 a[108:109], v18 offset:57344\n" \
  "ds_read_b64_tr_b16 a[110:111], v18 offset:58368\n" \
  "ds_read_b64_tr_b16 a[120:121], v19 offset:49152\n" \
  "ds_read_b64_tr_b16 a[122:123], v19 offset:50176\n" \
  "ds_read_b64_tr_b16 a[124:125], v19 offset:57344\n" \
  "ds_read_b64_tr_b16 a[126:127], v19 offset:58368\n" \
  "s_waitcnt vmcnt(16) lgkmcnt(0)\n" \
  "s_barrier\n" \
  "s_add_u32 m0, s86, 98304\n" \
  "s_mov_b32 s85, 2097152\n" \
  "buffer_load_dwordx4 v16, s[56:59], s85 offen nt lds\n" \
  "s_add_u32 m0, s86, 99328\n" \
  "s_mov_b32 s85, 2113536\n" \
  "buffer_load_dwordx4 v16, s[56:59], s85 offen nt lds\n" \
  "s_add_u32 m0, s86, 100352\n" \
  "s_mov_b32 s85, 2129920\n" \
  "buffer_load_dwordx4 v17, s[56:59], s85 offen nt lds\n" \
  "s_add_u32 m0, s86, 101376\n" \
  "s_mov_b32 s85, 2146304\n" \
  "buffer_load_dwordx4 v17, s[56:59], s85 offen nt lds\n" \
  "s_add_u32 m0, s86, 114688\n" \
  "s_mov_b32 s85, 2621440\n" \
  "buffer_load_dwordx4 v16, s[56:59], s85 offen nt lds\n" \
  "s_add_u32 m0, s86, 115712\n" \
  "s_mov_b32 s85, 2637824\n" \
  "buffer_load_dwordx4 v16, s[56:59], s85 offen nt lds\n" \
  "s_add_u32 m0, s86, 116736\n" \
  "s_mov_b32 s85, 2654208\n" \
  "buffer_load_dwordx4 v17, s[56:59], s85 offen nt lds\n" \
  "s_add_u32 m0, s86, 117760\n" \
  "s_mov_b32 s85, 2670592\n" \
  "buffer_load_dwordx4 v17, s[56:59], s85 offen nt lds\n" \
  "v_xor_b32 v18, 0x10000, v18\n" \
  "v_xor_b32 v19, 0x10000, v19\n" \
  "ds_read_b64_tr_b16 a[128:129], v18 offset:0\n" \
  "ds_read_b64_tr_b16 a[130:131], v18 offset:1024\n" \
  "ds_read_b64_tr_b16 a[132:133], v18 offset:8192\n" \
  "ds_read_b64_tr_b16 a[134:135], v18 offset:9216\n" \
  "ds_read_b64_tr_b16 a[144:145], v19 offset:0\n" \
  "ds_read_b64_tr_b16 a[146:147], v19 offset:1024\n" \
  "ds_read_b64_tr_b16 a[148:149], v19 offset:8192\n" \
  "ds_read_b64_tr_b16 a[150:151], v19 offset:9216\n" \
  "ds_read_b64_tr_b16 a[136:137], v18 offset:16384\n" \
  "ds_read_b64_tr_b16 a[138:139], v18 offset:17408\n" \
  "ds_read_b64_tr_b16 a[140:141], v18 offset:24576\n" \
  "ds_read_b64_tr_b16 a[142:143], v18 offset:25600\n" \
  "ds_read_b64_tr_b16 a[152:153], v19 offset:16384\n" \
  "ds_read_b64_tr_b16 a[154:155], v19 offset:17408\n" \
  "ds_read_b64_tr_b16 a[156:157], v19 offset:24576\n" \
  "ds_read_b64_tr_b16 a[158:159], v19 offset:25600\n" \
  "s_waitcnt vmcnt(16) lgkmcnt(0)\n" \
  "s_barrier\n" \
  "s_add_u32 m0, s86, 0\n" \
  "s_mov_b32 s85, 3145728\n" \
  "buffer_load_dwordx4 v16, s[56:59], s85 offen nt lds\n" \
  "s_add_u32 m0, s86, 1024\n" \
  "s_mov_b32 s85, 3162112\n" \
  "buffer_load_dwordx4 v16, s[56:59], s85 offen nt lds\n" \
  "s_add_u32 m0, s86, 2048\n" \
  "s_mov_b32 s85, 3178496\n" \
  "buffer_load_dwordx4 v17, s[56:59], s85 offen nt lds\n" \
  "s_add_u32 m0, s86, 3072\n" \
  "s_mov_b32 s85, 3194880\n" \
  "buffer_load_dwordx4 v17, s[56:59], s85 offen nt lds\n" \
  "s_add_u32 m0, s86, 16384\n" \
  "s_mov_b32 s85, 3670016\n" \
  "buffer_load_dwordx4 v16, s[56:59], s85 offen nt lds\n" \
  "s_add_u32 m0, s86, 17408\n" \
  "s_mov_b32 s85, 3686400\n" \
  "buffer_load_dwordx4 v16, s[56:59], s85 offen nt lds\n" \
  "s_add_u32 m0, s86, 18432\n" \
  "s_mov_b32 s85, 3702784\n" \
  "buffer_load_dwordx4 v17, s[56:59], s85 offen nt lds\n" \
  "s_add_u32 m0, s86, 19456\n" \
  "s_mov_b32 s85, 3719168\n" \
  "buffer_load_dwordx4 v17, s[56:59], s85 offen nt lds\n" \
  "ds_read_b64_tr_b16 a[160:161], v18 offset:32768\n" \
  "ds_read_b64_tr_b16 a[162:163], v18 offset:33792\n" \
  "ds_read_b64_tr_b16 a[164:165], v18 offset:40960\n" \
  "ds_read_b64_tr_b16 a[166:167], v18 offset:41984\n" \
  "ds_read_b64_tr_b16 a[240:241], v19 offset:32768\n" \
  "ds_read_b64_tr_b16 a[242:243], v19 offset:33792\n" \
  "ds_read_b64_tr_b16 a[244:245], v19 offset:40960\n" \
  "ds_read_b64_tr_b16 a[246:247], v19 offset:41984\n" \
  "ds_read_b64_tr_b16 a[168:169], v18 offset:49152\n" \
  "ds_read_b64_tr_b16 a[170:171], v18 offset:50176\n" \
  "ds_read_b64_tr_b16 a[172:173], v18 offset:57344\n" \
  "ds_read_b64_tr_b16 a[174:175], v18 offset:58368\n" \
  "ds_read_b64_tr_b16 a[248:249], v19 offset:49152\n" \
  "ds_read_b64_tr_b16 a[250:251], v19 offset:50176\n" \
  "ds_read_b64_tr_b16 a[252:253], v19 offset:57344\n" \
  "ds_read_b64_tr_b16 a[254:255], v19 offset:58368\n" \
  "s_waitcnt vmcnt(16) lgkmcnt(0)\n" \
  "s_barrier\n" \
  "s_add_u32 m0, s86, 32768\n" \
  "s_mov_b32 s85, 4194304\n" \
  "buffer_load_dwordx4 v16, s[56:59], s85 offen nt lds\n" \
  "s_add_u32 m0, s86, 33792\n" \
  "s_mov_b32 s85, 4210688\n" \
  "buffer_load_dwordx4 v16, s[56:59], s85 offen nt lds\n" \
  "s_add_u32 m0, s86, 34816\n" \
  "s_mov_b32 s85, 4227072\n" \
  "buffer_load_dwordx4 v17, s[56:59], s85 offen nt lds\n" \
  "s_add_u32 m0, s86, 35840\n" \
  "s_mov_b32 s85, 4243456\n" \
  "buffer_load_dwordx4 v17, s[56:59], s85 offen nt lds\n" \
  "s_add_u32 m0, s86, 49152\n" \
  "s_mov_b32 s85, 4718592\n" \
  "buffer_load_dwordx4 v16, s[56:59], s85 offen nt lds\n" \
  "s_add_u32 m0, s86, 50176\n" \
  "s_mov_b32 s85, 4734976\n" \
  "buffer_load_dwordx4 v16, s[56:59], s85 offen nt lds\n" \
  "s_add_u32 m0, s86, 51200\n" \
  "s_mov_b32 s85, 4751360\n" \
  "buffer_load_dwordx4 v17, s[56:59], s85 offen nt lds\n" \
  "s_add_u32 m0, s86, 52224\n" \
  "s_mov_b32 s85, 4767744\n" \
  "buffer_load_dwordx4 v17, s[56:59], s85 offen nt lds\n" \
  "v_xor_b32 v18, 0x10000, v18\n" \
  "v_xor_b32 v19, 0x10000, v19\n" \
  "ds_read_b64_tr_b16 a[176:177], v18 offset:0\n" \
  "ds_read_b64_tr_b16 a[178:179], v18 offset:1024\n" \
  "ds_read_b64_tr_b16 a[180:181], v18 offset:8192\n" \
  "ds_read_b64_tr_b16 a[182:183], v18 offset:9216\n" \
  "ds_read_b64_tr_b16 v[192:193], v19 offset:0\n" \
  "ds_read_b64_tr_b16 v[194:195], v19 offset:1024\n" \
  "ds_read_b64_tr_b16 v[196:197], v19 offset:8192\n" \
  "ds_read_b64_tr_b16 v[198:199], v19 offset:9216\n" \
  "ds_read_b64_tr_b16 a[184:185], v18 offset:16384\n" \
  "ds_read_b64_tr_b16 a[186:187], v18 offset:17408\n" \
  "ds_read_b64_tr_b16 a[188:189], v18 offset:24576\n" \
  "ds_read_b64_tr_b16 a[190:191], v18 offset:25600\n" \
  "ds_read_b64_tr_b16 v[200:201], v19 offset:16384\n" \
  "ds_read_b64_tr_b16 v[202:203], v19 offset:17408\n" \
  "ds_read_b64_tr_b16 v[204:205], v19 offset:24576\n" \
  "ds_read_b64_tr_b16 v[206:207], v19 offset:25600\n" \
  "s_waitcnt vmcnt(16) lgkmcnt(0)\n" \
  "s_barrier\n" \
  "ds_read_b64_tr_b16 a[192:193], v18 offset:32768\n" \
  "ds_read_b64_tr_b16 a[194:195], v18 offset:33792\n" \
  "ds_read_b64_tr_b16 a[196:197], v18 offset:40960\n" \
  "ds_read_b64_tr_b16 a[198:199], v18 offset:41984\n" \
  "ds_read_b64_tr_b16 v[208:209], v19 offset:32768\n" \
  "ds_read_b64_tr_b16 v[210:211], v19 offset:33792\n" \
  "ds_read_b64_tr_b16 v[212:213], v19 offset:40960\n" \
  "ds_read_b64_tr_b16 v[214:215], v19 offset:41984\n" \
  "ds_read_b64_tr_b16 a[200:201], v18 offset:49152\n" \
  "ds_read_b64_tr_b16 a[202:203], v18 offset:50176\n" \
  "ds_read_b64_tr_b16 a[204:205], v18 offset:57344\n" \
  "ds_read_b64_tr_b16 a[206:207], v18 offset:58368\n" \
  "ds_read_b64_tr_b16 v[216:217], v19 offset:49152\n" \
  "ds_read_b64_tr_b16 v[218:219], v19 offset:50176\n" \
  "ds_read_b64_tr_b16 v[220:221], v19 offset:57344\n" \
  "ds_read_b64_tr_b16 v[222:223], v19 offset:58368\n" \
  "s_waitcnt vmcnt(8) lgkmcnt(0)\n" \
  "s_barrier\n" \
  "v_xor_b32 v18, 0x10000, v18\n" \
  "v_xor_b32 v19, 0x10000, v19\n" \
  "ds_read_b64_tr_b16 a[208:209], v18 offset:0\n" \
  "ds_read_b64_tr_b16 a[210:211], v18 offset:1024\n" \
  "ds_read_b64_tr_b16 a[212:213], v18 offset:8192\n" \
  "ds_read_b64_tr_b16 a[214:215], v18 offset:9216\n" \
  "ds_read_b64_tr_b16 v[224:225], v19 offset:0\n" \
  "ds_read_b64_tr_b16 v[226:227], v19 offset:1024\n" \
  "ds_read_b64_tr_b16 v[228:229], v19 offset:8192\n" \
  "ds_read_b64_tr_b16 v[230:231], v19 offset:9216\n" \
  "ds_read_b64_tr_b16 a[216:217], v18 offset:16384\n" \
  "ds_read_b64_tr_b16 a[218:219], v18 offset:17408\n" \
  "ds_read_b64_tr_b16 a[220:221], v18 offset:24576\n" \
  "ds_read_b64_tr_b16 a[222:223], v18 offset:25600\n" \
  "ds_read_b64_tr_b16 v[232:233], v19 offset:16384\n" \
  "ds_read_b64_tr_b16 v[234:235], v19 offset:17408\n" \
  "ds_read_b64_tr_b16 v[236:237], v19 offset:24576\n" \
  "ds_read_b64_tr_b16 v[238:239], v19 offset:25600\n" \
  "s_waitcnt vmcnt(0) lgkmcnt(0)\n" \
  "s_barrier\n" \
  "ds_read_b64_tr_b16 a[224:225], v18 offset:32768\n" \
  "ds_read_b64_tr_b16 a[226:227], v18 offset:33792\n" \
  "ds_read_b64_tr_b16 a[228:229], v18 offset:40960\n" \
  "ds_read_b64_tr_b16 a[230:231], v18 offset:41984\n" \
  "ds_read_b64_tr_b16 v[240:241], v19 offset:32768\n" \
  "ds_read_b64_tr_b16 v[242:243], v19 offset:33792\n" \
  "ds_read_b64_tr_b16 v[244:245], v19 offset:40960\n" \
  "ds_read_b64_tr_b16 v[246:247], v19 offset:41984\n" \
  "ds_read_b64_tr_b16 a[232:233], v18 offset:49152\n" \
  "ds_read_b64_tr_b16 a[234:235], v18 offset:50176\n" \
  "ds_read_b64_tr_b16 a[236:237], v18 offset:57344\n" \
  "ds_read_b64_tr_b16 a[238:239], v18 offset:58368\n" \
  "ds_read_b64_tr_b16 v[248:249], v19 offset:49152\n" \
  "ds_read_b64_tr_b16 v[250:251], v19 offset:50176\n" \
  "ds_read_b64_tr_b16 v[252:253], v19 offset:57344\n" \
  "ds_read_b64_tr_b16 v[254:255], v19 offset:58368\n" \
  "s_waitcnt lgkmcnt(0)\n" \
  "s_barrier\n" \
  "s_mov_b32 s55, 0x40000\n" \
  "s_mov_b32 s88, 119808\n" \
  "s_mov_b64 exec, 1\n" \
  "buffer_load_dwordx2 v[188:189], off, s[36:39], s88 sc1\n" \
  "IW%=:\n" \
  "buffer_load_dwordx2 v[190:191], off, s[36:39], s65 offset:1032 sc1\n" \
  "s_sub_u32 s55, s55, 1\n" \
  "s_cbranch_scc1 IX%=\n" \
  "s_waitcnt vmcnt(0)\n" \
  "v_readfirstlane_b32 s85, v188\n" \
  "v_readfirstlane_b32 s86, v189\n" \
  "v_readfirstlane_b32 s10, v190\n" \
  "v_readfirstlane_b32 s11, v191\n" \
  "s_mul_i32 s12, s85, 0x9e3779b1\n" \
  "s_add_u32 s12, s12, 0x7f4a7c15\n" \
  "s_mul_i32 s13, s86, 0x85ebca6b\n" \
  "s_add_u32 s13, s13, 0xc2b2ae35\n" \
  "s_xor_b64 s[10:11], s[10:11], s[12:13]\n" \
  "s_lshr_b32 s12, s11, 1\n" \
  "s_or_b32 s12, s12, s10\n" \
  "s_cbranch_scc0 IX%=\n" \
  "s_sleep 3\n" \
  "s_branch IW%=\n" \
  "IX%=:\n" \
  "s_waitcnt vmcnt(0)\n" \
  "s_mov_b64 exec, -1\n" \
  "s_mov_b32 s55, 0x10000\n" \
  "s_and_b32 s85, s11, 1\n" \
  "s_lshl_b32 s85, s85, 13\n" \
  "s_add_u32 s40, s40, s85\n" \
  "s_cmp_lg_u32 s64, 0\n" \
  "s_cbranch_scc1 NF%=\n" \
  "s_mov_b64 exec, 1\n" \
  "buffer_store_dword v10, off, s[36:39], s76 sc1\n" \
  "s_mov_b64 exec, -1\n" \
  "NF%=:\n" \
  "s_sub_u32 s9, s73, 2113536\n" \
  "s_lshl_b32 s9, s9, 2\n" \
  "s_add_u32 s9, s9, 11534336\n" \
  "s_mov_b64 s[2:3], 0\n" \
  "s_mov_b64 s[4:5], 0\n" \
  "s_mov_b64 s[6:7], 0\n" \
  "s_mov_b32 s80, 0\n" \
  "s_mov_b32 s55, 0x10000\n" \
  "s_mov_b32 s81, 0\n" \
  "s_mov_b32 s82, 65536\n" \
  "s_mov_b32 s83, 196608\n" \
  "s_mov_b32 s89, 1\n" \
  "s_mov_b32 s90, 0x2000\n" \
  "s_mov_b32 s91, 0x3e0293ee\n" \
  "v_mov_b64 v[16:17], 0\n" \
  "v_mov_b64 v[18:19], 0\n" \
  "v_mov_b64 v[20:21], 0\n" \
  "v_mov_b64 v[22:23], 0\n" \
  "v_mov_b64 v[24:25], 0\n" \
  "v_mov_b64 v[26:27], 0\n" \
  "v_mov_b64 v[28:29], 0\n" \
  "v_mov_b64 v[30:31], 0\n" \
  "v_mov_b64 v[32:33], 0\n" \
  "v_mov_b64 v[34:35], 0\n" \
  "v_mov_b64 v[36:37], 0\n" \
  "v_mov_b64 v[38:39], 0\n" \
  "v_mov_b64 v[40:41], 0\n" \
  "v_mov_b64 v[42:43], 0\n" \
  "v_mov_b64 v[44:45], 0\n" \
  "v_mov_b64 v[46:47], 0\n" \
  "v_mov_b64 v[48:49], 0\n" \
  "v_mov_b64 v[50:51], 0\n" \
  "v_mov_b64 v[52:53], 0\n" \
  "v_mov_b64 v[54:55], 0\n" \
  "v_mov_b64 v[56:57], 0\n" \
  "v_mov_b64 v[58:59], 0\n" \
  "v_mov_b64 v[60:61], 0\n" \
  "v_mov_b64 v[62:63], 0\n" \
  "s_waitcnt lgkmcnt(0)\n" \
  "s_barrier\n" \
  "LOOP%=:\n" \
  "s_waitcnt vmcnt(2)\n" \
  "s_barrier\n" \
  "ds_read_b128 v[64:67], v0 offset:0\n" \
  "ds_read_b128 v[68:71], v0 offset:3072\n" \
  "ds_read_b128 v[72:75], v0 offset:6144\n" \
  "ds_read_b128 v[76:79], v0 offset:9216\n" \
  "s_waitcnt vmcnt(0)\n" \
  "s_barrier\n" \
  "ds_read_b128 v[80:83], v0 offset:1024\n" \
  "ds_read_b128 v[84:87], v0 offset:4096\n" \
  "ds_read_b128 v[88:91], v0 offset:7168\n" \
  "ds_read_b128 v[92:95], v0 offset:10240\n" \
  "ds_read_b128 v[96:99], v13 offset:2048\n" \
  "ds_read_b128 v[100:103], v13 offset:8192\n" \
  "s_waitcnt lgkmcnt(9)\n" \
  "v_smfmac_f32_16x16x64_bf16 v[16:19], v[64:67], a[160:167], v5\n" \
  "s_waitcnt lgkmcnt(8)\n" \
  "v_smfmac_f32_16x16x64_bf16 v[16:19], v[68:71], a[168:175], v5\n" \
  "s_waitcnt lgkmcnt(7)\n" \
  "v_smfmac_f32_16x16x64_bf16 v[16:19], v[72:75], a[176:183], v5\n" \
  "s_waitcnt lgkmcnt(6)\n" \
  "v_smfmac_f32_16x16x64_bf16 v[16:19], v[76:79], a[184:191], v5\n" \
  "s_waitcnt lgkmcnt(5)\n" \
  "v_smfmac_f32_16x16x64_bf16 v[16:19], v[80:83], a[192:199], v5\n" \
  "s_waitcnt lgkmcnt(4)\n" \
  "v_smfmac_f32_16x16x64_bf16 v[16:19], v[84:87], a[200:207], v5\n" \
  "s_waitcnt lgkmcnt(3)\n" \
  "v_smfmac_f32_16x16x64_bf16 v[16:19], v[88:91], a[208:215], v5\n" \
  "s_waitcnt lgkmcnt(2)\n" \
  "v_smfmac_f32_16x16x64_bf16 v[16:19], v[92:95], a[216:223], v5\n" \
  "s_waitcnt lgkmcnt(1)\n" \
  "v_smfmac_f32_16x16x64_bf16 v[16:19], v[96:99], a[224:231], v5\n" \
  "s_waitcnt lgkmcnt(0)\n" \
  "v_smfmac_f32_16x16x64_bf16 v[16:19], v[100:103], a[232:239], v5\n" \
  "v_smfmac_f32_16x16x64_bf16 v[20:23], v[64:67], a[240:247], v5\n" \
  "s_nop 1\n" \
  "v_smfmac_f32_16x16x64_bf16 v[20:23], v[68:71], a[248:255], v5\n" \
  "s_nop 1\n" \
  "v_smfmac_f32_16x16x64_bf16 v[20:23], v[72:75], v[192:199], v5\n" \
  "s_nop 0\n" \
  "v_add_f32 v16, v16, v18\n" \
  "v_add_f32 v17, v17, v19\n" \
  "v_cvt_pk_bf16_f32 v14, v16, v17\n" \
  "ds_write_b32 v2, v14\n" \
  "v_cmp_u_f32 vcc, v16, v17\n" \
  "s_cbranch_vccnz RB%=\n" \
  "RBB%=:\n" \
  "v_smfmac_f32_16x16x64_bf16 v[20:23], v[76:79], v[200:207], v5\n" \
  "v_smfmac_f32_16x16x64_bf16 v[20:23], v[80:83], v[208:215], v5\n" \
  "v_smfmac_f32_16x16x64_bf16 v[20:23], v[84:87], v[216:223], v5\n" \
  "v_smfmac_f32_16x16x64_bf16 v[20:23], v[88:91], v[224:231], v5\n" \
  "s_waitcnt lgkmcnt(0)\n" \
  "s_barrier\n" \
  "ds_read_b64_tr_b16 v[64:65], v3 offset:0\n" \
  "ds_read_b64_tr_b16 v[66:67], v4 offset:0\n" \
  "v_smfmac_f32_16x16x64_bf16 v[20:23], v[92:95], v[232:239], v5\n" \
  "v_smfmac_f32_16x16x64_bf16 v[20:23], v[96:99], v[240:247], v5\n" \
  "v_smfmac_f32_16x16x64_bf16 v[20:23], v[100:103], v[248:255], v5\n" \
  "s_add_u32 s86, s81, s32\n" \
  "s_waitcnt lgkmcnt(0)\n" \
  "v_smfmac_f32_16x16x64_bf16 v[24:27], v[64:67], a[0:7], v5\n" \
  "s_nop 1\n" \
  "v_smfmac_f32_16x16x64_bf16 v[28:31], v[64:67], a[16:23], v5\n" \
  "s_nop 1\n" \
  "v_add_f32 v20, v20, v22\n" \
  "v_add_f32 v21, v21, v23\n" \
  "v_cvt_pk_bf16_f32 v15, v20, v21\n" \
  "ds_write_b32 v2, v15 offset:256\n" \
  "v_smfmac_f32_16x16x64_bf16 v[32:35], v[64:67], a[32:39], v5\n" \
  "v_smfmac_f32_16x16x64_bf16 v[36:39], v[64:67], a[48:55], v5\n" \
  "v_smfmac_f32_16x16x64_bf16 v[40:43], v[64:67], a[64:71], v5\n" \
  "v_smfmac_f32_16x16x64_bf16 v[44:47], v[64:67], a[80:87], v5\n" \
  "s_waitcnt lgkmcnt(0)\n" \
  "s_barrier\n" \
  "ds_read_b64_tr_b16 v[68:69], v3 offset:256\n" \
  "ds_read_b64_tr_b16 v[70:71], v4 offset:256\n" \
  "v_smfmac_f32_16x16x64_bf16 v[48:51], v[64:67], a[96:103], v5\n" \
  "v_smfmac_f32_16x16x64_bf16 v[52:55], v[64:67], a[112:119], v5\n" \
  "v_smfmac_f32_16x16x64_bf16 v[56:59], v[64:67], a[128:135], v5\n" \
  "s_waitcnt lgkmcnt(0)\n" \
  "v_smfmac_f32_16x16x64_bf16 v[24:27], v[68:71], a[8:15], v5\n" \
  "v_smfmac_f32_16x16x64_bf16 v[32:35], v[68:71], a[40:47], v5\n" \
  "v_smfmac_f32_16x16x64_bf16 v[28:31], v[68:71], a[24:31], v5\n" \
  "v_smfmac_f32_16x16x64_bf16 v[36:39], v[68:71], a[56:63], v5\n" \
  "v_smfmac_f32_16x16x64_bf16 v[60:63], v[64:67], a[144:151], v5\n" \
  "v_smfmac_f32_16x16x64_bf16 v[40:43], v[68:71], a[72:79], v5\n" \
  "s_nop 2\n" \
  "v_add_f32 v24, v24, v26\n" \
  "v_add_f32 v25, v25, v27\n" \
  "v_add_f32 v32, v32, v34\n" \
  "v_add_f32 v33, v33, v35\n" \
  "v_cvt_pk_bf16_f32 v16, v24, v25\n" \
  "v_cvt_pk_bf16_f32 v18, v32, v33\n" \
  "v_add_f32 v28, v28, v30\n" \
  "v_add_f32 v29, v29, v31\n" \
  "v_add_f32 v36, v36, v38\n" \
  "v_add_f32 v37, v37, v39\n" \
  "v_cvt_pk_bf16_f32 v17, v28, v29\n" \
  "v_cvt_pk_bf16_f32 v19, v36, v37\n" \
  "v_permlane32_swap_b32 v16, v18\n" \
  "v_smfmac_f32_16x16x64_bf16 v[48:51], v[68:71], a[104:111], v5\n" \
  "v_permlane32_swap_b32 v17, v19\n" \
  "v_smfmac_f32_16x16x64_bf16 v[44:47], v[68:71], a[88:95], v5\n" \
  "s_nop 0\n" \
  "v_permlane16_swap_b32 v16, v17\n" \
  "v_permlane16_swap_b32 v18, v19\n" \
  "buffer_store_dwordx4 v[16:19], v0, s[40:43], s86 offen offset:0 sc1\n" \
  "v_smfmac_f32_16x16x64_bf16 v[52:55], v[68:71], a[120:127], v5\n" \
  "v_smfmac_f32_16x16x64_bf16 v[56:59], v[68:71], a[136:143], v5\n" \
  "v_smfmac_f32_16x16x64_bf16 v[60:63], v[68:71], a[152:159], v5\n" \
  "v_add_f32 v40, v40, v42\n" \
  "v_add_f32 v41, v41, v43\n" \
  "v_add_f32 v48, v48, v50\n" \
  "v_add_f32 v49, v49, v51\n" \
  "v_cvt_pk_bf16_f32 v20, v40, v41\n" \
  "v_cvt_pk_bf16_f32 v22, v48, v49\n" \
  "v_add_f32 v44, v44, v46\n" \
  "v_add_f32 v45, v45, v47\n" \
  "v_add_f32 v52, v52, v54\n" \
  "v_add_f32 v53, v53, v55\n" \
  "v_cvt_pk_bf16_f32 v21, v44, v45\n" \
  "v_cvt_pk_bf16_f32 v23, v52, v53\n" \
  "v_permlane32_swap_b32 v20, v22\n" \
  "v_add_f32 v56, v56, v58\n" \
  "v_add_f32 v57, v57, v59\n" \
  "v_permlane32_swap_b32 v21, v23\n" \
  "v_add_f32 v60, v60, v62\n" \
  "v_add_f32 v61, v61, v63\n" \
  "v_permlane16_swap_b32 v20, v21\n" \
  "v_permlane16_swap_b32 v22, v23\n" \
  "v_cvt_pk_bf16_f32 v64, v56, v57\n" \
  "buffer_store_dwordx4 v[20:23], v0, s[40:43], s86 offen offset:1024 sc1\n" \
  "v_cvt_pk_bf16_f32 v65, v60, v61\n" \
  "v_permlane32_swap_b32 v64, v66\n" \
  "s_nop 0\n" \
  "v_permlane32_swap_b32 v65, v67\n" \
  "s_nop 1\n" \
  "v_permlane16_swap_b32 v64, v65\n" \
  "v_permlane16_swap_b32 v66, v67\n" \
  "buffer_store_dwordx4 v[64:67], v12, s[40:43], s86 offen offset:2048 sc1\n" \
  "s_cmp_eq_u32 s80, 63\n" \
  "s_cbranch_scc1 LAST%=\n" \
  "v_mov_b64 v[24:25], 0\n" \
  "v_mov_b64 v[26:27], 0\n" \
  "v_mov_b64 v[28:29], 0\n" \
  "v_mov_b64 v[30:31], 0\n" \
  "v_mov_b64 v[32:33], 0\n" \
  "v_mov_b64 v[34:35], 0\n" \
  "v_mov_b64 v[36:37], 0\n" \
  "v_mov_b64 v[38:39], 0\n" \
  "v_mov_b64 v[40:41], 0\n" \
  "v_mov_b64 v[42:43], 0\n" \
  "v_mov_b64 v[44:45], 0\n" \
  "v_mov_b64 v[46:47], 0\n" \
  "v_mov_b64 v[48:49], 0\n" \
  "v_mov_b64 v[50:51], 0\n" \
  "v_mov_b64 v[52:53], 0\n" \
  "v_mov_b64 v[54:55], 0\n" \
  "v_mov_b64 v[56:57], 0\n" \
  "v_mov_b64 v[58:59], 0\n" \
  "v_mov_b64 v[60:61], 0\n" \
  "v_mov_b64 v[62:63], 0\n" \
  "v_mov_b64 v[16:17], 0\n" \
  "v_mov_b64 v[18:19], 0\n" \
  "v_mov_b64 v[20:21], 0\n" \
  "v_mov_b64 v[22:23], 0\n" \
  "s_add_u32 s98, s82, s71\n" \
  "s_cmp_lg_u32 s64, 3\n" \
  "s_cbranch_scc1 NT%=\n" \
  "s_add_u32 s87, s82, s70\n" \
  "s_add_u32 s8, s82, s66\n" \
  "s_mov_b64 exec, 1\n" \
  "buffer_load_dwordx4 v[64:67], off, s[40:43], s87 sc1\n" \
  "buffer_load_dword v191, off, s[40:43], s8 offset:256 sc1\n" \
  "s_mov_b64 exec, -1\n" \
  "s_waitcnt vmcnt(0)\n" \
  "v_readfirstlane_b32 s88, v64\n" \
  "s_cmp_eq_u32 s88, -1\n" \
  "s_cbranch_scc1 RT%=\n" \
  "RTB%=:\n" \
  "v_readfirstlane_b32 s96, v65\n" \
  "v_readfirstlane_b32 s1, v67\n" \
  "v_readfirstlane_b32 s86, v191\n" \
  "s_bfe_u32 s85, s96, 0x90010\n" \
  "s_and_b32 s96, s96, 0x3fff\n" \
  "s_add_u32 s88, s88, s85\n" \
  "s_bfe_u32 s85, s1, 0xa000a\n" \
  "s_cmp_eq_u32 s86, -1\n" \
  "s_cselect_b32 s85, 0, s85\n" \
  "s_add_u32 s88, s88, s85\n" \
  "s_movk_i32 s86, 0x400\n" \
  "NWb%=:\n" \
  "s_memrealtime vcc\n" \
  "s_waitcnt lgkmcnt(0)\n" \
  "s_sub_u32 s85, s88, vcc_lo\n" \
  "s_lshl_b32 s85, s85, 1\n" \
  "s_cmp_lt_i32 s85, 68\n" \
  "s_cbranch_scc1 NXb%=\n" \
  "s_sub_u32 s86, s86, 1\n" \
  "s_cbranch_scc0 NWb%=\n" \
  "NXb%=:\n" \
  "s_waitcnt lgkmcnt(0)\n" \
  "s_memtime s[10:11]\n" \
  "s_memrealtime s[12:13]\n" \
  "s_nop 1\n" \
  "s_memrealtime s[14:15]\n" \
  "s_nop 1\n" \
  "s_memrealtime s[16:17]\n" \
  "s_nop 1\n" \
  "s_memrealtime s[18:19]\n" \
  "s_nop 1\n" \
  "s_memrealtime s[20:21]\n" \
  "s_nop 1\n" \
  "s_memrealtime s[22:23]\n" \
  "s_nop 1\n" \
  "s_memrealtime s[24:25]\n" \
  "s_nop 1\n" \
  "s_memrealtime s[26:27]\n" \
  "s_nop 1\n" \
  "s_memrealtime s[28:29]\n" \
  "s_nop 1\n" \
  "s_memrealtime s[30:31]\n" \
  "s_nop 1\n" \
  "s_mov_b32 s85, 0\n" \
  "s_waitcnt lgkmcnt(0)\n" \
  "s_cmp_eq_u32 s14, s12\n" \
  "s_addc_u32 s85, s85, 0\n" \
  "s_cmp_eq_u32 s16, s12\n" \
  "s_addc_u32 s85, s85, 0\n" \
  "s_cmp_eq_u32 s18, s12\n" \
  "s_addc_u32 s85, s85, 0\n" \
  "s_cmp_eq_u32 s20, s12\n" \
  "s_addc_u32 s85, s85, 0\n" \
  "s_cmp_eq_u32 s22, s12\n" \
  "s_addc_u32 s85, s85, 0\n" \
  "s_cmp_eq_u32 s24, s12\n" \
  "s_addc_u32 s85, s85, 0\n" \
  "s_cmp_eq_u32 s26, s12\n" \
  "s_addc_u32 s85, s85, 0\n" \
  "s_cmp_eq_u32 s28, s12\n" \
  "s_addc_u32 s85, s85, 0\n" \
  "s_cmp_eq_u32 s30, s12\n" \
  "s_addc_u32 s85, s85, 0\n" \
  "s_mul_i32 s85, s85, 12\n" \
  "s_add_u32 s10, s10, s85\n" \
  "s_sub_u32 s85, s88, s12\n" \
  "s_lshl_b32 s85, s85, 1\n" \
  "s_ashr_i32 s85, s85, 1\n" \
  "s_sub_i32 s85, s85, 4\n" \
  "s_max_i32 s85, s85, -1000\n" \
  "s_min_i32 s85, s85, 1000\n" \
  "s_mul_i32 s85, s85, s96\n" \
  "s_ashr_i32 s85, s85, 8\n" \
  "s_add_u32 s97, s10, s85\n" \
  "FWb%=:\n" \
  "s_memtime vcc\n" \
  "s_waitcnt lgkmcnt(0)\n" \
  "s_sub_u32 s85, s97, vcc_lo\n" \
  "s_cmp_gt_i32 s85, 360\n" \
  "s_cbranch_scc1 FWb%=\n" \
  "s_sub_i32 s85, s85, 108\n" \
  "s_max_i32 s85, s85, 0\n" \
  "s_and_b32 s85, s85, 0xfc\n" \
  "s_getpc_b64 vcc\n" \
  "FPb%=:\n" \
  "s_add_u32 vcc_lo, vcc_lo, FEb%=-FPb%=\n" \
  "s_addc_u32 vcc_hi, vcc_hi, 0\n" \
  "s_sub_u32 vcc_lo, vcc_lo, s85\n" \
  "s_subb_u32 vcc_hi, vcc_hi, 0\n" \
  "s_setpc_b64 vcc\n" \
  "s_nop 0\n" \
  "s_nop 0\n" \
  "s_nop 0\n" \
  "s_nop 0\n" \
  "s_nop 0\n" \
  "s_nop 0\n" \
  "s_nop 0\n" \
  "s_nop 0\n" \
  "s_nop 0\n" \
  "s_nop 0\n" \
  "s_nop 0\n" \
  "s_nop 0\n" \
  "s_nop 0\n" \
  "s_nop 0\n" \
  "s_nop 0\n" \
  "s_nop 0\n" \
  "s_nop 0\n" \
  "s_nop 0\n" \
  "s_nop 0\n" \
  "s_nop 0\n" \
  "s_nop 0\n" \
  "s_nop 0\n" \
  "s_nop 0\n" \
  "s_nop 0\n" \
  "s_nop 0\n" \
  "s_nop 0\n" \
  "s_nop 0\n" \
  "s_nop 0\n" \
  "s_nop 0\n" \
  "s_nop 0\n" \
  "s_nop 0\n" \
  "s_nop 0\n" \
  "s_nop 0\n" \
  "s_nop 0\n" \
  "s_nop 0\n" \
  "s_nop 0\n" \
  "s_nop 0\n" \
  "s_nop 0\n" \
  "s_nop 0\n" \
  "s_nop 0\n" \
  "s_nop 0\n" \
  "s_nop 0\n" \
  "s_nop 0\n" \
  "s_nop 0\n" \
  "s_nop 0\n" \
  "s_nop 0\n" \
  "s_nop 0\n" \
  "s_nop 0\n" \
  "s_nop 0\n" \
  "s_nop 0\n" \
  "s_nop 0\n" \
  "s_nop 0\n" \
  "s_nop 0\n" \
  "s_nop 0\n" \
  "s_nop 0\n" \
  "s_nop 0\n" \
  "s_nop 0\n" \
  "s_nop 0\n" \
  "s_nop 0\n" \
  "s_nop 0\n" \
  "s_nop 0\n" \
  "s_nop 0\n" \
  "s_nop 0\n" \
  "s_nop 0\n" \
  "FEb%=:\n" \
  "NT%=:\n" \
  "s_mov_b32 m0, s72\n" \
  "s_barrier\n" \
  "buffer_load_dwordx4 v0, s[40:43], s98 offen sc1 lds\n" \
  "buffer_load_dwordx4 v0, s[40:43], s98 offen offset:1024 sc1 lds\n" \
  "buffer_load_dwordx4 v12, s[40:43], s98 offen offset:2048 sc1 lds\n" \
  "s_add_u32 s80, s80, 1\n" \
  "s_xor_b32 s81, s81, 786432\n" \
  "s_add_u32 s82, s82, 65536\n" \
  "s_and_b32 s82, s82, 262143\n" \
  "s_xor_b32 s83, s82, 131072\n" \
  "s_branch LOOP%=\n" \
  "RB%=:\n" \
  "v_max_u32 v191, v64, v65\n" \
  "v_max3_u32 v191, v66, v67, v191\n" \
  "v_max3_u32 v191, v68, v69, v191\n" \
  "v_max3_u32 v191, v70, v71, v191\n" \
  "v_max3_u32 v191, v72, v73, v191\n" \
  "v_max3_u32 v191, v74, v75, v191\n" \
  "v_max3_u32 v191, v76, v77, v191\n" \
  "v_max3_u32 v191, v78, v79, v191\n" \
  "v_max3_u32 v191, v80, v81, v191\n" \
  "v_max3_u32 v191, v82, v83, v191\n" \
  "v_max3_u32 v191, v84, v85, v191\n" \
  "v_max3_u32 v191, v86, v87, v191\n" \
  "v_max3_u32 v191, v88, v89, v191\n" \
  "v_max3_u32 v191, v90, v91, v191\n" \
  "v_max3_u32 v191, v92, v93, v191\n" \
  "v_max3_u32 v191, v94, v95, v191\n" \
  "v_max3_u32 v191, v96, v97, v191\n" \
  "v_max3_u32 v191, v98, v99, v191\n" \
  "v_max3_u32 v191, v100, v101, v191\n" \
  "v_max3_u32 v191, v102, v103, v191\n" \
  "v_cmp_eq_u32 vcc, -1, v191\n" \
  "s_cbranch_vccz RBB%=\n" \
  "s_cmp_eq_u32 s90, 0\n" \
  "s_cbranch_scc1 RBB%=\n" \
  "s_cmp_eq_u32 s80, 0\n" \
  "s_cbranch_scc1 RBB%=\n" \
  "s_add_u32 s89, s89, 2\n" \
  "s_bitset1_b64 s[2:3], s80\n" \
  "s_cmp_eq_u32 s67, 0\n" \
  "s_cbranch_scc1 RB0%=\n" \
  "s_add_u32 s85, s82, s66\n" \
  "s_mov_b64 exec, 1\n" \
  "buffer_store_dword v10, off, s[40:43], s85 offset:256 sc1\n" \
  "s_mov_b64 exec, -1\n" \
  "RB0%=:\n" \
  "s_nop 7\n" \
  "RB1%=:\n" \
  "s_waitcnt lgkmcnt(0)\n" \
  "s_barrier\n" \
  "s_mov_b32 m0, s72\n" \
  "s_nop 0\n" \
  "buffer_load_dwordx4 v0, s[40:43], s98 offen sc1 lds\n" \
  "buffer_load_dwordx4 v0, s[40:43], s98 offen offset:1024 sc1 lds\n" \
  "buffer_load_dwordx4 v12, s[40:43], s98 offen offset:2048 sc1 lds\n" \
  "s_sub_u32 s90, s90, 1\n" \
  "s_waitcnt vmcnt(0)\n" \
  "s_barrier\n" \
  "ds_read_b128 v[64:67], v0 offset:0\n" \
  "ds_read_b128 v[68:71], v0 offset:3072\n" \
  "ds_read_b128 v[72:75], v0 offset:6144\n" \
  "ds_read_b128 v[76:79], v0 offset:9216\n" \
  "ds_read_b128 v[80:83], v0 offset:1024\n" \
  "ds_read_b128 v[84:87], v0 offset:4096\n" \
  "ds_read_b128 v[88:91], v0 offset:7168\n" \
  "ds_read_b128 v[92:95], v0 offset:10240\n" \
  "ds_read_b128 v[96:99], v13 offset:2048\n" \
  "ds_read_b128 v[100:103], v13 offset:8192\n" \
  "s_waitcnt lgkmcnt(9)\n" \
  "v_max_u32 v191, v64, v65\n" \
  "v_max3_u32 v191, v66, v67, v191\n" \
  "s_waitcnt lgkmcnt(8)\n" \
  "v_max3_u32 v191, v68, v69, v191\n" \
  "v_max3_u32 v191, v70, v71, v191\n" \
  "s_waitcnt lgkmcnt(7)\n" \
  "v_max3_u32 v191, v72, v73, v191\n" \
  "v_max3_u32 v191, v74, v75, v191\n" \
  "s_waitcnt lgkmcnt(6)\n" \
  "v_max3_u32 v191, v76, v77, v191\n" \
  "v_max3_u32 v191, v78, v79, v191\n" \
  "s_waitcnt lgkmcnt(5)\n" \
  "v_max3_u32 v191, v80, v81, v191\n" \
  "v_max3_u32 v191, v82, v83, v191\n" \
  "s_waitcnt lgkmcnt(4)\n" \
  "v_max3_u32 v191, v84, v85, v191\n" \
  "v_max3_u32 v191, v86, v87, v191\n" \
  "s_waitcnt lgkmcnt(3)\n" \
  "v_max3_u32 v191, v88, v89, v191\n" \
  "v_max3_u32 v191, v90, v91, v191\n" \
  "s_waitcnt lgkmcnt(2)\n" \
  "v_max3_u32 v191, v92, v93, v191\n" \
  "v_max3_u32 v191, v94, v95, v191\n" \
  "s_waitcnt lgkmcnt(1)\n" \
  "v_max3_u32 v191, v96, v97, v191\n" \
  "v_max3_u32 v191, v98, v99, v191\n" \
  "s_waitcnt lgkmcnt(0)\n" \
  "v_max3_u32 v191, v100, v101, v191\n" \
  "v_max3_u32 v191, v102, v103, v191\n" \
  "v_cmp_eq_u32 vcc, -1, v191\n" \
  "s_cbranch_vccz RB2%=\n" \
  "s_cmp_lg_u32 s90, 0\n" \
  "s_cbranch_scc1 RB1%=\n" \
  "RB2%=:\n" \
  "v_mov_b64 v[16:17], 0\n" \
  "v_mov_b64 v[18:19], 0\n" \
  "v_mov_b64 v[20:21], 0\n" \
  "v_mov_b64 v[22:23], 0\n" \
  "s_branch LOOP%=\n" \
  "LAST%=:\n" \
  "buffer_store_dwordx4 v[16:19], v7, s[44:47], s48 offen sc1\n" \
  "s_add_u32 s48, s48, 8388608\n" \
  "buffer_store_dwordx4 v[20:23], v7, s[44:47], s48 offen sc1\n" \
  "s_add_u32 s48, s48, 8388608\n" \
  "buffer_store_dwordx4 v[64:67], v8, s[44:47], s48 offen sc1\n" \
  "s_add_u32 s48, s48, 8388608\n" \
  "s_waitcnt vmcnt(0)\n" \
  "s_mov_b64 exec, 1\n" \
  "buffer_atomic_add v10, off, s[36:39], s52\n" \
  "s_mov_b64 exec, -1\n" \
  "s_branch DONE%=\n" \
  "RT%=:\n" \
  "s_add_u32 s89, s89, 0x1000000\n" \
  "s_movk_i32 s85, 0x4000\n" \
  "RT1%=:\n" \
  "s_mov_b64 exec, 1\n" \
  "buffer_load_dwordx4 v[64:67], off, s[40:43], s87 sc1\n" \
  "buffer_load_dword v191, off, s[40:43], s8 offset:256 sc1\n" \
  "s_mov_b64 exec, -1\n" \
  "s_waitcnt vmcnt(0)\n" \
  "s_sub_u32 s85, s85, 1\n" \
  "s_cbranch_scc1 RTB%=\n" \
  "v_readfirstlane_b32 s88, v64\n" \
  "s_cmp_eq_u32 s88, -1\n" \
  "s_cbranch_scc1 RT1%=\n" \
  "s_branch RTB%=\n" \
  "DONE%=:\n" \
  "s_cmp_eq_u32 s73, -1\n" \
  "s_cbranch_scc1 END%=\n" \
  "s_mov_b64 exec, 1\n" \
  "v_mov_b32 v32, s89\n" \
  "buffer_store_dword v32, off, s[44:47], s73\n" \
  "v_mov_b32 v33, s2\n" \
  "buffer_store_dword v33, off, s[44:47], s73 offset:40\n" \
  "v_mov_b32 v33, s3\n" \
  "buffer_store_dword v33, off, s[44:47], s73 offset:44\n" \
  "v_mov_b32 v33, s4\n" \
  "buffer_store_dword v33, off, s[44:47], s73 offset:48\n" \
  "v_mov_b32 v33, s5\n" \
  "buffer_store_dword v33, off, s[44:47], s73 offset:52\n" \
  "v_mov_b32 v33, s6\n" \
  "buffer_store_dword v33, off, s[44:47], s73 offset:56\n" \
  "v_mov_b32 v33, s7\n" \
  "buffer_store_dword v33, off, s[44:47], s73 offset:60\n" \
  "s_mov_b64 exec, -1\n" \
  "END%=:\n" \
  "s_waitcnt vmcnt(0) lgkmcnt(0)\n" \
  "s_endpgm\n"
#define REC_ASM_CLOBBERS "v255", "a255", "s99", "vcc", "scc", "memory"

typedef unsigned short u16;
typedef unsigned v2u __attribute__((ext_vector_type(2)));
typedef unsigned v4u __attribute__((ext_vector_type(4)));
typedef unsigned v16u __attribute__((ext_vector_type(16)));
typedef unsigned v32u __attribute__((ext_vector_type(32)));
typedef float f2 __attribute__((ext_vector_type(2)));
typedef float f4 __attribute__((ext_vector_type(4)));
typedef __bf16 bf2 __attribute__((ext_vector_type(2)));
typedef __bf16 bf8 __attribute__((ext_vector_type(8)));
typedef __amdgpu_buffer_rsrc_t rsrc_t;
#define LAS __attribute__((address_space(3)))
#define DEV __device__ __forceinline__

constexpr int S = 2048, D = 640, H = 32, HD = 4096, NSEG = 64, FF = 1728;
constexpr unsigned OOBS = 0x80000000u, OOBV = 0x40000000u;

DEV float b2f(u16 v) { return __uint_as_float(((unsigned)v) << 16); }
DEV unsigned pk2(float lo, float hi) { f2 v = {lo, hi}; bf2 b = __builtin_convertvector(v, bf2); return __builtin_bit_cast(unsigned, b); }
DEV float rb(float f) { return __uint_as_float(pk2(f, f) << 16); }
DEV float lo16(unsigned u) { return __uint_as_float(u << 16); }
DEV float hi16(unsigned u) { return __uint_as_float(u & 0xffff0000u); }
DEV f4 mfma32(v4u a, v4u b, f4 c) { return __builtin_amdgcn_mfma_f32_16x16x32_bf16(__builtin_bit_cast(bf8, a), __builtin_bit_cast(bf8, b), c, 0, 0, 0); }
DEV unsigned rfl(unsigned v) { return __builtin_amdgcn_readfirstlane(v); }
DEV void lds_barrier() { asm volatile("s_waitcnt lgkmcnt(0)\n\ts_barrier" ::: "memory"); }
DEV void drain() { asm volatile("s_waitcnt vmcnt(0)" ::: "memory"); }
DEV v4u ld16(rsrc_t r, int vo, int so) { return __builtin_amdgcn_raw_buffer_load_b128(r, vo, so, 16); }
DEV float xsum(float v) { v += __shfl_xor(v, 16); return v + __shfl_xor(v, 32); }

constexpr unsigned MB2 = 2u << 20;
constexpr unsigned WS_MOD = MB2;
constexpr unsigned WS_DBG = WS_MOD + 16384;
constexpr unsigned WS_HF = WS_DBG + 65536;
constexpr unsigned WS_X1 = WS_HF + S * D * 2 + 81920;
constexpr unsigned WS_YF = WS_X1 + S * D * 2;
constexpr unsigned WS_SH = 6 * MB2;
constexpr unsigned SH_K = 0, SH_Q = 524288, SH_P = 1048576;
constexpr unsigned WS_UX = WS_SH + H * MB2;
constexpr unsigned WS_UM = WS_UX + 40 * MB2;
constexpr unsigned WS_FP = 96 * MB2;
constexpr unsigned WS_END = WS_FP + 8 * MB2;
static_assert(WS_UM + 10 * MB2 <= WS_FP && WS_YF + NSEG * 57344 <= WS_SH && WS_END + 2 * MB2 <= (256u << 20), "workspace");
constexpr int CM_UM = 0, CM_UM_SLOT = 48 * 8192;
constexpr int CM_RING = 2 * CM_UM_SLOT, CM_RING_SLOT = 32768, RG_MEM = 512, RG_TB = 11264, RG_PM = 12288, RG_PX = 16384;
constexpr int CM_BYTES = CM_RING + 4 * CM_RING_SLOT;
static_assert(CM_BYTES * 2 <= (2 << 20) - 65536 && CM_BYTES * 2 == 112 * 16384, "hand-off region");
constexpr int PG_QK = 0, PG_VU = 32, PG_SX = 64, PG_VM = 96, PG_X = 128, PG_BACK = 192, PG_G = 224, PG_GO = 232, PG_NEAR = 240, PG_M2 = 248, PG_C = 256, PG_FLAG = 320, PG_R = 384, PG_LN = 448, PG_F = 576, PG_MP = 768, PG_INIT = 928, PG_EPOCH = 936, CTL_BYTES = 944 * 128;
constexpr unsigned EP_K0 = 0x9E3779B1u, EP_C0 = 0x7F4A7C15u, EP_K1 = 0x85EBCA6Bu, EP_C1 = 0xC2B2AE35u;
DEV int amap(int off) { return (off >> 13) * 16384 + (off & 8191); }
DEV int prog(int i) { return i * 128; }
DEV int um_off(int h, int w) { int b = h * 4 + w; return CM_UM + (b / 3) * 8192 + (b % 3) * 2560; }
DEV int mem_blk(int T, int stride) { const int ks = T >> 2, G = T & 3; return ks < 8 ? (ks & 3) * stride + (ks >> 2) * 1024 + G * 256 : (2 * (ks - 8) + (G >> 1)) * stride + 2048 + (G & 1) * 256; }
DEV int swz(int p) { return (p & 1) * 4 + (p >> 1) * 16; }
constexpr int L_MEM = 0, L_QI = 12288, L_VI = 12288, L_STG = 32768, L_CB = 16384, MAIN_LDS = 163840;

struct Args { const u16 *x, *mem0, *Wq, *Wk, *Wv, *Wo, *w1, *w2, *c, *wmod; u16* out; char* ws; char* ctl; int pa, pc, px, pm, pad, dbg, pf, da, db, p9; };

DEV unsigned rt32() { return (unsigned)__builtin_amdgcn_s_memrealtime(); }
DEV void wait_prog(rsrc_t crs, int vo, int need, unsigned& budget) {
  for (;;) {
    asm volatile("" ::: "memory");
    const int v = (int)__builtin_amdgcn_raw_buffer_load_b32(crs, vo, 0, 16);
    if (!__any(v < need) || budget == 0) break;
    budget--; __builtin_amdgcn_s_sleep(4);
  }
  asm volatile("" ::: "memory");
}
DEV rsrc_t ctl_rsrc(const Args& a) { return __builtin_amdgcn_make_buffer_rsrc(a.ctl, 0, CTL_BYTES, 0x00020000); }
DEV void probe_near(const Args& a, int w, LAS unsigned char* L) {
  const int lane = threadIdx.x & 63;
  const rsrc_t rs = __builtin_amdgcn_make_buffer_rsrc(a.ws + (2 << 20) - 65536, 0, 65536, 0x00020000);
  const v4u ones = {~0u, ~0u, ~0u, ~0u};
  unsigned best0 = ~0u, best1 = ~0u;
#pragma unroll
  for (int k = 0; k < 2; k++) __builtin_amdgcn_raw_buffer_store_b128(ones, rs, (lane >> 3) * 8192 + k * 4096 + w * 512 + (lane & 7) * 16, 0, 16);
  for (int i = 0; i < 16; i++) {
    const int off = lane == 0 ? (i & 7) * 8192 + (i >> 3) * 4096 + w * 512 : (int)OOBV;
    unsigned long long c0, c1; unsigned x;
    asm volatile("s_waitcnt vmcnt(0)\n\ts_memtime %0\n\tbuffer_load_dword %2, %3, %4, 0 offen sc1\n\ts_waitcnt vmcnt(0) lgkmcnt(0)\n\ts_memtime %1\n\ts_waitcnt lgkmcnt(0)"
                 : "=&s"(c0), "=&s"(c1), "=&v"(x) : "v"(off), "s"(rs) : "memory");
    if (i & 1) best1 = min(best1, (unsigned)(c1 - c0)); else best0 = min(best0, (unsigned)(c1 - c0));
    if (lane == 0) *(volatile LAS v2u*)(L + w * 8) = (v2u){best0, best1};
  }
}

DEV v2u next_epoch(const Args& a) {
  const v2u e = __builtin_amdgcn_raw_buffer_load_b64(ctl_rsrc(a), (threadIdx.x & 63) == 0 ? prog(PG_EPOCH) : OOBV, 0, 16);
  return (v2u){rfl(e.x) * EP_K0 + EP_C0, rfl(e.y) * EP_K1 + EP_C1};
}
DEV void wait_init(const Args& a, v2u tag) {
  for (unsigned budget = 1u << 18; budget; budget--) {
    asm volatile("" ::: "memory");
    const v2u v = __builtin_amdgcn_raw_buffer_load_b64(ctl_rsrc(a), (threadIdx.x & 63) == 0 ? prog(PG_INIT + (blockIdx.x & 7)) : OOBV, 0, 16);
    if (rfl(v.x) == tag.x && rfl(v.y) == tag.y) break;
    __builtin_amdgcn_s_sleep(3);
  }
  asm volatile("" ::: "memory");
}
template <int NR>
DEV f2 mod_cols(const Args& a, int col0, int row0, bool check, LAS unsigned char* L) {
  const int tid = threadIdx.x, lane = tid & 63, w = tid >> 6, cg = tid & 7, rg = tid >> 3;
  v4u wm[NR]; u16 cv[NR];
#pragma unroll
  for (int r = 0; r < NR; r++) { wm[r] = __builtin_nontemporal_load((const v4u*)(a.wmod + (size_t)(row0 + rg + 32 * r) * 3840 + col0 + cg * 8)); cv[r] = a.c[row0 + rg + 32 * r]; }
  if (check && rfl(w) == 3) wait_init(a, next_epoch(a));
  float acc[8] = {0, 0, 0, 0, 0, 0, 0, 0};
#pragma unroll
  for (int r = 0; r < NR; r++) {
    const float x = b2f(cv[r]), sv = rb(x / (1.f + __expf(-x)));
#pragma unroll
    for (int i = 0; i < 4; i++) { acc[2 * i] += sv * lo16(wm[r][i]); acc[2 * i + 1] += sv * hi16(wm[r][i]); }
  }
  LAS float* rm = (LAS float*)(L + 1024);
#pragma unroll
  for (int i = 0; i < 8; i++) { float t = acc[i]; t += __shfl_xor(t, 8); t = xsum(t); if (lane < 8) rm[w * 64 + lane * 8 + i] = t; }
  lds_barrier();
  const int j = (lane & 31) * 2;
  return (f2){rm[j] + rm[64 + j] + rm[128 + j] + rm[192 + j], rm[j + 1] + rm[65 + j] + rm[129 + j] + rm[193 + j]};
}
DEV int wait_start(const Args& a, int pg, LAS unsigned char* L) {
  if (rfl(threadIdx.x >> 6) == 0) {
    unsigned budget = 1u << 18;
    const int off = prog(pg + (blockIdx.x & 7));
    wait_prog(ctl_rsrc(a), (threadIdx.x & 63) == 0 ? off : OOBV, 0, budget);
    *(LAS unsigned*)(L + 8192) = rfl(__builtin_amdgcn_raw_buffer_load_b32(ctl_rsrc(a), (threadIdx.x & 63) == 0 ? off : OOBV, 0, 16));
  }
  lds_barrier();
  return rfl(*(LAS unsigned*)(L + 8192));
}

DEV void late_mod(const Args& a, int tile, LAS unsigned char* L) {
  const int lane = threadIdx.x & 63, w = rfl(threadIdx.x >> 6);
  const rsrc_t wrs = __builtin_amdgcn_make_buffer_rsrc(a.ws, 0, WS_END, 0x00020000), prs = ctl_rsrc(a);
  {
    const f2 m = mod_cols<20>(a, 1280 + (tile - 63) * 64, 0, false, L);
    if (w == 0) {
      unsigned o = pk2(m[0], m[1]);
      if (tile >= 83 && tile < 93) o = pk2(1.f + lo16(o), 1.f + hi16(o));
      __builtin_amdgcn_raw_buffer_store_b32(o, wrs, lane < 32 ? WS_MOD + (1280 + (tile - 63) * 64) * 2 + lane * 4 : OOBV, 0, 16);
      drain();
      __builtin_amdgcn_raw_buffer_store_b32(1u, prs, lane == 0 ? prog(PG_LN + tile) + 8 : OOBV, 0, 16);
    }
    lds_barrier();
  }
}
#define TS(i) if (a.dbg && tid == 64) *(unsigned*)(a.ws + WS_DBG + 49152 + tile * 32 + (i) * 4) = rt32();
DEV void startup(const Args& a, int tile, LAS unsigned char* L) {
  const int tid = threadIdx.x, lane = tid & 63, w = rfl(tid >> 6), g = lane >> 4, n = lane & 15;
  const rsrc_t wrs = __builtin_amdgcn_make_buffer_rsrc(a.ws, 0, WS_END, 0x00020000), prs = ctl_rsrc(a);
  TS(0)
  v4u v[5];
  if (tile >= 40 && tile < 120) {
    const int mi = tile - 40;
    const f2 m = mod_cols<5>(a, (mi >> 2) * 64, (mi & 3) * 160, true, L);
    if (w == 0) __builtin_amdgcn_raw_buffer_store_b64((v2u){min(__float_as_uint(m[0]), ~1u), min(__float_as_uint(m[1]), ~1u)}, prs, lane < 32 ? prog(PG_MP) + (mi & 3) * 5120 + (mi >> 2) * 256 + lane * 8 : OOBV, 0, 16);
  }
  auto init_share = [&]() {
    const v4u ones = {~0u, ~0u, ~0u, ~0u};
#pragma unroll
    for (int i = 0; i < 4; i++) __builtin_amdgcn_raw_buffer_store_b128(ones, wrs, tile * 16384 + i * 4096 + tid * 16, 0, 16);
    drain();
    lds_barrier();
    if (w == 0) __builtin_amdgcn_raw_buffer_store_b32(1u, prs, lane == 0 ? prog(PG_LN + tile) + 4 : OOBV, 0, 16);
  };
  if (tile >= 40 && tile < 112) init_share();
  if (tile >= 40 && tile < 127) for (int i = 0; i < a.pf; i++) __builtin_amdgcn_s_sleep(32);
  if (tile >= 63 && tile < 103) late_mod(a, tile, L);
#pragma unroll
  for (int k = 0; k < 5; k++) v[k] = __builtin_nontemporal_load((const v4u*)(a.x + (size_t)(tile * 16 + n) * D + (5 * w + k) * 32 + g * 8));
  if ((tile < 40 || (tile >= 120 && tile < 127)) && w == 3) wait_init(a, next_epoch(a));
  TS(1)
  LAS float* red = (LAS float*)L;
  float s = 0;
#pragma unroll
  for (int k = 0; k < 5; k++)
#pragma unroll
    for (int i = 0; i < 4; i++) s += lo16(v[k][i]) + hi16(v[k][i]);
  s = xsum(s);
  if (lane < 16) red[w * 16 + n] = s;
  lds_barrier();
  const float mean = (red[n] + red[16 + n] + red[32 + n] + red[48 + n]) * (1.f / D);
  float q = 0;
#pragma unroll
  for (int k = 0; k < 5; k++)
#pragma unroll
    for (int i = 0; i < 4; i++) { const float x0 = lo16(v[k][i]) - mean, x1 = hi16(v[k][i]) - mean; q += x0 * x0 + x1 * x1; }
  q = xsum(q);
  if (lane < 16) red[64 + w * 16 + n] = q;
  lds_barrier();
  q = red[64 + n] + red[80 + n] + red[96 + n] + red[112 + n];
  const float m = rb(mean), r = 1.f / rb(sqrtf(rb(rb(q * (1.f / D)) + 1e-5f)));
#pragma unroll
  for (int k = 0; k < 5; k++)
#pragma unroll
    for (int i = 0; i < 4; i++) { const unsigned t = pk2(lo16(v[k][i]) - m, hi16(v[k][i]) - m); v[k][i] = pk2(lo16(t) * r, hi16(t) * r); }
#pragma unroll
  for (int k = 0; k < 5; k++) asm volatile("" : "+v"(v[k]));
  TS(2)
  {
    unsigned budget = 1u << 16;
    v4u p[8];
    for (;;) { asm volatile("" ::: "memory");
      unsigned mx = 0;
#pragma unroll
      for (int i = 0; i < 8; i++) { p[i] = ld16(prs, tid < 160 ? tid * 32 + (i & 1) * 16 : OOBV, prog(PG_MP) + (i >> 1) * 5120); mx = max(mx, max(max(p[i].x, p[i].y), max(p[i].z, p[i].w))); }
      if (!__any(mx == ~0u) || budget == 0) break;
      budget--; __builtin_amdgcn_s_sleep(4); }
    v4u o;
#pragma unroll
    for (int i = 0; i < 4; i++) { const int k = i >> 1, e = (i & 1) * 2;
      o[i] = pk2((__uint_as_float(p[k][e]) + __uint_as_float(p[2 + k][e])) + (__uint_as_float(p[4 + k][e]) + __uint_as_float(p[6 + k][e])),
                 (__uint_as_float(p[k][e + 1]) + __uint_as_float(p[2 + k][e + 1])) + (__uint_as_float(p[4 + k][e + 1]) + __uint_as_float(p[6 + k][e + 1])));
      if (tid >= 80) o[i] = pk2(1.f + lo16(o[i]), 1.f + hi16(o[i])); }
    if (tid < 160) *(LAS v4u*)(L + 4096 + tid * 16) = o;
  }
  TS(3)
  lds_barrier();
  TS(4)
#pragma unroll
  for (int k = 0; k < 5; k++) {
    const int d = (5 * w + k) * 32 + g * 8;
    const v4u sh = *(LAS v4u*)(L + 4096 + d * 2), sc = *(LAS v4u*)(L + 4096 + 2 * D + d * 2);
    v4u o;
#pragma unroll
    for (int i = 0; i < 4; i++) {
      const unsigned t = pk2(lo16(v[k][i]) * lo16(sc[i]), hi16(v[k][i]) * hi16(sc[i]));
      o[i] = pk2(lo16(t) + lo16(sh[i]), hi16(t) + hi16(sh[i]));
    }
    __builtin_amdgcn_raw_buffer_store_b128(o, wrs, WS_HF + tile * 20480 + lane * 16 + (5 * w + k) * 1024, 0, 16);
  }
  TS(5)
  drain();
  lds_barrier();
  if (w == 0) __builtin_amdgcn_raw_buffer_store_b32(1u, prs, lane == 0 ? prog(PG_LN + tile) : OOBV, 0, 16);
  TS(6)
  if (tile < 40) init_share();
}
DEV int wmin(int v) {
#pragma unroll
  for (int o = 32; o; o >>= 1) v = min(v, __shfl_xor(v, o));
  return v;
}
DEV void wait_until(unsigned T) { for (int i = 0; i < 2048; i++) if ((int)((T - rt32()) << 1) <= 0) break; }
DEV void role_time(const Args& a, unsigned tstart, LAS unsigned char* L) {
  const int lane = threadIdx.x & 63, w = rfl(threadIdx.x >> 6);
  unsigned budget = 1u << 18, fails = 0, fmask0 = 0, fmask1 = 0;
  const rsrc_t prs = ctl_rsrc(a);
  { const v4u ones = {~0u, ~0u, ~0u, ~0u};
#pragma unroll
    for (int i = 0; i < 5; i++) __builtin_amdgcn_raw_buffer_store_b128(ones, prs, prog(PG_MP) + (i * 256 + threadIdx.x) * 16, 0, 16);
#pragma unroll
    for (int i = 0; i < 3; i++) __builtin_amdgcn_raw_buffer_store_b128(ones, prs, i * 256 + threadIdx.x < PG_MP ? prog(i * 256 + threadIdx.x) : OOBV, 0, 16); }
  const v2u tag = next_epoch(a);
  if (threadIdx.x < 8) *(LAS unsigned*)(L + threadIdx.x * 4) = ~0u;
  drain();
  lds_barrier();
  if (w == 0) __builtin_amdgcn_raw_buffer_store_b64(tag, prs, lane < 8 ? prog(PG_INIT + lane) : OOBV, 0, 16);
  if (w != 0) probe_near(a, w, L);
  if (w == 1) {
    wait_prog(prs, prog(PG_LN + lane), 0, budget); wait_prog(prs, prog(PG_LN + 64 + lane), 0, budget);
    wait_prog(prs, prog(PG_LN + 63 + min(lane, 39)) + 8, 0, budget);
    __builtin_amdgcn_raw_buffer_store_b32(1u, prs, lane < 8 ? prog(PG_M2 + lane) : OOBV, 0, 16);
  }
  if (w == 2) {
    for (int seen = -1; seen < NSEG - RING_SEGS && budget; budget--) {
      asm volatile("" ::: "memory");
      const int v = rfl(wmin((int)__builtin_amdgcn_raw_buffer_load_b32(prs, prog(PG_R + min(lane, 39)), 0, 16)));
      if (v > seen) { seen = v; __builtin_amdgcn_raw_buffer_store_b32((unsigned)v, prs, lane < 16 ? prog(PG_BACK + lane) : OOBV, 0, 16); }
      __builtin_amdgcn_s_sleep(8);
    }
  }
  if (w != 0) return;
  wait_prog(prs, prog(PG_LN + min(lane, 39)), 0, budget);
  __builtin_amdgcn_raw_buffer_store_b128((v4u){1u, 0u, tag.x, tag.y}, prs, lane < 8 ? prog(PG_GO + lane) : OOBV, 0, 16);
  const unsigned tln = rt32();
  wait_prog(prs, prog(PG_LN + lane) + 4, 0, budget); wait_prog(prs, prog(PG_LN + 48 + lane) + 4, 0, budget);
  const v2u b1 = *(volatile LAS v2u*)(L + 8), b2 = *(volatile LAS v2u*)(L + 16), b3 = *(volatile LAS v2u*)(L + 24);
  const unsigned xcc = (unsigned)__builtin_amdgcn_s_getreg(20 | (3 << 11)) & 0xfu;
  const unsigned l0 = min(b1.x, min(b2.x, b3.x)), l1 = min(b1.y, min(b2.y, b3.y));
  const int near = rfl((l1 < l0) ^ (xcc >= 4));
  __builtin_amdgcn_raw_buffer_store_b128((v4u){(unsigned)near, 0u, tag.x, tag.y ^ (unsigned)near}, prs, lane < 8 ? prog(PG_NEAR + lane) : OOBV, 0, 16);
  const rsrc_t crs = __builtin_amdgcn_make_buffer_rsrc(a.ws + near * 8192, 0, (2 << 20) - 8192, 0x00020000);
  unsigned r0 = tln, c0 = (unsigned)__builtin_amdgcn_s_memtime(), cpt = 24 * 256;
  wait_prog(prs, prog(PG_FLAG + lane), 0, budget);
  const unsigned tsv = rt32();
  wait_prog(prs, prog(PG_QK + lane), 3, budget);
  int rdy = -1;
  unsigned TA = rt32() + a.pm;
  const unsigned t0 = TA;
  const int g0 = amap(CM_RING + RG_MEM + min(lane, 39) * 256), g1 = g0 + 128;
  const int to = lane < 3 ? amap(CM_RING + RG_TB) + lane * 256 : (int)OOBV, tr = lane < 3 ? to : lane < 5 ? amap(CM_RING) + (lane - 3) * 256 : (int)OOBV;
  const int ho = lane == 0 ? amap(CM_RING) : (int)OOBV;
  unsigned db = 0;
#pragma unroll 1
  for (int s = 0; s < NSEG - 1; s++) {
    const int ms = ((s + 1) & 3) * (2 * CM_RING_SLOT), mr = ms ^ (4 * CM_RING_SLOT);
    TA &= 0x7fffffffu;
    const unsigned ex = s < 5 ? a.pad : 0, pa = a.pa + ex, pc = a.pc + ex;
    __builtin_amdgcn_raw_buffer_store_b128((v4u){TA, cpt | (pc << 16), (unsigned)rdy, (pc + pa) | (db << 10)}, crs, to + ms, 0, 16);
    __builtin_amdgcn_raw_buffer_store_b128((v4u){~0u, ~0u, ~0u, ~0u}, crs, tr + mr, 0, 16);
    __builtin_amdgcn_raw_buffer_store_b32((unsigned)(s - 2), prs, lane < 4 ? prog(PG_G + lane) : (int)OOBS, 0, 16);
    if ((s & 3) == 0) { const unsigned r1 = rt32(), c1 = (unsigned)__builtin_amdgcn_s_memtime();
      cpt = min(max((unsigned)((float)(c1 - c0) * 256.f / (float)(r1 - r0 + 1u)), 2048u), 8192u); r0 = r1; c0 = c1; }
    unsigned TB = TA + pc;
    wait_until(TA);
    const bool hb = rfl(__builtin_amdgcn_raw_buffer_load_b32(crs, ho + 256, ms, 16)) != ~0u;
    rdy = wmin((int)__builtin_amdgcn_raw_buffer_load_b32(prs, prog(PG_QK + lane), 0, 16));
    if (hb) TB += db;
    const bool ha = false;
    wait_until(TB);
    bool failed = false;
    for (;;) { asm volatile("" ::: "memory");
      const unsigned m = max(__builtin_amdgcn_raw_buffer_load_b32(crs, g0, ms, 16), __builtin_amdgcn_raw_buffer_load_b32(crs, g1, ms, 16));
      if (!__any(m == ~0u) || budget == 0) break;
      budget--; failed = true; }
    TA = TB + pa; db = a.db;
    if (failed) { const int late = (int)((rt32() + a.px - TA) << 1) >> 1; TA += max(late, 0); db = 0; }
    if (failed | ha | hb) { fails += failed + ha * 256 + hb * 65536; if (s < 32) fmask0 |= 1u << s; else fmask1 |= 1u << (s - 32); }
  }
  __builtin_amdgcn_raw_buffer_store_b32((unsigned)(NSEG - 3), prs, lane < 4 ? prog(PG_G + lane) : (int)OOBS, 0, 16);
  __builtin_amdgcn_raw_buffer_store_b64(tag, prs, lane == 0 ? prog(PG_EPOCH) : OOBV, 0, 16);
  if (a.dbg && lane == 0) { unsigned* d = (unsigned*)(a.ws + WS_DBG + 32768); d[0] = fails; d[1] = (TA - t0) & 0x7fffffffu; d[2] = t0 - tstart; d[3] = tstart; d[4] = near; d[5] = fmask0; d[6] = fmask1; d[7] = tln - tstart; d[8] = tsv - tstart; d[9] = l0; d[10] = l1; }
}

#define SETDESC(V, I, P, N) { unsigned long long p_ = (unsigned long long)(P); V[I] = (unsigned)p_; V[I + 1] = (unsigned)(p_ >> 32) & 0xffffu; V[I + 2] = (N); V[I + 3] = 0x00020000u; }
#define RUN(TEXT) asm volatile("s_nop 4\n" TEXT : : "{v[0:31]}"(c), "{s[32:47]}"(s0), "{s[48:63]}"(s1), "{s[64:79]}"(s2) : REC_ASM_CLOBBERS)
enum { R_S, R_V, R_C, R_X, R_R, R_QK, R_VU, R_F };

__global__ void __launch_bounds__(256, 1) k_main(Args a) {
  extern __shared__ __attribute__((aligned(16))) unsigned char smem[];
  LAS unsigned char* L = (LAS unsigned char*)smem;
  const int bx = blockIdx.x, half = (bx >> 2) & 1, idx = (bx >> 3) * 4 + (bx & 3);
  a.ws += CTL_BYTES + ((0u - (unsigned)(uintptr_t)(a.ws + CTL_BYTES)) & (MB2 - 1));
  const unsigned tstart = rt32();
  const int wi = half ? idx : idx + 23;
  if ((a.dbg & 16) && idx >= 64) { if (!half && idx == 104 && threadIdx.x == 0) ((unsigned*)(a.ws + WS_DBG + 32768))[3] = tstart; return; }
  if ((a.dbg & 32) && half) return;
  if (!half && idx == 104) { role_time(a, tstart, L); return; }
  if (idx >= 64) {
    const int hi = half * 64 + idx - 64, tile = hi - (hi > 40);
    startup(a, tile, L);
    if (hi == 63) { lds_barrier(); startup(a, 127, L); }
  }
  int role, id;
  if (!half && idx < 105) {
    role = idx < 32 ? R_S : idx < 64 ? R_V : R_C; id = role == R_C ? idx - 64 : idx & 31;
  } else {
    if (wi >= 142) return;
    if ((a.dbg & 2) && wi >= 64) return;
    role = wi < 32 ? R_QK : wi < 64 ? R_VU : wi < 104 ? R_X : wi < 114 ? R_R : R_F; id = role == R_F ? wi - 114 : role == R_R ? wi - 104 : role == R_X ? wi - 64 : wi & 31;
  }
  const int tid = threadIdx.x, lane = tid & 63, w = rfl(tid >> 6), g = lane >> 4, n = lane & 15, q4 = n >> 2, p4 = lane & 3;
  const int h = (id & 3) * 8 + ((id >> 2) & 7);
  const unsigned hb = WS_SH + h * MB2;
  v32u c;
#pragma unroll
  for (int i = 0; i < 32; i++) c[i] = 0;
  v16u s0, s1, s2;
#pragma unroll
  for (int i = 0; i < 16; i++) { s0[i] = 0; s1[i] = 0; s2[i] = 0; }
  c[0] = lane * 16; c[5] = (n & 2) ? 0xEEEE : 0x4444; c[10] = 1;
  const int f = q4 | ((g & 1) << 2), rbase = L_STG + (8 * g + q4) * 256, ch = n ^ (g << 1);
  const int trn0 = rbase + p4 * 8 + (w ^ f) * 32, trn1 = rbase + p4 * 8 + ((4 + w) ^ f) * 32;
  const int trp0 = rbase + ((2 * w + (p4 >> 1)) ^ f) * 32 + (p4 & 1) * 16, trp1 = trp0 + 8;
  c[20] = (16 * w + g) * 1280 + ch * 16; c[21] = c[20] ^ 128;
  int near = 0;
  if (role == R_C) near = wait_start(a, PG_NEAR, L) & 1;
  if (role == R_X || role == R_R) wait_start(a, PG_M2, L);
  const rsrc_t wrs = __builtin_amdgcn_make_buffer_rsrc(a.ws, 0, WS_END, 0x00020000);
  SETDESC(s0, 4, a.ctl, CTL_BYTES);
  SETDESC(s0, 8, a.ws + near * 8192, (2 << 20) - 8192);
  SETDESC(s0, 12, a.ws, WS_END);
  s2[0] = w; s2[1] = prog(PG_GO + (bx & 7)); s2[9] = !a.dbg ? ~0u : role <= R_C ? WS_DBG + (idx * 4 + w) * 64 : WS_DBG + 36864 + wi * 4;
  if (role <= R_C) {
    s2[12] = prog(PG_FLAG + idx); s2[2] = amap(CM_RING); s2[3] = role == R_C ? id < 4 : id < 4 && w == 0;
    s2[7] = amap(CM_RING + RG_MEM + w * 2560); s2[8] = L_MEM + w * 3072;
  }
  if (role <= R_V) {
#pragma unroll
    for (int k = 0; k < 3; k++) { const int T = k < 2 ? (4 * k + w) * 4 + g : 32 + 2 * w + (g & 1);
      const unsigned long long p = (unsigned long long)(a.mem0 + (2 * (n >> 2) + (n & 1)) * D + T * 16 + ((n >> 1) & 1) * 8);
      c[24 + 2 * k] = (unsigned)p; c[25 + 2 * k] = (unsigned)(p >> 32); }
    c[12] = lane < 32 ? lane * 16 : OOBV; c[13] = lane * 16 + (lane >> 5) * 2560;
    const int pl = role == R_S ? 128 : 64;
    c[3] = L_QI + (p4 * pl + ((8 * g + q4) ^ swz(p4))) * 8;
    c[4] = L_QI + (p4 * pl + ((8 * g + 4 + q4) ^ swz(p4))) * 8;
    c[16] = (16 * w + 8 * (g >> 1) + (g & 1)) * 8192 + ch * 16; c[17] = c[16] ^ 128;
    c[18] = trn0; c[19] = trn1;
  }
  if (role == R_S || role == R_QK) { SETDESC(s1, 8, a.Wq + h * 128, D * HD * 2 - h * 256); SETDESC(s1, 12, a.Wk + h * 128, D * HD * 2 - h * 256); }
  if (role == R_QK || role == R_VU) { c[16] = (16 * w + g) * 8192 + ch * 16; c[17] = c[16] ^ 128; s1[0] = WS_HF + w * 10240;
    c[9] = w == 0 ? prog(PG_BACK + (id & 15)) : OOBV; s1[5] = 1 - RING_SEGS; }
  if (role == R_V || role == R_VU) { SETDESC(s1, 8, a.Wv + h * 128, D * HD * 2 - h * 256); SETDESC(s1, 12, a.Wo + (size_t)h * 128 * D, 128 * D * 2); }
  if (role >= R_QK) { c[1] = L_STG + lane * 16; c[2] = tid * 16; c[18] = trp0; c[19] = trp1; }
  if (role == R_S) {
    const int tok = 2 * q4 + (n & 1), dl = (n >> 1) & 1, qt = w == 2;
    c[1] = 16 * qt + n - 4 * g;
    c[2] = L_QI + (g * 128 + ((16 * w + n) ^ swz(g))) * 8;
    c[6] = g * 128 + tok * 16 + dl * 8; c[7] = dl ? OOBV : tok * 16 + g * 4; c[8] = n * 16 + g * 4; c[9] = prog(PG_QK + h);
    s0[2] = amap(CM_RING + RG_PX + h * 512); s0[3] = amap(CM_RING + RG_PM + h * 128);
    s1[0] = hb + SH_K; s1[1] = hb + SH_Q + qt * 4096; s1[2] = hb + SH_P + (1 - qt) * 1024; s1[3] = hb + SH_P + 1536 + qt * 256;
    s1[4] = prog(PG_SX + h); s1[5] = 3; s2[6] = amap(CM_RING + RG_TB);
  } else if (role == R_V) {
    c[2] = L_VI + (g * 64 + ((8 * w + q4 * 2 + (n & 1)) ^ swz(g))) * 8 + ((n >> 1) & 1) * 4;
    s0[0] = amap(um_off(h, w)); s1[0] = WS_UM + (w * NSEG + RING_SEGS) * 8192 + h * 256;
    c[7] = ((g >> 1) * 8 + (g & 1) * 4) * (NSEG * 8192) + n * 16; c[8] = lane < 32 ? c[7] : OOBV;
    s1[4] = prog(PG_VM + h);
    s2[6] = amap(CM_RING + RG_TB + 256);
  } else if (role == R_C) {
    const int T = id, vw = T & 3, tl = (T >> 3) * 2 + ((T >> 2) & 1), r = lane >> 2, jj = lane & 3;
    c[1] = tid * 16;
    c[2] = amap(um_off(8 * w + g, vw)) + tl * 256 + n * 16; c[3] = amap(um_off(8 * w + 4 + g, vw)) + tl * 256 + n * 16;
    c[4] = amap(CM_RING + RG_PX + w * 4096) + (n >> 3) * 512 + g * 128 + (n & 7) * 16;
    c[5] = amap(CM_RING + RG_PM + w * 1024) + (4 * (n >> 3) + g) * 128 + (n & 7) * 16;
    c[6] = amap(CM_RING + RG_MEM + mem_blk(T, 2560)) + lane * 4;
    c[7] = ((2 * ((r >> 1) & 1) + (jj >> 1)) * 16 + 2 * (r >> 2) + (r & 1)) * 16 + (jj & 1) * 8;
    c[9] = prog(PG_VU + 8 * w + (lane & 7));
    s0[1] = WS_UX + T * (NSEG * 32768) + w * 8192; s1[0] = WS_UM + (T * NSEG + RING_SEGS - 1) * 8192 + w * 2048; s1[4] = prog(PG_C + T); s1[5] = 3;
    s2[6] = amap(CM_RING + RG_TB + 512);
  } else if (role == R_X) {
    const int rep = id / 10, j = id % 10, T = j * 4 + w, d0 = T * 16 + g * 4, sl = NSEG - 4 + rep;
    const v2u gav = __builtin_amdgcn_raw_buffer_load_b64(wrs, d0 * 2, WS_MOD + 4 * D, 16), cbv = __builtin_amdgcn_raw_buffer_load_b64(wrs, d0 * 2, WS_MOD + 8 * D, 16);
    c[16] = gav.x << 16; c[17] = gav.x & 0xffff0000u; c[18] = gav.y << 16; c[19] = gav.y & 0xffff0000u;
    c[20] = cbv.x << 16; c[21] = cbv.x & 0xffff0000u; c[22] = cbv.y << 16; c[23] = cbv.y & 0xffff0000u;
    c[1] = lane * 8; c[2] = g * 2048 + 1536 + n * 16; c[3] = (n * D + d0) * 2;
    c[4] = (T >> 1) * 1024 + ((2 * (T & 1) + (g >> 1)) * 16 + n) * 16 + (g & 1) * 8;
    c[5] = lane < 16 ? 20480 + (T >> 1) * 256 + n * 16 + (T & 1) * 8 : OOBV;
    if (w == 1) { c[9] = c[8] = prog(PG_VU + (lane & 31)); c[6] = 4 * rep + 3; c[7] = 16; c[11] = 0; }
    else { c[9] = prog(PG_G + rep); c[6] = rep; c[7] = 4;
      c[8] = rep < 2 ? c[9] : prog(lane < 32 || rep == 3 ? PG_SX + lane : PG_C + j * 4 + (lane & 3)); c[11] = rep < 2 ? sl : lane >= 32 ? 3 : rep == 3 ? 2 : 0; }
    SETDESC(s1, 8, a.x, S * D * 2);
    s0[1] = WS_UX + (T * NSEG + rep) * 32768;
    s1[0] = WS_SH + SH_P + 2 * w * MB2 + rep * 2048; s1[1] = WS_UM + (T * NSEG + rep) * 8192; s1[2] = WS_YF + rep * 57344; s1[3] = WS_X1 + rep * 40960;
    s1[4] = prog(PG_X + rep * 16 + j); s1[5] = rep; s1[6] = rep == 3 ? 8192 : 0; s2[15] = rep * 40960;
  } else if (role == R_R) {
    const int t = w >> 1, tok = (w & 1) * 8 + (lane & 7), pc = lane >> 3, b = 2 * id + (pc >> 2), lo = ((pc & 3) * 16 + tok) * 16, d0 = id * 64 + pc * 8;
    const v4u gb = ld16(wrs, d0 * 2, WS_MOD + 10 * D);
#pragma unroll
    for (int i = 0; i < 4; i++) { c[16 + 2 * i] = gb[i] << 16; c[17 + 2 * i] = gb[i] & 0xffff0000u; }
    c[24] = 0x00003f80u; c[25] = 0x3f800000u;
    c[1] = (b * 2 + t) * 14336 + lo; c[2] = t * 20480 + b * 1024 + lo; c[3] = ((t * 16 + tok) * D + d0) * 2; c[9] = prog(PG_F + (t * 14 + min(lane >> 1, 13)) * 4 + ((2 * id + (lane & 1)) & 3));
    SETDESC(s1, 8, a.out, S * D * 2);
    s1[0] = WS_FP; s1[1] = WS_X1; s1[2] = 0; s1[4] = prog(PG_R + id * 4 + w); s1[5] = 0;
  } else if (role == R_QK) {
    s1[1] = hb + SH_K + w * 1024; s1[2] = hb + SH_Q + w * 1024; s1[4] = prog(PG_QK + h);
  } else if (role == R_VU) {
    c[3] = lane * 16; c[22] = trn0; c[23] = trn1;
    s1[1] = WS_UX + w * (NSEG * 32768) + h * 1024; s1[4] = prog(PG_VU + h);
  } else {
    const int rep = id / 14, fi = id % 14;
    c[3] = L_CB + g * 16; c[22] = trp0; c[23] = trp1;
    c[16] = (fi == 13 && ch >= 8) ? OOBV : (16 * w + g) * 3456 + ch * 16;
    c[17] = (fi == 13 && (ch ^ 8) >= 8) ? OOBV : (16 * w + g) * 3456 + (ch ^ 8) * 16;
    c[9] = w == 0 ? prog(PG_X + min(lane, 9)) : OOBV;
    SETDESC(s1, 8, a.w1 + fi * 128, D * FF * 2 - fi * 256);
    SETDESC(s1, 12, a.w2 + (size_t)fi * 128 * D, min(128, FF - fi * 128) * D * 2);
    s1[0] = WS_YF + rep * 28672 + w * 7168; s1[1] = WS_FP + (w * 2 + rep) * 14336 + fi * 1024; s1[2] = WS_MOD + 6 * D + w * 1024; s1[4] = prog(PG_F + id * 4 + w); s1[5] = 3;
  }
#pragma unroll
  for (int i = 0; i < 16; i++) { s0[i] = rfl(s0[i]); s1[i] = rfl(s1[i]); s2[i] = rfl(s2[i]); }
  if (role == R_S) RUN(REC_S_ASM);
  else if (role == R_V) RUN(REC_V_ASM);
  else if (role == R_C) RUN(REC_C_ASM);
  else if (role == R_X) RUN(REC_X_ASM);
  else if (role == R_R) RUN(REC_R_ASM);
  else if (role == R_QK) RUN(REC_QK_ASM);
  else if (role == R_VU) RUN(REC_VU_ASM);
  else RUN(REC_F_ASM);
  __builtin_unreachable();
}

extern "C" void kernel_launch(void* const* d_in, const int* in_sizes, int n_in, void* d_out, int out_size, void* d_ws, size_t ws_size, hipStream_t stream) {
  char* ws = (char*)d_ws;
  Args a;
  a.x = (const u16*)d_in[0]; a.mem0 = (const u16*)d_in[1]; a.Wq = (const u16*)d_in[2]; a.Wk = (const u16*)d_in[3]; a.Wv = (const u16*)d_in[4]; a.Wo = (const u16*)d_in[5];
  a.w1 = (const u16*)d_in[8]; a.w2 = (const u16*)d_in[9]; a.out = (u16*)d_out; a.c = (const u16*)d_in[10]; a.wmod = (const u16*)d_in[11]; a.ws = ws; a.ctl = ws;
  a.pa = 110; a.pc = 76; a.px = 170; a.pm = 130; a.pad = 8; a.dbg = 0; a.pf = 4; a.da = 48; a.db = 120; a.p9 = 0;
  (void)hipFuncSetAttribute((const void*)k_main, hipFuncAttributeMaxDynamicSharedMemorySize, MAIN_LDS);
  k_main<<<256, 256, MAIN_LDS, stream>>>(a);
}
```
